```python
import torch

def _softmax(x, axis=-1):
    return torch.softmax(x, dim=axis)
def _gelu(x):
    return torch.nn.functional.gelu(x, approximate="tanh")
def _silu(x):
    return torch.nn.functional.silu(x)
def _sigmoid(x):
    return torch.sigmoid(x)
def _layernorm(x, axis=-1, eps=1e-5):
    m = x.mean(dim=axis, keepdim=True)
    v = x.var(dim=axis, keepdim=True, unbiased=False)
    return (x - m) / torch.sqrt(v + eps)
def _scatter_add(tgt, idx, src, axis=0):
    return tgt.clone().index_add_(axis, idx, src)
def _segment_sum(src, seg, n):
    out = torch.zeros((n,) + src.shape[1:], dtype=src.dtype)
    return out.index_add_(0, seg, src)
def _pad(x, pw):
    flat = tuple(v for pair in reversed(pw) for v in pair)
    return torch.nn.functional.pad(x, flat)

kernel_name = 'concat_heads__ssm_conv_s5__grouped_kv_mqa__prompt'

def setup_inputs():

    _w = (lambda a: (lambda t: t.to(torch.bfloat16) if t.is_floating_point() else t)(torch.as_tensor(a)))
    def _A_setup():
        B, S, D = 1, 2048, 448
        P = D
        Lambda_re = -0.5 * (lambda _x: torch.exp(torch.as_tensor(_x)))((lambda *s: torch.randn(*s, dtype=torch.bfloat16) * 0.4)(P))
        Lambda_im = (lambda *s: torch.randn(*s, dtype=torch.bfloat16) * 0.8)(P) * 1.0
        B_re = (lambda *s: torch.randn(*s, dtype=torch.bfloat16) * 0.4)(P, D) * (D ** -0.5)
        B_im = (lambda *s: torch.randn(*s, dtype=torch.bfloat16) * 0.4)(P, D) * (D ** -0.5)
        C_re = (lambda *s: torch.randn(*s, dtype=torch.bfloat16) * 0.4)(D, P) * (P ** -0.5)
        C_im = (lambda *s: torch.randn(*s, dtype=torch.bfloat16) * 0.4)(D, P) * (P ** -0.5)
        log_dt = (lambda *s: torch.randn(*s, dtype=torch.bfloat16) * 0.4)(P) * 0.3 - 2.0
        Dskip = (lambda *s: torch.randn(*s, dtype=torch.bfloat16) * 0.1)(D)
        return {
            "x": _w((lambda *s: torch.randn(*s, dtype=torch.bfloat16) * 0.05)(B, S, D)),
            "Lambda_re": _w(Lambda_re),
            "Lambda_im": _w(Lambda_im),
            "B_re": _w(B_re),
            "B_im": _w(B_im),
            "C_re": _w(C_re),
            "C_im": _w(C_im),
            "log_dt": _w(log_dt),
            "Dskip": _w(Dskip),
        }
    _A_in = _A_setup()

    def _B_setup():
        B, S, D, H = 1, 2048, 448, 16
        Dh = D // H
        return {
            "x": _w((lambda *s: torch.randn(*s, dtype=torch.bfloat16) * 0.05)(B, S, D)),
            "wq": _w((lambda *s: torch.randn(*s, dtype=torch.bfloat16) * 1.6)(D, H * Dh)),
            "wk": _w((lambda *s: torch.randn(*s, dtype=torch.bfloat16) * 1.6)(D, Dh)),
            "wv": _w((lambda *s: torch.randn(*s, dtype=torch.bfloat16) * 0.05)(D, Dh)),
            "wo": _w((lambda *s: torch.randn(*s, dtype=torch.bfloat16) * 0.05)(H * Dh, D)),
        }
    _B_in = _B_setup()

    return {
        "A_x": _A_in["x"],
        "A_Lambda_re": _A_in["Lambda_re"],
        "A_Lambda_im": _A_in["Lambda_im"],
        "A_B_re": _A_in["B_re"],
        "A_B_im": _A_in["B_im"],
        "A_C_re": _A_in["C_re"],
        "A_C_im": _A_in["C_im"],
        "A_log_dt": _A_in["log_dt"],
        "A_Dskip": _A_in["Dskip"],
        "B_x": _B_in["x"],
        "B_wq": _B_in["wq"],
        "B_wk": _B_in["wk"],
        "B_wv": _B_in["wv"],
        "B_wo": _B_in["wo"],
    }

def reference(A_x, A_Lambda_re, A_Lambda_im, A_B_re, A_B_im, A_C_re, A_C_im, A_log_dt, A_Dskip, B_x, B_wq, B_wk, B_wv, B_wo):
    def _A_ref(x, Lambda_re, Lambda_im, B_re, B_im, C_re, C_im, log_dt, Dskip):
        Bsz, L, H = x.shape
        Lam = Lambda_re + 1j * Lambda_im
        Bmat = B_re + 1j * B_im
        Cmat = C_re + 1j * C_im
        dt = torch.exp(log_dt)
        Lam_bar = torch.exp(dt * Lam)
        B_bar = ((Lam_bar - 1.0) / Lam)[:, None] * Bmat
        Bu = torch.einsum("bld,pd->blp", x + 0j, B_bar)
        h = torch.zeros_like(Bu[:, 0, :])
        hs = []
        for t in range(L):
            h = Lam_bar[None, :] * h + Bu[:, t, :]
            hs.append(h)
        hseq = torch.stack(hs, 1)
        y = 2.0 * torch.einsum("blp,dp->bld", hseq, Cmat).real + x * Dskip[None, None, :]
        return y

    def _B_ref(x, wq, wk, wv, wo):
        B, S, D = x.shape
        H = 16
        Dh = D // H
        q = (lambda x, *d: x.permute(*d))((x @ wq).reshape(B, S, H, Dh), 0, 2, 1, 3)
        k = x @ wk
        v = x @ wv
        scores = torch.einsum("bhqd,bkd->bhqk", q, k) * (Dh ** -0.5)
        p = _softmax(scores, axis=-1)
        o = torch.einsum("bhqk,bkd->bhqd", p, v)
        return (lambda x, *d: x.permute(*d))(o, 0, 2, 1, 3).reshape(B, S, H * Dh) @ wo

    a = _A_ref(A_x, A_Lambda_re, A_Lambda_im, A_B_re, A_B_im, A_C_re, A_C_im, A_log_dt, A_Dskip)
    b = _B_ref(B_x, B_wq, B_wk, B_wv, B_wo)
    return torch.concatenate([a, b], axis=-1)
```

```cpp
#include <hip/hip_runtime.h>
#include <hip/hip_bf16.h>
typedef unsigned short u16;
__device__ __forceinline__ float bf2f(u16 v){ return __uint_as_float(((unsigned)v)<<16); }
__device__ __forceinline__ u16 f2bf(float f){ unsigned u=__float_as_uint(f); u += 0x7fffu + ((u>>16)&1u); return (u16)(u>>16); }
constexpr int S=2048, D=448, H=16, DH=28;

__global__ void k_bu(const u16* x, const u16* Bre, const u16* Bim, float* bu){
  int t=blockIdx.x;
  for(int c=threadIdx.x;c<896;c+=blockDim.x){
    const u16* B = c<448? Bre + c*448 : Bim + (c-448)*448;
    float a=0; for(int d=0;d<448;d++) a += bf2f(x[t*448+d])*bf2f(B[d]);
    bu[t*896+c]=a;
  }
}
__global__ void k_scan(const u16* lre,const u16* lim,const u16* ldt,float* bu){
  int p=blockIdx.x*blockDim.x+threadIdx.x; if(p>=448) return;
  float dt = bf2f(f2bf(expf(bf2f(ldt[p]))));
  float a=bf2f(lre[p]), b=bf2f(lim[p]);
  float er=expf(dt*a); float lr=er*cosf(dt*b), li=er*sinf(dt*b);
  float nr=lr-1.f, ni=li; float den=a*a+b*b; float cr=(nr*a+ni*b)/den, ci=(ni*a-nr*b)/den;
  float hr=0,hi=0;
  for(int t=0;t<S;t++){
    float ur=bu[t*896+p], ui=bu[t*896+448+p];
    float vr=cr*ur-ci*ui, vi=cr*ui+ci*ur;
    float nhr=lr*hr-li*hi+vr, nhi=lr*hi+li*hr+vi; hr=nhr;hi=nhi;
    bu[t*896+p]=hr; bu[t*896+448+p]=hi;
  }
}
__global__ void k_cproj(const float* h,const u16* Cre,const u16* Cim,const u16* x,const u16* Dsk,float* out){
  int t=blockIdx.x;
  for(int d=threadIdx.x;d<448;d+=blockDim.x){
    float a=0; for(int p=0;p<448;p++) a += h[t*896+p]*bf2f(Cre[d*448+p]) - h[t*896+448+p]*bf2f(Cim[d*448+p]);
    out[t*896+d]=2.f*a + bf2f(f2bf(bf2f(x[t*448+d])*bf2f(Dsk[d])));
  }
}
__global__ void k_qkv(const u16* x,const u16* wq,const u16* wk,const u16* wv,u16* q,u16* k,u16* v){
  int t=blockIdx.x;
  for(int c=threadIdx.x;c<448+56;c+=blockDim.x){
    float a=0;
    if(c<448){ for(int d=0;d<448;d++) a+=bf2f(x[t*448+d])*bf2f(wq[d*448+c]); q[t*448+c]=f2bf(a);}
    else if(c<448+28){ int j=c-448; for(int d=0;d<448;d++) a+=bf2f(x[t*448+d])*bf2f(wk[d*28+j]); k[t*28+j]=f2bf(a);}
    else { int j=c-476; for(int d=0;d<448;d++) a+=bf2f(x[t*448+d])*bf2f(wv[d*28+j]); v[t*28+j]=f2bf(a);}
  }
}
__global__ void k_attn(const u16* q,const u16* k,const u16* v,u16* o){
  int idx=blockIdx.x*blockDim.x+threadIdx.x; int t=idx/16,h=idx%16;
  float qq[28]; for(int d=0;d<28;d++) qq[d]=bf2f(q[t*448+h*28+d]);
  float m=-1e30f,l=0,acc[28]; for(int d=0;d<28;d++) acc[d]=0;
  const float sc=0.18898223650461358f;
  for(int j=0;j<S;j++){
    float s=0; for(int d=0;d<28;d++) s+=qq[d]*bf2f(k[j*28+d]); s*=sc;
    float mn=fmaxf(m,s); float al=expf(m-mn); float p=expf(s-mn); l=l*al+p;
    for(int d=0;d<28;d++) acc[d]=acc[d]*al+p*bf2f(v[j*28+d]); m=mn;
  }
  for(int d=0;d<28;d++) o[t*448+h*28+d]=f2bf(acc[d]/l);
}
__global__ void k_oproj(const u16* o,const u16* wo,float* out){
  int t=blockIdx.x;
  for(int c=threadIdx.x;c<448;c+=blockDim.x){
    float a=0; for(int d=0;d<448;d++) a+=bf2f(o[t*448+d])*bf2f(wo[d*448+c]);
    out[t*896+448+c]=bf2f(f2bf(a));
  }
}
extern "C" void kernel_launch(void* const* d_in, const int* in_sizes, int n_in, void* d_out, int out_size, void* d_ws, size_t ws_size, hipStream_t stream) {
  const u16* Ax=(const u16*)d_in[0]; const u16* lre=(const u16*)d_in[1]; const u16* lim=(const u16*)d_in[2];
  const u16* Bre=(const u16*)d_in[3]; const u16* Bim=(const u16*)d_in[4]; const u16* Cre=(const u16*)d_in[5]; const u16* Cim=(const u16*)d_in[6];
  const u16* ldt=(const u16*)d_in[7]; const u16* Dsk=(const u16*)d_in[8];
  const u16* Bx=(const u16*)d_in[9]; const u16* wq=(const u16*)d_in[10]; const u16* wk=(const u16*)d_in[11]; const u16* wv=(const u16*)d_in[12]; const u16* wo=(const u16*)d_in[13];
  char* ws=(char*)d_ws; float* bu=(float*)ws; u16* q=(u16*)(ws+8*1024*1024); u16* k=q+S*448; u16* v=k+S*28; u16* o=v+S*28;
  float* out=(float*)d_out;
  k_bu<<<S,256,0,stream>>>(Ax,Bre,Bim,bu);
  k_scan<<<7,64,0,stream>>>(lre,lim,ldt,bu);
  k_cproj<<<S,256,0,stream>>>(bu,Cre,Cim,Ax,Dsk,out);
  k_qkv<<<S,256,0,stream>>>(Bx,wq,wk,wv,q,k,v);
  k_attn<<<S*16/64,64,0,stream>>>(q,k,v,o);
  k_oproj<<<S,256,0,stream>>>(o,wo,out);
}
```

```cpp
#include <hip/hip_runtime.h>

typedef unsigned short u16;
typedef unsigned int u32;
typedef unsigned long long u64;
using bf16x8 = __attribute__((ext_vector_type(8))) short;
using f32x16 = __attribute__((ext_vector_type(16))) float;
using u32x4 = __attribute__((ext_vector_type(4))) unsigned;
using u32x2 = __attribute__((ext_vector_type(2))) unsigned;
typedef short v4i16_t __attribute__((ext_vector_type(4)));
typedef __bf16 bf16x2_t __attribute__((ext_vector_type(2)));
typedef float f32x2 __attribute__((ext_vector_type(2)));
#define LAS __attribute__((address_space(3)))
#define GAS __attribute__((address_space(1)))
#define DI __device__ __forceinline__
#define MFMA(a, b, c) __builtin_amdgcn_mfma_f32_32x32x16_bf16((a), (b), (c), 0, 0, 0)
#define SBAR() __builtin_amdgcn_sched_barrier(0)
#define PIN(x) asm volatile("" : "+v"(x))
#define PIN2(a, b) asm volatile("" : "+v"(a), "+v"(b))
#define PIN4(a, b, c, d) asm volatile("" : "+v"(a), "+v"(b), "+v"(c), "+v"(d))
#define MPIN(x) asm volatile("s_nop 7\n\ts_nop 7" : "+v"(x))
#define MPIN2(a, b) asm volatile("s_nop 7\n\ts_nop 7" : "+v"(a), "+v"(b))
#define MPIN4(a, b, c, d) asm volatile("s_nop 7\n\ts_nop 7" : "+v"(a), "+v"(b), "+v"(c), "+v"(d))
#define VMCNT(n) asm volatile("s_waitcnt vmcnt(" #n ")" ::: "memory")
#define LGKM0() asm volatile("s_waitcnt lgkmcnt(0)" ::: "memory")
#define EX(x) __builtin_amdgcn_exp2f(x)

#define NOLOAD 0
#define THR 12
#define KV_AUX 16
#define H_AUX 16
#define FENCE 0
#define ABL 0
#define G1_PRIO 0
#define AUX3_PRIO 2
#define QEARLY 1
#define KV_DEPTH 4
#define AUX_PRIO 2
#define AUX2_PRIO 0
#define GATE 0
#define KV_REPL 32

constexpr u32 WS_EPOCH = 0;
constexpr u32 WS_HFLAG = 4096;
constexpr u32 WS_OFLAG = 8192;
constexpr u32 WS_KVFLAG = 12288;
constexpr u32 WS_AGG = 32768;
constexpr u32 WS_KVF = WS_AGG + 131072;
constexpr u32 WS_OF = WS_KVF + 262144;
constexpr u32 WS_HF = WS_OF + 2097152;
constexpr u32 WS_END = WS_HF + 32 * 2 * 56 * 1024;
constexpr u32 WS_DBG = 8u << 20;

constexpr u32 L_XB = 0;
constexpr u32 L_WQ = 57344;
constexpr u32 L_C = 107520;
constexpr u32 L_MISC = L_C + 55296;
constexpr u32 L_AGG = L_MISC + 128;
constexpr u32 L_KN = L_MISC + 896;
constexpr u32 L_PRIV = 26880;
constexpr u32 L_OS = 12288;
constexpr u32 L_QX = L_C + 54 * 864;
constexpr u32 LDS_BYTES = 163840;
enum { C_WQ = 0, C_Q2, C_QDONE, C_BFULL, C_AGG, C_WO, C_O, C_MAX, C_P, C_Y, C_FB, C_KVP, C_KMAX, C_KV, C_OF, C_HF, C_WQ2, C_CREG, C_EPOCH = 31 };

struct Params {
  const u16 *Ax, *lre, *lim, *Bre, *Bim, *Cre, *Cim, *ldt, *Dsk, *Bx, *wq, *wk, *wv, *wo;
  float* out;
  char* ws;
};

DI float bf2f(u16 v) { return __uint_as_float(((u32)v) << 16); }
DI u32 cvtpk(float a, float b) { return __builtin_bit_cast(u32, __builtin_convertvector((f32x2){a, b}, bf16x2_t)); }
DI float rbf(float a) { return __uint_as_float(cvtpk(a, 0.f) << 16); }
DI bf16x8 frag(u32x4 v) { return __builtin_bit_cast(bf16x8, v); }
DI void glds16(const void* sbase, u32 voff, u32 lds_base) {
  asm volatile("s_mov_b32 m0, %2\n\ts_nop 0\n\tglobal_load_lds_dwordx4 %0, %1" :: "v"(voff), "s"(sbase), "s"(lds_base) : "memory");
}
DI u32x4 ldsr128(u32 a) { return *(const LAS u32x4*)(size_t)a; }
DI void ldsw128(u32 a, u32x4 v) { *(LAS u32x4*)(size_t)a = v; }
DI u32 ldsu(u32 a) { return (u32)__builtin_amdgcn_readfirstlane(*(volatile LAS u32*)(size_t)a); }
DI u32x2 ldstr(u32 a) { return __builtin_bit_cast(u32x2, __builtin_amdgcn_ds_read_tr16_b64_v4i16((LAS v4i16_t*)(size_t)a)); }
DI u32x4 trfrag(u32 a0, u32 a1) { u32x2 lo = ldstr(a0), hi = ldstr(a1); return (u32x4){lo.x, lo.y, hi.x, hi.y}; }
DI u32x4 f4(const f32x16& v, int g) {
  return (u32x4){__float_as_uint(v[4 * g]), __float_as_uint(v[4 * g + 1]), __float_as_uint(v[4 * g + 2]), __float_as_uint(v[4 * g + 3])};
}
DI void add4(f32x16& v, int g, u32x4 a) {
  v[4 * g] += __uint_as_float(a.x); v[4 * g + 1] += __uint_as_float(a.y); v[4 * g + 2] += __uint_as_float(a.z); v[4 * g + 3] += __uint_as_float(a.w);
}
DI void arrive(int c, int lane) {
  asm volatile("s_waitcnt lgkmcnt(0)" ::: "memory");
  if (lane == 0) asm volatile("ds_add_u32 %0, %1" :: "v"(L_MISC + 4 * c), "v"(1u) : "memory");
}
DI void await(int c, u32 target) {
#pragma unroll 1
  for (u32 spin = 0; spin < (1u << 22); ++spin) {
    asm volatile("" ::: "memory");
    if (ldsu(L_MISC + 4 * c) >= target) break;
    __builtin_amdgcn_s_sleep(1);
  }
  asm volatile("" ::: "memory");
}
DI float swap32(float v) {
  auto r = __builtin_amdgcn_permlane32_swap(__float_as_uint(v), __float_as_uint(v), false, false);
  return __uint_as_float((threadIdx.x & 32) ? r[0] : r[1]);
}
DI float wave_max_nonneg(float v) {
  u32 x = __float_as_uint(v);
  x = max(x, (u32)__builtin_amdgcn_update_dpp(0, (int)x, 0x111, 0xf, 0xf, false));
  x = max(x, (u32)__builtin_amdgcn_update_dpp(0, (int)x, 0x112, 0xf, 0xf, false));
  x = max(x, (u32)__builtin_amdgcn_update_dpp(0, (int)x, 0x114, 0xf, 0xf, false));
  x = max(x, (u32)__builtin_amdgcn_update_dpp(0, (int)x, 0x118, 0xf, 0xf, false));
  x = max(x, (u32)__builtin_amdgcn_update_dpp(0, (int)x, 0x142, 0xa, 0xf, false));
  x = max(x, (u32)__builtin_amdgcn_update_dpp(0, (int)x, 0x143, 0xc, 0xf, false));
  return __uint_as_float((u32)__builtin_amdgcn_readlane((int)x, 63));
}
DI void gstore_flag(char* ws, u32 off, u32 v) { __hip_atomic_store((GAS u32*)(ws + off), v, __ATOMIC_RELAXED, __HIP_MEMORY_SCOPE_AGENT); }
DI void poll_words(char* ws, u32 off, int n, u32 tag, int lane) {
  const int i = lane < n ? lane : 0;
#pragma unroll 1
  for (u32 spin = 0; spin < (1u << 17); ++spin) {
    asm volatile("" ::: "memory");
    u32 v = __hip_atomic_load((GAS u32*)(ws + off) + i, __ATOMIC_RELAXED, __HIP_MEMORY_SCOPE_AGENT);
    if (__all(v == tag)) break;
    __builtin_amdgcn_s_sleep(1);
  }
  asm volatile("" ::: "memory");
}
DI float poll_kv(char* ws, int rep, u32 tag, int lane) {
  u64 g;
#pragma unroll 1
  for (u32 spin = 0; spin < (1u << 17); ++spin) {
    asm volatile("" ::: "memory");
    g = __hip_atomic_load((GAS u64*)(ws + WS_KVFLAG + rep * 256) + (lane & 31), __ATOMIC_RELAXED, __HIP_MEMORY_SCOPE_AGENT);
    if (__all((u32)g == tag)) break;
    __builtin_amdgcn_s_sleep(1);
  }
  asm volatile("" ::: "memory");
  return __uint_as_float((u32)(g >> 32));
}
DI void cmul(float ar, float ai, float br, float bi, float& cr, float& ci) {
  float x, y;
  asm("v_mul_f32 %0, %2, %4\n\tv_mul_f32 %1, %2, %5\n\tv_fma_f32 %0, -%3, %5, %0\n\tv_fma_f32 %1, %3, %4, %1"
      : "=&v"(x), "=&v"(y) : "v"(ar), "v"(ai), "v"(br), "v"(bi));
  cr = x; ci = y;
}
DI void cstep(float ar, float ai, float& hr, float& hi, float ur, float ui) {
  float x, y;
  asm("v_fma_f32 %0, -%3, %5, %6\n\tv_fma_f32 %1, %3, %4, %7\n\tv_fma_f32 %0, %2, %4, %0\n\tv_fma_f32 %1, %2, %5, %1"
      : "=&v"(x), "=&v"(y) : "v"(ar), "v"(ai), "v"(hr), "v"(hi), "v"(ur), "v"(ui));
  hr = x; hi = y;
}
DI void set_pad(u32x4& f, float m, int h) {
  const float a = rbf(-m), b = rbf(-m - a), c = rbf(-m - a - b);
  if (h) { f.z = cvtpk(a, b); f.w = cvtpk(c, 0.f); }
}

#define TS(i) do {} while (0)
#define TSD() do {} while (0)
#define TSW() do {} while (0)
#define LPRIO 0
#define PUP() do {} while (0)
#define PDN() SBAR()
#define STR_(x) #x
#define VMTHR_(n) asm volatile("s_waitcnt vmcnt(" STR_(n) ")" ::: "memory")
#define VMTHR() VMTHR_(THR)

template <bool W_IS_A, int WROW, bool MID>
DI f32x16 proj_tile(int row, int h, u32 wa, int lane) {
  const int sw = (row >> 1) & 7;
  u32 xa[4];
#pragma unroll
  for (int bb = 0; bb < 4; ++bb) xa[bb] = L_XB + row * 128 + (((2 * bb + h) ^ sw) << 4);
  u32x4 xf[28], wf[28];
  f32x16 acc = {};
#pragma unroll
  for (int ks = 0; ks < 28 + 5; ++ks) {
    if (ks >= 5) {
      const int k = ks - 5;
      acc = W_IS_A ? MFMA(frag(wf[k]), frag(xf[k]), acc) : MFMA(frag(xf[k]), frag(wf[k]), acc);
      SBAR();
    }
    if (ks < 28) {
      if (MID && ks == 14) {
        asm volatile("s_nop 7\n\ts_nop 7\n\ts_waitcnt vmcnt(0)" ::: "memory");
        if (lane == 0) asm volatile("ds_add_u32 %0, %1" :: "v"(L_MISC + 4 * C_WQ2), "v"(1u) : "memory");
        await(C_WQ2, 4);
      }
      xf[ks] = ldsr128(xa[ks & 3] + 8192 * (ks >> 2));
      wf[ks] = trfrag(wa + ks * 16 * WROW, wa + ks * 16 * WROW + 4 * WROW);
      SBAR();
    }
  }
  MPIN(acc);
  return acc;
}

__global__ void __launch_bounds__(512, 2) fused_kernel(Params P) {
  extern __shared__ __attribute__((aligned(16))) char lds_dyn[];
  const int tid = threadIdx.x, lane = tid & 63, wv = __builtin_amdgcn_readfirstlane(tid >> 6);
  const int r = lane & 31, h = lane >> 5;
  const int b = blockIdx.x;
  const int tb = (b & 7) * 4 + (b >> 6), sl = (b >> 3) & 7;
  char* ws = P.ws;
  TSD();
  TS(0);
  {
    const int row = 8 * wv + (lane >> 3);
    const u32 vo = row * 896 + (((lane & 7) ^ ((row >> 1) & 7)) << 4);
    const char* src = (const char*)P.Bx + (size_t)tb * 64 * 896;
#pragma unroll
    for (int c = 0; c < 7; ++c) glds16(src + c * 128, vo, L_XB + c * 8192 + wv * 1024);
  }
  if (tid < 31) *(LAS u32*)(size_t)(L_MISC + 4 * tid) = 0;
  if (tid == 0) asm volatile("s_mov_b32 m0, %2\n\ts_nop 0\n\tglobal_load_lds_dword %0, %1 sc1" :: "v"(0), "s"(ws), "s"(L_MISC + 4 * C_EPOCH) : "memory");
  const auto rs_ws = __builtin_amdgcn_make_buffer_rsrc((void*)ws, 0, WS_END, 0x00020000);
  const int g1 = (lane >> 4) & 1, q4 = (lane >> 2) & 3, p4 = lane & 3;
  const int l7 = lane / 7;
  const u32 vo7 = l7 * 896 + (lane - 7 * l7) * 16;
  const u32 vo = lane * 16;

  if (wv < 4) {
    const int w = wv;
    const char* wqsrc = (const char*)P.wq + sl * 112 + w * 7 * 7168;
    if (sl != 7) {
      if (lane < 56) {
#pragma unroll
        for (int j = 0; j < 14; ++j) glds16(wqsrc + (j % 7 + 28 * (j / 7)) * 7168, vo7, L_WQ + (w * 7 + j % 7 + 28 * (j / 7)) * 896);
      }
      asm volatile("s_waitcnt vmcnt(14) lgkmcnt(0)\n\ts_barrier" ::: "memory");
    } else {
      asm volatile("s_waitcnt vmcnt(0) lgkmcnt(0)\n\ts_barrier" ::: "memory");
      if (lane < 56) {
#pragma unroll
        for (int j = 0; j < 14; ++j) glds16(wqsrc + (j % 7 + 28 * (j / 7)) * 7168, vo7, L_WQ + (w * 7 + j % 7 + 28 * (j / 7)) * 896);
      }
    }
    TS(13);
    const u32 tag = ldsu(L_MISC + 4 * C_EPOCH) + 1u;
    VMCNT(7);
    TS(14);
    arrive(C_WQ, lane);
    await(C_WQ, 4);
    TS(1);
    {
      f32x16 acc = proj_tile<true, 112, true>(32 * (w & 1) + r, h, L_WQ + (8 * h + q4) * 112 + (w >> 1) * 56 + g1 * 32 + 8 * p4, lane);
      TS(8);
      const float qs = 0.27264060f;
      float n2 = 0.f;
#pragma unroll
      for (int i = 0; i < 16; ++i) { acc[i] *= qs; if (i < 12) n2 = fmaf(acc[i], acc[i], n2); else n2 = h ? n2 : fmaf(acc[i], acc[i], n2); }
      n2 += swap32(n2);
      u32x4 f0 = {cvtpk(acc[0], acc[1]), cvtpk(acc[2], acc[3]), cvtpk(acc[4], acc[5]), cvtpk(acc[6], acc[7])};
      u32x4 f1 = {cvtpk(acc[8], acc[9]), cvtpk(acc[10], acc[11]), cvtpk(acc[12], acc[13]), cvtpk(acc[14], acc[15])};
      if (h) { f1.z = 0; f1.w = __float_as_uint(__builtin_amdgcn_sqrtf(n2)); }
      if (sl == 7) await(C_AGG, 4);
      ldsw128(L_QX + w * 2048 + vo, f0);
      ldsw128(L_QX + w * 2048 + 1024 + vo, f1);
      arrive(C_Q2, lane);
    }
    u32x4 qf[4][2];
    if (QEARLY) {
      await(C_Q2, 4);
#pragma unroll
      for (int t = 0; t < 4; ++t) { const u32 a = L_QX + ((w + t) & 3) * 2048 + vo; qf[t][0] = ldsr128(a); qf[t][1] = ldsr128(a + 1024); }
      arrive(C_QDONE, lane);
    }
    TS(2);
    await(C_KV, 1);
    TS(3);
    const u32 kvbase = WS_KVF + w * 65536;
    const int rot = b >> 3;
#define KVOFF(it) (kvbase + ((((it) + rot) & 15) << 12))
#define LDK(it, s) __builtin_amdgcn_raw_buffer_load_b128(rs_ws, vo + 1024 * (s), KVOFF(it), KV_AUX)
#define LDV(it, s) __builtin_amdgcn_raw_buffer_load_b128(rs_ws, vo + 2048 + 1024 * (s), KVOFF(it), KV_AUX)
    f32x16 o[4];
    for (int attempt = 0; attempt < 2; ++attempt) {
      u32x4 kf[KV_DEPTH][2], vf[KV_DEPTH][2];
#pragma unroll
      for (int d = 0; d < KV_DEPTH; ++d) {
        kf[d][0] = LDK(d, 0); kf[d][1] = LDK(d, 1);
        if (d < KV_DEPTH - 1) { vf[d][0] = LDV(d, 0); vf[d][1] = LDV(d, 1); }
      }
      vf[KV_DEPTH - 1][0] = (u32x4){0, 0, 0, 0}; vf[KV_DEPTH - 1][1] = (u32x4){0, 0, 0, 0};
      SBAR();
      if (attempt == 0) {
        if (!QEARLY) {
        await(C_Q2, 4);
#pragma unroll
        for (int t = 0; t < 4; ++t) { const u32 a = L_QX + ((w + t) & 3) * 2048 + vo; qf[t][0] = ldsr128(a); qf[t][1] = ldsr128(a + 1024); }
        arrive(C_QDONE, lane);
        }
        const float kmax = __builtin_amdgcn_sqrtf(wave_max_nonneg(*(LAS float*)(size_t)(L_KN + 4 * r)));
#pragma unroll
        for (int t = 0; t < 4; ++t) set_pad(qf[t][1], __uint_as_float(qf[t][1].w) * kmax, h);
      }
#pragma unroll
      for (int t = 0; t < 4; ++t) { o[t] = (f32x16){}; PIN(o[t]); PIN(qf[t][0]); PIN(qf[t][1]); }
      SBAR();
      const f32x16 zero = {};
      f32x16 sc = MFMA(frag(kf[0][0]), frag(qf[0][0]), zero);
      sc = MFMA(frag(kf[0][1]), frag(qf[0][1]), sc);
      TS(9);
      float e0 = 0.f, e1 = 0.f, e2 = 0.f, e3 = 0.f;
      u32x4 p1 = {0, 0, 0, 0};
      for (int it0 = 0; it0 < 16; it0 += KV_DEPTH) {
#pragma unroll
        for (int d = 0; d < KV_DEPTH; ++d) {
#pragma unroll
          for (int qi = 0; qi < 4; ++qi) {
            const int dn = qi == 3 ? (d + 1) % KV_DEPTH : d, qn = (qi + 1) & 3;
            const int dp = qi == 0 ? (d + KV_DEPTH - 1) % KV_DEPTH : d, qp = (qi + 3) & 3;
            f32x16 sn; u32x4 p0;
            float c0, c1, c2, c3;
            sn = MFMA(frag(kf[dn][0]), frag(qf[qn][0]), zero); SBAR();
            p1.z = cvtpk(e0, e1); p1.w = cvtpk(e2, e3); PIN(p1); SBAR();
            c0 = EX(sc[0]); c1 = EX(sc[1]); c2 = EX(sc[2]); c3 = EX(sc[3]); PIN4(c0, c1, c2, c3); SBAR();
            o[qp] = MFMA(frag(vf[dp][1]), frag(p1), o[qp]); SBAR();
            p0.x = cvtpk(c0, c1); p0.y = cvtpk(c2, c3); PIN2(p0.x, p0.y); SBAR();
            c0 = EX(sc[4]); c1 = EX(sc[5]);
            if (!NOLOAD && qi == 0) { PIN2(c0, c1); SBAR(); PUP(); vf[dp][0] = LDV(it0 + d - 1 + KV_DEPTH, 0); PDN(); }
            if (!NOLOAD && qi == 1) { PIN2(c0, c1); SBAR(); const int dq = (d + KV_DEPTH - 1) % KV_DEPTH; PUP(); vf[dq][1] = LDV(it0 + d - 1 + KV_DEPTH, 1); PDN(); }
            c2 = EX(sc[6]); c3 = EX(sc[7]); PIN4(c0, c1, c2, c3); SBAR();
            sn = MFMA(frag(kf[dn][1]), frag(qf[qn][1]), sn); SBAR();
            p0.z = cvtpk(c0, c1); p0.w = cvtpk(c2, c3); PIN(p0); SBAR();
            c0 = EX(sc[8]); c1 = EX(sc[9]);
            if (!NOLOAD && qi == 2) { PIN2(c0, c1); SBAR(); PUP(); kf[d][0] = LDK(it0 + d + KV_DEPTH, 0); PDN(); }
            c2 = EX(sc[10]); c3 = EX(sc[11]); PIN4(c0, c1, c2, c3); SBAR();
            o[qi] = MFMA(frag(vf[d][0]), frag(p0), o[qi]); SBAR();
            p1.x = cvtpk(c0, c1); p1.y = cvtpk(c2, c3); PIN2(p1.x, p1.y); SBAR();
            e0 = EX(sc[12]); e1 = EX(sc[13]);
            if (!NOLOAD && qi == 2) { PIN2(e0, e1); SBAR(); PUP(); kf[d][1] = LDK(it0 + d + KV_DEPTH, 1); PDN(); }
            e2 = EX(sc[14]); e3 = EX(sc[15]); PIN4(e0, e1, e2, e3); SBAR();
            sc = sn;
          }
        }
      }
      p1.z = cvtpk(e0, e1); p1.w = cvtpk(e2, e3);
      o[3] = MFMA(frag(vf[KV_DEPTH - 1][1]), frag(p1), o[3]);
      MPIN4(o[0], o[1], o[2], o[3]);
      TS(4);
      if (attempt == 0) {
        bool bad = false;
#pragma unroll
        for (int t = 0; t < 4; ++t) bad |= !(o[t][12] >= 0x1p-80f && o[t][12] <= 0x1p100f);
        if (__any(bad && h)) { if (lane == 0) *(LAS u32*)(size_t)(L_MISC + 4 * C_FB) = 1u; }
        await(C_CREG, 4);
      }
#pragma unroll
      for (int t = 1; t < 4; ++t) {
#pragma unroll
        for (int g = 0; g < 4; ++g) ldsw128(w * L_PRIV + L_OS + (t - 1) * 4096 + g * 1024 + vo, f4(o[t], g));
      }
      arrive(C_O, lane);
      await(C_O, 4 * (attempt + 1));
      TS(10);
      const u32 fb = attempt == 0 ? ldsu(L_MISC + 4 * C_FB) : 0u;
      if (!fb) break;
      {
        float mx[4];
#pragma unroll
        for (int t = 0; t < 4; ++t) { mx[t] = -3.0e38f; if (h) { qf[t][1].z = 0; qf[t][1].w = 0; } }
#pragma unroll 1
        for (int it = 0; it < 16; ++it) {
          const u32x4 k0 = LDK(it, 0), k1 = LDK(it, 1);
#pragma unroll
          for (int t = 0; t < 4; ++t) {
            f32x16 s = MFMA(frag(k0), frag(qf[t][0]), zero);
            s = MFMA(frag(k1), frag(qf[t][1]), s);
#pragma unroll
            for (int i = 0; i < 16; ++i) mx[t] = fmaxf(mx[t], s[i]);
          }
        }
        const u32 ma = L_C + 51200;
#pragma unroll
        for (int t = 0; t < 4; ++t) { mx[t] = fmaxf(mx[t], swap32(mx[t])); *(LAS float*)(size_t)(ma + (w * 4 + ((w + t) & 3)) * 256 + lane * 4) = mx[t]; }
        arrive(C_MAX, lane);
        await(C_MAX, 4);
#pragma unroll
        for (int t = 0; t < 4; ++t) {
          float m = -3.0e38f;
#pragma unroll
          for (int ww = 0; ww < 4; ++ww) m = fmaxf(m, *(LAS float*)(size_t)(ma + (ww * 4 + ((w + t) & 3)) * 256 + lane * 4));
          set_pad(qf[t][1], m, h);
        }
      }
    }
    {
      f32x16 own = o[0];
#pragma unroll
      for (int pp = 1; pp < 4; ++pp) {
        const u32 a = ((w + pp) & 3) * L_PRIV + L_OS + (3 - pp) * 4096 + vo;
#pragma unroll
        for (int g = 0; g < 4; ++g) add4(own, g, ldsr128(a + g * 1024));
      }
      const float other = swap32(own[12]);
      const float inv = __builtin_amdgcn_rcpf(h ? own[12] : other);
      u32x4 f0 = {cvtpk(own[0] * inv, own[1] * inv), cvtpk(own[2] * inv, own[3] * inv), cvtpk(own[4] * inv, own[5] * inv), cvtpk(own[6] * inv, own[7] * inv)};
      u32x4 f1 = {cvtpk(own[8] * inv, own[9] * inv), cvtpk(own[10] * inv, own[11] * inv), cvtpk(own[12] * inv, own[13] * inv), cvtpk(own[14] * inv, own[15] * inv)};
      if (h) { f1.z = 0; f1.w = 0; }
      const u32 so = WS_OF + ((tb * 2 + (w & 1)) * 16 + 2 * sl + (w >> 1)) * 2048 + vo;
      __builtin_amdgcn_raw_buffer_store_b128(f0, rs_ws, so, 0, 16);
      __builtin_amdgcn_raw_buffer_store_b128(f1, rs_ws, so + 1024, 0, 16);
    }
    {
      const int tm = w & 1, kh = w >> 1;
      TS(11);
      VMCNT(0);
      if (lane == 0) gstore_flag(ws, WS_OFLAG + (tb * 32 + sl * 4 + w) * 4, tag);
      TS(5);
      await(C_WO, 4);
      u32x4 wf[16][2];
      const u32 wa = L_C + (224 * kh + 4 * h + q4) * 112 + g1 * 32 + 8 * p4;
      const u32 wa0 = wa + 64 * kh, wa1 = wa + 64 * (1 - kh);
#pragma unroll
      for (int i = 0; i < 16; ++i) {
        const u32 a = (28 * (i >> 1) + 16 * (i & 1)) * 112;
        wf[i][0] = trfrag(wa0 + a, wa0 + a + 896); wf[i][1] = trfrag(wa1 + a, wa1 + a + 896);
      }
#pragma unroll
      for (int i = 0; i < 16; ++i) PIN2(wf[i][0], wf[i][1]);
      if (w == 0) { poll_words(ws, WS_OFLAG + tb * 128, 32, tag, lane); arrive(C_OF, lane); }
      else await(C_OF, 1);
      TS(6);
      u32x4 of[16];
      const u32 so = WS_OF + ((tb * 2 + tm) * 16 + 8 * kh) * 2048;
#pragma unroll
      for (int i = 0; i < 16; ++i) of[i] = __builtin_amdgcn_raw_buffer_load_b128(rs_ws, vo + (i & 3) * 1024, so + (i >> 2) * 4096, H_AUX);
      SBAR();
      f32x16 a0 = {}, a1 = {};
#pragma unroll
      for (int i = 0; i < 16; ++i) {
        a0 = MFMA(frag(of[i]), frag(wf[i][0]), a0);
        a1 = MFMA(frag(of[i]), frag(wf[i][1]), a1);
      }
      MPIN2(a0, a1);
      TS(12);
      const f32x16 snd = a1;
      f32x16 own = a0;
      const u32 aw = w * L_PRIV + L_OS + vo, ar = (w ^ 2) * L_PRIV + L_OS + vo;
#pragma unroll
      for (int g = 0; g < 4; ++g) ldsw128(aw + g * 1024, f4(snd, g));
      arrive(C_P, lane);
      await(C_P, 4);
#pragma unroll
      for (int g = 0; g < 4; ++g) add4(own, g, ldsr128(ar + g * 1024));
      if (32 * kh + r < 56) {
        float* op = P.out + (size_t)(tb * 64 + 32 * tm + 4 * h) * 896 + 448 + 56 * sl + 32 * kh + r;
#pragma unroll
        for (int i = 0; i < 16; ++i) op[((i & 3) + 8 * (i >> 2)) * 896] = rbf(own[i]);
      }
    }
    TS(7);
    TSW();
  } else {
    __builtin_amdgcn_s_setprio(AUX_PRIO);
    const int q = wv - 4;
    const u32 PA = q * L_PRIV;
    const int part = q >> 1, half = q & 1;
    u32 tag;
    auto stage_C = [&]() {
      if (!(ABL & 2) && lane < 56) {
        const char* src = (const char*)(part ? P.Cim : P.Cre) + (size_t)sl * 56 * 896 + half * 448;
        const int hi = lane >= 28, cs = lane - 28 * hi;
        u32 cvo[4];
#pragma unroll
        for (int t = 0; t < 4; ++t) cvo[t] = hi * 896 + ((cs ^ t) << 4);
#pragma unroll
        for (int i = 0; i < 28; ++i) { glds16(src + i * 1792, cvo[(i >> 1) & 3], PA + i * 896); VMTHR(); }
      }
    };
    auto stage_wo = [&]() {
      if (!(ABL & 4) && lane < 56) {
        const char* src = (const char*)P.wo + sl * 112 + q * 14 * 7168;
#pragma unroll
        for (int i = 0; i < 14; ++i) { glds16(src + i * 7168, vo7, L_C + (q * 14 + i) * 896); VMTHR(); }
        if (q == 3) glds16(src, vo7, L_C + 56 * 896);
      }
    };
    auto kv_wait = [&]() {
      const float kn = poll_kv(ws, (b >> 3) % KV_REPL, tag, lane);
      if (FENCE) { __builtin_amdgcn_fence(__ATOMIC_ACQUIRE, "agent"); VMCNT(0); }
      if (lane < 32) *(LAS float*)(size_t)(L_KN + 4 * lane) = kn;
      arrive(C_KV, lane);
    };
    u32x4 cf[2][14];
    auto read_C = [&]() {
#pragma unroll
      for (int tn = 0; tn < 2; ++tn) {
        int row = 32 * (tn ^ part) + r; row = row > 55 ? 55 : row;
        const int t = (row >> 2) & 3;
        const u32 a0 = PA + row * 448 + ((h ^ t) << 4), a1 = PA + row * 448 + (((2 + h) ^ t) << 4);
#pragma unroll
        for (int j = 0; j < 14; ++j) cf[tn][j] = ldsr128(((j & 1) ? a1 : a0) + 64 * (j >> 1));
      }
    };
    if (sl == 7) {
      const int kind = q >> 1, tm = q & 1;
      if (lane < 56) {
        const char* src = (const char*)(kind ? P.wv : P.wk) + tm * 14 * 896;
#pragma unroll
        for (int j = 0; j < 14; ++j) glds16(src + j * 896, vo, L_C + (q * 14 + j) * 896);
      }
      asm volatile("s_waitcnt vmcnt(0) lgkmcnt(0)\n\ts_barrier" ::: "memory");
      TS(1);
      const u32 wa = L_C + kind * 25088 + (8 * h + q4) * 56 + g1 * 32 + 8 * p4;
      f32x16 acc;
      if (kind == 0) acc = proj_tile<true, 56, false>(32 * tm + r, h, wa, lane);
      else acc = proj_tile<false, 56, false>(32 * tm + r, h, wa, lane);
      TS(3);
      u32x4 f0 = {cvtpk(acc[0], acc[1]), cvtpk(acc[2], acc[3]), cvtpk(acc[4], acc[5]), cvtpk(acc[6], acc[7])};
      u32x4 f1 = {cvtpk(acc[8], acc[9]), cvtpk(acc[10], acc[11]), cvtpk(acc[12], acc[13]), cvtpk(acc[14], acc[15])};
      if (kind == 0) {
        if (h) { f1.z = 0x3F803F80u; f1.w = 0x00003F80u; }
      } else if (r >= 28) {
        const u32 c = r == 28 ? 0x3F803F80u : 0u;
        f0 = (u32x4){c, c, c, c}; f1 = f0;
      }
      const u32 so = WS_KVF + (tb * 2 + tm) * 4096 + kind * 2048 + vo;
      __builtin_amdgcn_raw_buffer_store_b128(f0, rs_ws, so, 0, 16);
      __builtin_amdgcn_raw_buffer_store_b128(f1, rs_ws, so + 1024, 0, 16);
      SBAR();
      tag = ldsu(L_MISC + 4 * C_EPOCH) + 1u;
      if (kind == 0) {
        float n2 = 0.f;
#pragma unroll
        for (int i = 0; i < 16; ++i) { if (i < 12) n2 = fmaf(acc[i], acc[i], n2); else n2 = h ? n2 : fmaf(acc[i], acc[i], n2); }
        n2 += swap32(n2);
        n2 = wave_max_nonneg(n2);
        if (lane == 0) asm volatile("ds_max_u32 %0, %1" :: "v"(L_MISC + 4 * C_KMAX), "v"(n2) : "memory");
      }
      TS(4);
      VMCNT(0);
      TS(5);
      u32 old = 0;
      if (lane == 0) asm volatile("s_waitcnt lgkmcnt(0)\n\tds_add_rtn_u32 %0, %1, %2\n\ts_waitcnt lgkmcnt(0)" : "=&v"(old) : "v"(L_MISC + 4 * C_KVP), "v"(1u) : "memory");
      if (__builtin_amdgcn_readfirstlane(old) == 3) {
        const u32 km = ldsu(L_MISC + 4 * C_KMAX);
        if (lane < KV_REPL) __hip_atomic_store((GAS u64*)(ws + WS_KVFLAG + lane * 256 + tb * 8), (u64)tag | ((u64)km << 32), __ATOMIC_RELAXED, __HIP_MEMORY_SCOPE_AGENT);
      }
      TS(2);
      arrive(C_AGG, lane);
      if (q == 0) kv_wait();
      await(C_AGG, 4);
      await(C_QDONE, 4);
      stage_wo();
    } else {
      const int tb2 = tb >> 1, cg = (tb & 1) * 7 + sl, ch = 32 * cg + r;
      u32 vlre = P.lre[ch], vlim = P.lim[ch], vldt = P.ldt[ch];
      u32x4 blast_re = *(const u32x4*)(P.Bre + (size_t)ch * 448 + 432 + 8 * h);
      u32x4 blast_im = *(const u32x4*)(P.Bim + (size_t)ch * 448 + 432 + 8 * h);
      asm volatile("s_waitcnt vmcnt(0) lgkmcnt(0)\n\ts_barrier" ::: "memory");
      asm volatile("" : "+v"(vlre), "+v"(vlim), "+v"(vldt), "+v"(blast_re), "+v"(blast_im) :: "memory");
      await(C_WQ, 4);
      const char* bsrc = (const char*)(part ? P.Bim : P.Bre) + (size_t)(cg * 32 + half * 16) * 896;
      const u32 vo1 = (lane ^ 1) * 16;
      if (lane < 54) {
#pragma unroll
        for (int i = 0; i < 16; ++i)
          if (i < 6 || q != 3) glds16(bsrc + i * 896, i < 8 ? vo : vo1, L_C + (q * 16 + i) * 864);
      }
      tag = ldsu(L_MISC + 4 * C_EPOCH) + 1u;
      float lr, li, cr, ci;
      {
        const float dt = rbf(expf(__uint_as_float(vldt << 16))), a = __uint_as_float(vlre << 16), bb = __uint_as_float(vlim << 16);
        const float er = expf(dt * a);
        float sn, cs;
        sincosf(dt * bb, &sn, &cs);
        lr = er * cs; li = er * sn;
        const float nr = lr - 1.f, ni = li, den = 2.0f / (a * a + bb * bb);
        cr = (nr * a + ni * bb) * den; ci = (ni * a - nr * bb) * den;
      }
      float l16r = lr, l16i = li;
#pragma unroll
      for (int i = 0; i < 4; ++i) cmul(l16r, l16i, l16r, l16i, l16r, l16i);
      float l32r, l32i, l128r, l128i;
      cmul(l16r, l16i, l16r, l16i, l32r, l32i);
      cmul(l32r, l32i, l32r, l32i, l128r, l128i);
      cmul(l128r, l128i, l128r, l128i, l128r, l128i);
      PIN4(lr, li, cr, ci); PIN4(l16r, l16i, l32r, l32i); PIN2(l128r, l128i);
      const char* xsrc = (const char*)P.Ax + (size_t)(tb2 * 128 + q * 32) * 896;
      const u32 xv0 = (lane >> 3) * 896 + (((lane & 7) ^ (lane >> 4)) << 4), xv1 = xv0 ^ 64;
      const int rho = 16 * ((r >> 2) & 1) + (r & 3) + 4 * (r >> 3), sw = (rho >> 1) & 7;
      u32 xa[4];
#pragma unroll
      for (int bb = 0; bb < 4; ++bb) xa[bb] = PA + rho * 128 + (((2 * bb + h) ^ sw) << 4);
      const u32 ba = L_C + r * 864 + ((h ^ ((r >> 3) & 1)) << 4);
      PIN4(xa[0], xa[1], xa[2], xa[3]);
      TS(1);
      if (q == 0) kv_wait();
      await(C_QDONE, 4);
      if (q == 3 && lane < 54) {
#pragma unroll
        for (int i = 6; i < 16; ++i) glds16(bsrc + i * 896, i < 8 ? vo : vo1, L_C + (48 + i) * 864);
      }
#pragma unroll
      for (int c = 0; c < 6; ++c)
#pragma unroll
        for (int j = 0; j < 4; ++j) glds16(xsrc + c * 128 + j * 7168, (j & 1) ? xv1 : xv0, PA + c * 4096 + j * 1024);
      VMCNT(24);
      arrive(C_BFULL, lane);
      await(C_BFULL, 4);
      TS(2);
      f32x16 ur = {}, ui = {};
      PIN2(ur, ui);
      __builtin_amdgcn_s_setprio(G1_PRIO);
      {
        u32x4 xf[28], br[28], bi[28];
#pragma unroll
        for (int ks = 0; ks < 28 + 4; ++ks) {
          if (ks >= 4) {
            const int k = ks - 4;
            ur = MFMA(frag(xf[k]), frag(br[k]), ur);
            ui = MFMA(frag(xf[k]), frag(bi[k]), ui);
            SBAR();
            if (k == 3) {
#pragma unroll
              for (int j = 0; j < 4; ++j) glds16(xsrc + 6 * 128 + j * 7168, (j & 1) ? xv1 : xv0, PA + j * 1024);
            }
          }
          if (ks < 28) {
            if (ks == 0) VMCNT(20); else if (ks == 4) VMCNT(16); else if (ks == 8) VMCNT(16); else if (ks == 12) VMCNT(12);
            else if (ks == 16) VMCNT(8); else if (ks == 20) VMCNT(4); else if (ks == 24) VMCNT(0);
            xf[ks] = ldsr128(xa[ks & 3] + ((ks >> 2) % 6) * 4096);
            br[ks] = ks < 27 ? ldsr128(ba + 32 * ks) : blast_re;
            bi[ks] = ks < 27 ? ldsr128(ba + 27648 + 32 * ks) : blast_im;
            SBAR();
          }
        }
      }
      MPIN2(ur, ui);
      __builtin_amdgcn_s_setprio(AUX_PRIO);
      TS(3);
      float ar = 0.f, ai = 0.f;
#pragma unroll
      for (int i = 0; i < 16; ++i) {
        float tr, ti;
        cmul(cr, ci, ur[i], ui[i], tr, ti);
        ur[i] = tr; ui[i] = ti;
        cstep(lr, li, ar, ai, tr, ti);
      }
      const float or_ = swap32(ar), oi_ = swap32(ai);
      const float lor = h ? or_ : ar, loi = h ? oi_ : ai;
      float a32r = lor, a32i = loi;
      cstep(l16r, l16i, a32r, a32i, h ? ar : or_, h ? ai : oi_);
      if (q < 3 && h == 0) *(LAS f32x2*)(size_t)(L_AGG + q * 256 + r * 8) = (f32x2){a32r, a32i};
      arrive(C_AGG, lane);
      await(C_AGG, 4);
      f32x2 wagg[3];
#pragma unroll
      for (int j = 0; j < 3; ++j) wagg[j] = *(LAS f32x2*)(size_t)(L_AGG + j * 256 + r * 8);
      const u32 aggb = WS_AGG + cg * 8192;
      if (q == 3 && tb2 < 15) {
        float tr = wagg[0].x, ti = wagg[0].y;
        cstep(l32r, l32i, tr, ti, wagg[1].x, wagg[1].y);
        cstep(l32r, l32i, tr, ti, wagg[2].x, wagg[2].y);
        cstep(l32r, l32i, tr, ti, a32r, a32i);
        __hip_atomic_store((GAS u64*)(ws + aggb + tb2 * 512 + r * 16 + h * 8), (u64)__float_as_uint(h ? ti : tr) | ((u64)tag << 32), __ATOMIC_RELAXED, __HIP_MEMORY_SCOPE_AGENT);
      }
      stage_wo();
      TS(4);
      float hr = 0.f, hi = 0.f;
      if (!(ABL & 1) && tb2 > 0) {
        u32x4 g[8];
        u32 gv[8];
#pragma unroll
        for (int j = 0; j < 8; ++j) gv[j] = r * 16 + ((8 * h + j < tb2) ? (8 * h + j) * 512 : 0);
#pragma unroll 1
        for (u32 spin = 0; spin < (1u << 17); ++spin) {
          asm volatile("" ::: "memory");
          u32 bad = 0;
#pragma unroll
          for (int j = 0; j < 8; ++j) g[j] = __builtin_amdgcn_raw_buffer_load_b128(rs_ws, gv[j], aggb, 16);
#pragma unroll
          for (int j = 0; j < 8; ++j) bad |= (g[j].y ^ tag) | (g[j].w ^ tag);
          if (__all(bad == 0)) break;
          __builtin_amdgcn_s_sleep(4);
        }
        asm volatile("" ::: "memory");
#pragma unroll
        for (int j = 0; j < 8; ++j)
          if (j < tb2) cstep(l128r, l128i, hr, hi, __uint_as_float(g[j].x), __uint_as_float(g[j].z));
        const float sr = swap32(hr), si = swap32(hi);
        if (h) { hr = sr; hi = si; }
        if (tb2 > 8) {
#pragma unroll
          for (int j = 0; j < 7; ++j)
            if (8 + j < tb2) cstep(l128r, l128i, hr, hi, __uint_as_float(g[j].x), __uint_as_float(g[j].z));
          const float tr = swap32(hr), ti = swap32(hi);
          if (!h) { hr = tr; hi = ti; }
        }
      }
#pragma unroll
      for (int j = 0; j < 3; ++j)
        if (j < q) cstep(l32r, l32i, hr, hi, wagg[j].x, wagg[j].y);
      if (h) cstep(l16r, l16i, hr, hi, lor, loi);
      TS(5);
      u32 pr[8], pi[8];
#pragma unroll
      for (int i = 0; i < 16; i += 2) {
        cstep(lr, li, hr, hi, ur[i], ui[i]);
        const float r0 = hr, i0 = hi;
        cstep(lr, li, hr, hi, ur[i + 1], ui[i + 1]);
        pr[i >> 1] = cvtpk(r0, hr); pi[i >> 1] = cvtpk(-i0, -hi);
      }
      const u32 ta = PA + r * 80 + 32 * h;
      ldsw128(ta, (u32x4){pr[0], pr[1], pr[2], pr[3]}); ldsw128(ta + 16, (u32x4){pr[4], pr[5], pr[6], pr[7]});
      ldsw128(ta + 2560, (u32x4){pi[0], pi[1], pi[2], pi[3]}); ldsw128(ta + 2560 + 16, (u32x4){pi[4], pi[5], pi[6], pi[7]});
      LGKM0();
      const u32 tr = PA + (8 * h + q4) * 80 + g1 * 32 + 8 * p4;
      const int tbh = 2 * tb2 + (q >> 1);
      const u32 so = WS_HF + ((tbh * 2 + (q & 1)) * 56 + 2 * cg) * 1024 + vo;
#pragma unroll
      for (int pt = 0; pt < 2; ++pt)
#pragma unroll
        for (int s = 0; s < 2; ++s) {
          const u32 a = tr + pt * 2560 + s * 1280;
          if (!(ABL & 16)) __builtin_amdgcn_raw_buffer_store_b128(trfrag(a, a + 320), rs_ws, so + pt * 28 * 1024 + s * 1024, 0, 16);
        }
      VMCNT(0);
      if (lane == 0) gstore_flag(ws, WS_HFLAG + (tbh * 32 + cg * 2 + (q & 1)) * 4, tag);
      TS(6);
    }
    VMCNT(0);
    arrive(C_WO, lane);
    stage_C();
    const int tm = q & 1, tn = q >> 1;
    int col = 32 * tn + r; const bool valid = col < 56; col = valid ? col : 55;
    const int d = 56 * sl + col, t0 = tb * 64 + 32 * tm + 4 * h;
    float skip[16];
    {
      u16 xs[16];
#pragma unroll
      for (int i = 0; i < 16; ++i) xs[i] = (ABL & 32) ? (u16)i : P.Ax[(size_t)(t0 + (i & 3) + 8 * (i >> 2)) * 448 + d];
      const float dsk = bf2f(P.Dsk[d]);
#pragma unroll
      for (int i = 0; i < 16; i += 2) { float a = bf2f(xs[i]) * dsk, c = bf2f(xs[i + 1]) * dsk; PIN(a); skip[i >> 1] = __uint_as_float(cvtpk(a, c)); }
#pragma unroll
      for (int i = 0; i < 8; ++i) PIN(skip[i]);
    }
    VMCNT(0);
    read_C();
#pragma unroll
    for (int j = 0; j < 14; ++j) PIN2(cf[0][j], cf[1][j]);
    arrive(C_CREG, lane);
    TS(9);
    if (ABL & 8) { TSW(); if (tb == 31 && sl == 0 && q == 0) { for (int i = 0; i < 1000; ++i) __builtin_amdgcn_s_sleep(1); if (lane == 0) gstore_flag(ws, WS_EPOCH, tag); } return; }
    if (q == 0) { poll_words(ws, WS_HFLAG + tb * 128, 28, tag, lane); arrive(C_HF, lane); }
    else await(C_HF, 1);
    TS(7);
    {
      f32x16 y[4];
      const u32 so = WS_HF + ((tb * 2 + tm) * 56 + part * 28 + half * 14) * 1024, so1 = WS_HF + ((tb * 2 + (tm ^ 1)) * 56 + part * 28 + half * 14) * 1024;
      u32x4 hf[14];
#pragma unroll
      for (int j = 0; j < 14; ++j) hf[j] = __builtin_amdgcn_raw_buffer_load_b128(rs_ws, vo + (j & 3) * 1024, so + (j >> 2) * 4096, H_AUX);
      SBAR();
#pragma unroll
      for (int t = 0; t < 4; ++t) y[t] = (f32x16){};
      PIN4(y[0], y[1], y[2], y[3]);
      __builtin_amdgcn_s_setprio(AUX2_PRIO);
#pragma unroll
      for (int j = 0; j < 14; ++j) {
        y[0] = MFMA(frag(hf[j]), frag(cf[0][j]), y[0]);
        y[2] = MFMA(frag(hf[j]), frag(cf[1][j]), y[2]);
        SBAR();
        hf[j] = __builtin_amdgcn_raw_buffer_load_b128(rs_ws, vo + (j & 3) * 1024, so1 + (j >> 2) * 4096, H_AUX);
        SBAR();
      }
#pragma unroll
      for (int j = 0; j < 14; ++j) {
        y[1] = MFMA(frag(hf[j]), frag(cf[0][j]), y[1]);
        y[3] = MFMA(frag(hf[j]), frag(cf[1][j]), y[3]);
      }
      MPIN4(y[0], y[1], y[2], y[3]);
      __builtin_amdgcn_s_setprio(AUX3_PRIO);
#pragma unroll
      for (int t = 1; t < 4; ++t) {
#pragma unroll
        for (int g = 0; g < 4; ++g) ldsw128(PA + (t - 1) * 4096 + g * 1024 + vo, f4(y[t], g));
      }
      arrive(C_Y, lane);
      await(C_Y, 4);
      f32x16 own = y[0];
#pragma unroll
      for (int t = 1; t < 4; ++t) {
#pragma unroll
        for (int g = 0; g < 4; ++g) add4(own, g, ldsr128((q ^ t) * L_PRIV + (t - 1) * 4096 + g * 1024 + vo));
      }
      if (valid) {
        float* op = P.out + (size_t)t0 * 896 + d;
#pragma unroll
        for (int i = 0; i < 16; ++i) {
          const u32 pk = __float_as_uint(skip[i >> 1]);
          op[((i & 3) + 8 * (i >> 2)) * 896] = own[i] + __uint_as_float((i & 1) ? (pk & 0xffff0000u) : (pk << 16));
        }
      }
    }
    TS(8);
    TSW();
    if (tb == 31 && sl == 0 && q == 0) {
      await(C_KV, 1);
      if (lane == 0) gstore_flag(ws, WS_EPOCH, tag);
    }
  }
}

extern "C" void kernel_launch(void* const* d_in, const int* in_sizes, int n_in, void* d_out, int out_size, void* d_ws, size_t ws_size,
                              hipStream_t stream) {
  Params P;
  P.Ax = (const u16*)d_in[0]; P.lre = (const u16*)d_in[1]; P.lim = (const u16*)d_in[2]; P.Bre = (const u16*)d_in[3];
  P.Bim = (const u16*)d_in[4]; P.Cre = (const u16*)d_in[5]; P.Cim = (const u16*)d_in[6]; P.ldt = (const u16*)d_in[7];
  P.Dsk = (const u16*)d_in[8]; P.Bx = (const u16*)d_in[9]; P.wq = (const u16*)d_in[10]; P.wk = (const u16*)d_in[11];
  P.wv = (const u16*)d_in[12]; P.wo = (const u16*)d_in[13];
  P.out = (float*)d_out; P.ws = (char*)d_ws;
  (void)hipFuncSetAttribute((const void*)fused_kernel, hipFuncAttributeMaxDynamicSharedMemorySize, LDS_BYTES);
  fused_kernel<<<256, 512, LDS_BYTES, stream>>>(P);
}
```

```cpp
#include <hip/hip_runtime.h>

typedef unsigned short u16;
typedef unsigned int u32;
typedef unsigned long long u64;
using bf16x8 = __attribute__((ext_vector_type(8))) short;
using f32x16 = __attribute__((ext_vector_type(16))) float;
using u32x4 = __attribute__((ext_vector_type(4))) unsigned;
using u32x2 = __attribute__((ext_vector_type(2))) unsigned;
typedef short v4i16_t __attribute__((ext_vector_type(4)));
typedef __bf16 bf16x2_t __attribute__((ext_vector_type(2)));
typedef float f32x2 __attribute__((ext_vector_type(2)));
#define LAS __attribute__((address_space(3)))
#define GAS __attribute__((address_space(1)))
#define DI __device__ __forceinline__
#define MFMA(a, b, c) __builtin_amdgcn_mfma_f32_32x32x16_bf16((a), (b), (c), 0, 0, 0)
#define SBAR() __builtin_amdgcn_sched_barrier(0)
#define PIN(x) asm volatile("" : "+v"(x))
#define PIN2(a, b) asm volatile("" : "+v"(a), "+v"(b))
#define PIN4(a, b, c, d) asm volatile("" : "+v"(a), "+v"(b), "+v"(c), "+v"(d))
#define MPIN(x) asm volatile("s_nop 7\n\ts_nop 7" : "+v"(x))
#define MPIN2(a, b) asm volatile("s_nop 7\n\ts_nop 7" : "+v"(a), "+v"(b))
#define MPIN4(a, b, c, d) asm volatile("s_nop 7\n\ts_nop 7" : "+v"(a), "+v"(b), "+v"(c), "+v"(d))
#define VMCNT(n) asm volatile("s_waitcnt vmcnt(" #n ")" ::: "memory")
#define LGKM0() asm volatile("s_waitcnt lgkmcnt(0)" ::: "memory")
#define EX(x) __builtin_amdgcn_exp2f(x)

#define NOLOAD 0
#define THR 12
#define KV_AUX 16
#define H_AUX 16
#define FENCE 0
#define ABL 0
#define G1_PRIO 0
#define AUX3_PRIO 2
#define QEARLY 1
#define OUT_AUX 2
#define KV_DEPTH 4
#define AUX_PRIO 2
#define AUX2_PRIO 0
#define GATE 0
#define KV_REPL 32

constexpr u32 WS_EPOCH = 0;
constexpr u32 WS_HFLAG = 4096;
constexpr u32 WS_OFLAG = 8192;
constexpr u32 WS_KVFLAG = 12288;
constexpr u32 WS_AGG = 32768;
constexpr u32 WS_KVF = WS_AGG + 131072;
constexpr u32 WS_OF = WS_KVF + 262144;
constexpr u32 WS_HF = WS_OF + 2097152;
constexpr u32 WS_END = WS_HF + 32 * 2 * 56 * 1024;
constexpr u32 WS_DBG = 8u << 20;

constexpr u32 L_XB = 0;
constexpr u32 L_WQ = 57344;
constexpr u32 L_C = 107520;
constexpr u32 L_MISC = L_C + 55296;
constexpr u32 L_AGG = L_MISC + 128;
constexpr u32 L_KN = L_MISC + 896;
constexpr u32 L_PRIV = 26880;
constexpr u32 L_OS = 12288;
constexpr u32 L_QX = L_C + 54 * 864;
constexpr u32 LDS_BYTES = 163840;
enum { C_WQ = 0, C_Q2, C_QDONE, C_BFULL, C_AGG, C_WO, C_O, C_MAX, C_P, C_Y, C_FB, C_KVP, C_KMAX, C_KV, C_OF, C_HF, C_WQ2, C_CREG, C_EPOCH = 31 };

struct Params {
  const u16 *Ax, *lre, *lim, *Bre, *Bim, *Cre, *Cim, *ldt, *Dsk, *Bx, *wq, *wk, *wv, *wo;
  float* out;
  char* ws;
};

DI float bf2f(u16 v) { return __uint_as_float(((u32)v) << 16); }
DI u32 cvtpk(float a, float b) { return __builtin_bit_cast(u32, __builtin_convertvector((f32x2){a, b}, bf16x2_t)); }
DI float rbf(float a) { return __uint_as_float(cvtpk(a, 0.f) << 16); }
DI bf16x8 frag(u32x4 v) { return __builtin_bit_cast(bf16x8, v); }
DI void glds16(const void* sbase, u32 voff, u32 lds_base) {
  asm volatile("s_mov_b32 m0, %2\n\ts_nop 0\n\tglobal_load_lds_dwordx4 %0, %1" :: "v"(voff), "s"(sbase), "s"(lds_base) : "memory");
}
DI u32x4 ldsr128(u32 a) { return *(const LAS u32x4*)(size_t)a; }
DI void ldsw128(u32 a, u32x4 v) { *(LAS u32x4*)(size_t)a = v; }
DI u32 ldsu(u32 a) { return (u32)__builtin_amdgcn_readfirstlane(*(volatile LAS u32*)(size_t)a); }
DI u32x2 ldstr(u32 a) { return __builtin_bit_cast(u32x2, __builtin_amdgcn_ds_read_tr16_b64_v4i16((LAS v4i16_t*)(size_t)a)); }
DI u32x4 trfrag(u32 a0, u32 a1) { u32x2 lo = ldstr(a0), hi = ldstr(a1); return (u32x4){lo.x, lo.y, hi.x, hi.y}; }
DI u32x4 f4(const f32x16& v, int g) {
  return (u32x4){__float_as_uint(v[4 * g]), __float_as_uint(v[4 * g + 1]), __float_as_uint(v[4 * g + 2]), __float_as_uint(v[4 * g + 3])};
}
DI void add4(f32x16& v, int g, u32x4 a) {
  v[4 * g] += __uint_as_float(a.x); v[4 * g + 1] += __uint_as_float(a.y); v[4 * g + 2] += __uint_as_float(a.z); v[4 * g + 3] += __uint_as_float(a.w);
}
DI void arrive(int c, int lane) {
  asm volatile("s_waitcnt lgkmcnt(0)" ::: "memory");
  if (lane == 0) asm volatile("ds_add_u32 %0, %1" :: "v"(L_MISC + 4 * c), "v"(1u) : "memory");
}
DI void await(int c, u32 target) {
#pragma unroll 1
  for (u32 spin = 0; spin < (1u << 22); ++spin) {
    asm volatile("" ::: "memory");
    if (ldsu(L_MISC + 4 * c) >= target) break;
    __builtin_amdgcn_s_sleep(1);
  }
  asm volatile("" ::: "memory");
}
DI float swap32(float v) {
  auto r = __builtin_amdgcn_permlane32_swap(__float_as_uint(v), __float_as_uint(v), false, false);
  return __uint_as_float((threadIdx.x & 32) ? r[0] : r[1]);
}
DI float wave_max_nonneg(float v) {
  u32 x = __float_as_uint(v);
  x = max(x, (u32)__builtin_amdgcn_update_dpp(0, (int)x, 0x111, 0xf, 0xf, false));
  x = max(x, (u32)__builtin_amdgcn_update_dpp(0, (int)x, 0x112, 0xf, 0xf, false));
  x = max(x, (u32)__builtin_amdgcn_update_dpp(0, (int)x, 0x114, 0xf, 0xf, false));
  x = max(x, (u32)__builtin_amdgcn_update_dpp(0, (int)x, 0x118, 0xf, 0xf, false));
  x = max(x, (u32)__builtin_amdgcn_update_dpp(0, (int)x, 0x142, 0xa, 0xf, false));
  x = max(x, (u32)__builtin_amdgcn_update_dpp(0, (int)x, 0x143, 0xc, 0xf, false));
  return __uint_as_float((u32)__builtin_amdgcn_readlane((int)x, 63));
}
DI void gstore_flag(char* ws, u32 off, u32 v) { __hip_atomic_store((GAS u32*)(ws + off), v, __ATOMIC_RELAXED, __HIP_MEMORY_SCOPE_AGENT); }
DI void poll_words(char* ws, u32 off, int n, u32 tag, int lane) {
  const int i = lane < n ? lane : 0;
#pragma unroll 1
  for (u32 spin = 0; spin < (1u << 17); ++spin) {
    asm volatile("" ::: "memory");
    u32 v = __hip_atomic_load((GAS u32*)(ws + off) + i, __ATOMIC_RELAXED, __HIP_MEMORY_SCOPE_AGENT);
    if (__all(v == tag)) break;
    __builtin_amdgcn_s_sleep(1);
  }
  asm volatile("" ::: "memory");
}
DI float poll_kv(char* ws, int rep, u32 tag, int lane) {
  u64 g;
#pragma unroll 1
  for (u32 spin = 0; spin < (1u << 17); ++spin) {
    asm volatile("" ::: "memory");
    g = __hip_atomic_load((GAS u64*)(ws + WS_KVFLAG + rep * 256) + (lane & 31), __ATOMIC_RELAXED, __HIP_MEMORY_SCOPE_AGENT);
    if (__all((u32)g == tag)) break;
    __builtin_amdgcn_s_sleep(1);
  }
  asm volatile("" ::: "memory");
  return __uint_as_float((u32)(g >> 32));
}
DI void cmul(float ar, float ai, float br, float bi, float& cr, float& ci) {
  float x, y;
  asm("v_mul_f32 %0, %2, %4\n\tv_mul_f32 %1, %2, %5\n\tv_fma_f32 %0, -%3, %5, %0\n\tv_fma_f32 %1, %3, %4, %1"
      : "=&v"(x), "=&v"(y) : "v"(ar), "v"(ai), "v"(br), "v"(bi));
  cr = x; ci = y;
}
DI void cstep(float ar, float ai, float& hr, float& hi, float ur, float ui) {
  float x, y;
  asm("v_fma_f32 %0, -%3, %5, %6\n\tv_fma_f32 %1, %3, %4, %7\n\tv_fma_f32 %0, %2, %4, %0\n\tv_fma_f32 %1, %2, %5, %1"
      : "=&v"(x), "=&v"(y) : "v"(ar), "v"(ai), "v"(hr), "v"(hi), "v"(ur), "v"(ui));
  hr = x; hi = y;
}
DI void set_pad(u32x4& f, float m, int h) {
  const float a = rbf(-m), b = rbf(-m - a), c = rbf(-m - a - b);
  if (h) { f.z = cvtpk(a, b); f.w = cvtpk(c, 0.f); }
}

#define TS(i) do {} while (0)
#define TSD() do {} while (0)
#define TSW() do {} while (0)
#define LPRIO 0
#define PUP() do {} while (0)
#define PDN() SBAR()
#define STR_(x) #x
#define VMTHR_(n) asm volatile("s_waitcnt vmcnt(" STR_(n) ")" ::: "memory")
#define VMTHR() VMTHR_(THR)

template <bool W_IS_A, int WROW, bool MID>
DI f32x16 proj_tile(int row, int h, u32 wa, int lane) {
  const int sw = (row >> 1) & 7;
  u32 xa[4];
#pragma unroll
  for (int bb = 0; bb < 4; ++bb) xa[bb] = L_XB + row * 128 + (((2 * bb + h) ^ sw) << 4);
  u32x4 xf[28], wf[28];
  f32x16 acc = {};
#pragma unroll
  for (int ks = 0; ks < 28 + 5; ++ks) {
    if (ks >= 5) {
      const int k = ks - 5;
      acc = W_IS_A ? MFMA(frag(wf[k]), frag(xf[k]), acc) : MFMA(frag(xf[k]), frag(wf[k]), acc);
      SBAR();
    }
    if (ks < 28) {
      if (MID && ks == 14) {
        asm volatile("s_nop 7\n\ts_nop 7\n\ts_waitcnt vmcnt(0)" ::: "memory");
        if (lane == 0) asm volatile("ds_add_u32 %0, %1" :: "v"(L_MISC + 4 * C_WQ2), "v"(1u) : "memory");
        await(C_WQ2, 4);
      }
      xf[ks] = ldsr128(xa[ks & 3] + 8192 * (ks >> 2));
      wf[ks] = trfrag(wa + ks * 16 * WROW, wa + ks * 16 * WROW + 4 * WROW);
      SBAR();
    }
  }
  MPIN(acc);
  return acc;
}

__global__ void __launch_bounds__(512, 2) fused_kernel(Params P) {
  extern __shared__ __attribute__((aligned(16))) char lds_dyn[];
  const int tid = threadIdx.x, lane = tid & 63, wv = __builtin_amdgcn_readfirstlane(tid >> 6);
  const int r = lane & 31, h = lane >> 5;
  const int b = blockIdx.x;
  const int tb = (b & 7) * 4 + (b >> 6), sl = (b >> 3) & 7;
  char* ws = P.ws;
  TSD();
  TS(0);
  {
    const int row = 8 * wv + (lane >> 3);
    const u32 vo = row * 896 + (((lane & 7) ^ ((row >> 1) & 7)) << 4);
    const char* src = (const char*)P.Bx + (size_t)tb * 64 * 896;
#pragma unroll
    for (int c = 0; c < 7; ++c) glds16(src + c * 128, vo, L_XB + c * 8192 + wv * 1024);
  }
  if (tid < 31) *(LAS u32*)(size_t)(L_MISC + 4 * tid) = 0;
  if (tid == 0) asm volatile("s_mov_b32 m0, %2\n\ts_nop 0\n\tglobal_load_lds_dword %0, %1 sc1" :: "v"(0), "s"(ws), "s"(L_MISC + 4 * C_EPOCH) : "memory");
  const auto rs_ws = __builtin_amdgcn_make_buffer_rsrc((void*)ws, 0, WS_END, 0x00020000);
  const auto rs_out = __builtin_amdgcn_make_buffer_rsrc((void*)P.out, 0, 2048 * 896 * 4, 0x00020000);
  const int g1 = (lane >> 4) & 1, q4 = (lane >> 2) & 3, p4 = lane & 3;
  const int l7 = lane / 7;
  const u32 vo7 = l7 * 896 + (lane - 7 * l7) * 16;
  const u32 vo = lane * 16;

  if (wv < 4) {
    const int w = wv;
    const char* wqsrc = (const char*)P.wq + sl * 112 + w * 7 * 7168;
    if (sl != 7) {
      if (lane < 56) {
#pragma unroll
        for (int j = 0; j < 14; ++j) glds16(wqsrc + (j % 7 + 28 * (j / 7)) * 7168, vo7, L_WQ + (w * 7 + j % 7 + 28 * (j / 7)) * 896);
      }
      asm volatile("s_waitcnt vmcnt(14) lgkmcnt(0)\n\ts_barrier" ::: "memory");
    } else {
      asm volatile("s_waitcnt vmcnt(0) lgkmcnt(0)\n\ts_barrier" ::: "memory");
      if (lane < 56) {
#pragma unroll
        for (int j = 0; j < 14; ++j) glds16(wqsrc + (j % 7 + 28 * (j / 7)) * 7168, vo7, L_WQ + (w * 7 + j % 7 + 28 * (j / 7)) * 896);
      }
    }
    TS(13);
    const u32 tag = ldsu(L_MISC + 4 * C_EPOCH) + 1u;
    VMCNT(7);
    TS(14);
    arrive(C_WQ, lane);
    await(C_WQ, 4);
    TS(1);
    {
      f32x16 acc = proj_tile<true, 112, true>(32 * (w & 1) + r, h, L_WQ + (8 * h + q4) * 112 + (w >> 1) * 56 + g1 * 32 + 8 * p4, lane);
      TS(8);
      const float qs = 0.27264060f;
      float n2 = 0.f;
#pragma unroll
      for (int i = 0; i < 16; ++i) { acc[i] *= qs; if (i < 12) n2 = fmaf(acc[i], acc[i], n2); else n2 = h ? n2 : fmaf(acc[i], acc[i], n2); }
      n2 += swap32(n2);
      u32x4 f0 = {cvtpk(acc[0], acc[1]), cvtpk(acc[2], acc[3]), cvtpk(acc[4], acc[5]), cvtpk(acc[6], acc[7])};
      u32x4 f1 = {cvtpk(acc[8], acc[9]), cvtpk(acc[10], acc[11]), cvtpk(acc[12], acc[13]), cvtpk(acc[14], acc[15])};
      if (h) { f1.z = 0; f1.w = __float_as_uint(__builtin_amdgcn_sqrtf(n2)); }
      if (sl == 7) await(C_AGG, 4);
      ldsw128(L_QX + w * 2048 + vo, f0);
      ldsw128(L_QX + w * 2048 + 1024 + vo, f1);
      arrive(C_Q2, lane);
    }
    u32x4 qf[4][2];
    if (QEARLY) {
      await(C_Q2, 4);
#pragma unroll
      for (int t = 0; t < 4; ++t) { const u32 a = L_QX + ((w + t) & 3) * 2048 + vo; qf[t][0] = ldsr128(a); qf[t][1] = ldsr128(a + 1024); }
      arrive(C_QDONE, lane);
    }
    TS(2);
    await(C_KV, 1);
    TS(3);
    const u32 kvbase = WS_KVF + w * 65536;
    const int rot = b >> 3;
#define KVOFF(it) (kvbase + ((((it) + rot) & 15) << 12))
#define LDK(it, s) __builtin_amdgcn_raw_buffer_load_b128(rs_ws, vo + 1024 * (s), KVOFF(it), KV_AUX)
#define LDV(it, s) __builtin_amdgcn_raw_buffer_load_b128(rs_ws, vo + 2048 + 1024 * (s), KVOFF(it), KV_AUX)
    f32x16 o[4];
    for (int attempt = 0; attempt < 2; ++attempt) {
      u32x4 kf[KV_DEPTH][2], vf[KV_DEPTH][2];
#pragma unroll
      for (int d = 0; d < KV_DEPTH; ++d) {
        kf[d][0] = LDK(d, 0); kf[d][1] = LDK(d, 1);
        if (d < KV_DEPTH - 1) { vf[d][0] = LDV(d, 0); vf[d][1] = LDV(d, 1); }
      }
      vf[KV_DEPTH - 1][0] = (u32x4){0, 0, 0, 0}; vf[KV_DEPTH - 1][1] = (u32x4){0, 0, 0, 0};
      SBAR();
      if (attempt == 0) {
        if (!QEARLY) {
        await(C_Q2, 4);
#pragma unroll
        for (int t = 0; t < 4; ++t) { const u32 a = L_QX + ((w + t) & 3) * 2048 + vo; qf[t][0] = ldsr128(a); qf[t][1] = ldsr128(a + 1024); }
        arrive(C_QDONE, lane);
        }
        const float kmax = __builtin_amdgcn_sqrtf(wave_max_nonneg(*(LAS float*)(size_t)(L_KN + 4 * r)));
#pragma unroll
        for (int t = 0; t < 4; ++t) set_pad(qf[t][1], __uint_as_float(qf[t][1].w) * kmax, h);
      }
#pragma unroll
      for (int t = 0; t < 4; ++t) { o[t] = (f32x16){}; PIN(o[t]); PIN(qf[t][0]); PIN(qf[t][1]); }
      SBAR();
      const f32x16 zero = {};
      f32x16 sc = MFMA(frag(kf[0][0]), frag(qf[0][0]), zero);
      sc = MFMA(frag(kf[0][1]), frag(qf[0][1]), sc);
      TS(9);
      float e0 = 0.f, e1 = 0.f, e2 = 0.f, e3 = 0.f;
      u32x4 p1 = {0, 0, 0, 0};
      for (int it0 = 0; it0 < 16; it0 += KV_DEPTH) {
#pragma unroll
        for (int d = 0; d < KV_DEPTH; ++d) {
#pragma unroll
          for (int qi = 0; qi < 4; ++qi) {
            const int dn = qi == 3 ? (d + 1) % KV_DEPTH : d, qn = (qi + 1) & 3;
            const int dp = qi == 0 ? (d + KV_DEPTH - 1) % KV_DEPTH : d, qp = (qi + 3) & 3;
            f32x16 sn; u32x4 p0;
            float c0, c1, c2, c3;
            sn = MFMA(frag(kf[dn][0]), frag(qf[qn][0]), zero); SBAR();
            p1.z = cvtpk(e0, e1); p1.w = cvtpk(e2, e3); PIN(p1); SBAR();
            c0 = EX(sc[0]); c1 = EX(sc[1]); c2 = EX(sc[2]); c3 = EX(sc[3]); PIN4(c0, c1, c2, c3); SBAR();
            o[qp] = MFMA(frag(vf[dp][1]), frag(p1), o[qp]); SBAR();
            p0.x = cvtpk(c0, c1); p0.y = cvtpk(c2, c3); PIN2(p0.x, p0.y); SBAR();
            c0 = EX(sc[4]); c1 = EX(sc[5]);
            if (!NOLOAD && qi == 0) { PIN2(c0, c1); SBAR(); PUP(); vf[dp][0] = LDV(it0 + d - 1 + KV_DEPTH, 0); PDN(); }
            if (!NOLOAD && qi == 1) { PIN2(c0, c1); SBAR(); const int dq = (d + KV_DEPTH - 1) % KV_DEPTH; PUP(); vf[dq][1] = LDV(it0 + d - 1 + KV_DEPTH, 1); PDN(); }
            c2 = EX(sc[6]); c3 = EX(sc[7]); PIN4(c0, c1, c2, c3); SBAR();
            sn = MFMA(frag(kf[dn][1]), frag(qf[qn][1]), sn); SBAR();
            p0.z = cvtpk(c0, c1); p0.w = cvtpk(c2, c3); PIN(p0); SBAR();
            c0 = EX(sc[8]); c1 = EX(sc[9]);
            if (!NOLOAD && qi == 2) { PIN2(c0, c1); SBAR(); PUP(); kf[d][0] = LDK(it0 + d + KV_DEPTH, 0); PDN(); }
            c2 = EX(sc[10]); c3 = EX(sc[11]); PIN4(c0, c1, c2, c3); SBAR();
            o[qi] = MFMA(frag(vf[d][0]), frag(p0), o[qi]); SBAR();
            p1.x = cvtpk(c0, c1); p1.y = cvtpk(c2, c3); PIN2(p1.x, p1.y); SBAR();
            e0 = EX(sc[12]); e1 = EX(sc[13]);
            if (!NOLOAD && qi == 2) { PIN2(e0, e1); SBAR(); PUP(); kf[d][1] = LDK(it0 + d + KV_DEPTH, 1); PDN(); }
            e2 = EX(sc[14]); e3 = EX(sc[15]); PIN4(e0, e1, e2, e3); SBAR();
            sc = sn;
          }
        }
      }
      p1.z = cvtpk(e0, e1); p1.w = cvtpk(e2, e3);
      o[3] = MFMA(frag(vf[KV_DEPTH - 1][1]), frag(p1), o[3]);
      MPIN4(o[0], o[1], o[2], o[3]);
      TS(4);
      if (attempt == 0) {
        bool bad = false;
#pragma unroll
        for (int t = 0; t < 4; ++t) bad |= !(o[t][12] >= 0x1p-80f && o[t][12] <= 0x1p100f);
        if (__any(bad && h)) { if (lane == 0) *(LAS u32*)(size_t)(L_MISC + 4 * C_FB) = 1u; }
        await(C_CREG, 4);
      }
#pragma unroll
      for (int t = 1; t < 4; ++t) {
#pragma unroll
        for (int g = 0; g < 4; ++g) ldsw128(w * L_PRIV + L_OS + (t - 1) * 4096 + g * 1024 + vo, f4(o[t], g));
      }
      arrive(C_O, lane);
      await(C_O, 4 * (attempt + 1));
      TS(10);
      const u32 fb = attempt == 0 ? ldsu(L_MISC + 4 * C_FB) : 0u;
      if (!fb) break;
      {
        float mx[4];
#pragma unroll
        for (int t = 0; t < 4; ++t) { mx[t] = -3.0e38f; if (h) { qf[t][1].z = 0; qf[t][1].w = 0; } }
#pragma unroll 1
        for (int it = 0; it < 16; ++it) {
          const u32x4 k0 = LDK(it, 0), k1 = LDK(it, 1);
#pragma unroll
          for (int t = 0; t < 4; ++t) {
            f32x16 s = MFMA(frag(k0), frag(qf[t][0]), zero);
            s = MFMA(frag(k1), frag(qf[t][1]), s);
#pragma unroll
            for (int i = 0; i < 16; ++i) mx[t] = fmaxf(mx[t], s[i]);
          }
        }
        const u32 ma = L_C + 51200;
#pragma unroll
        for (int t = 0; t < 4; ++t) { mx[t] = fmaxf(mx[t], swap32(mx[t])); *(LAS float*)(size_t)(ma + (w * 4 + ((w + t) & 3)) * 256 + lane * 4) = mx[t]; }
        arrive(C_MAX, lane);
        await(C_MAX, 4);
#pragma unroll
        for (int t = 0; t < 4; ++t) {
          float m = -3.0e38f;
#pragma unroll
          for (int ww = 0; ww < 4; ++ww) m = fmaxf(m, *(LAS float*)(size_t)(ma + (ww * 4 + ((w + t) & 3)) * 256 + lane * 4));
          set_pad(qf[t][1], m, h);
        }
      }
    }
    {
      f32x16 own = o[0];
#pragma unroll
      for (int pp = 1; pp < 4; ++pp) {
        const u32 a = ((w + pp) & 3) * L_PRIV + L_OS + (3 - pp) * 4096 + vo;
#pragma unroll
        for (int g = 0; g < 4; ++g) add4(own, g, ldsr128(a + g * 1024));
      }
      const float other = swap32(own[12]);
      const float inv = __builtin_amdgcn_rcpf(h ? own[12] : other);
      u32x4 f0 = {cvtpk(own[0] * inv, own[1] * inv), cvtpk(own[2] * inv, own[3] * inv), cvtpk(own[4] * inv, own[5] * inv), cvtpk(own[6] * inv, own[7] * inv)};
      u32x4 f1 = {cvtpk(own[8] * inv, own[9] * inv), cvtpk(own[10] * inv, own[11] * inv), cvtpk(own[12] * inv, own[13] * inv), cvtpk(own[14] * inv, own[15] * inv)};
      if (h) { f1.z = 0; f1.w = 0; }
      const u32 so = WS_OF + ((tb * 2 + (w & 1)) * 16 + 2 * sl + (w >> 1)) * 2048 + vo;
      __builtin_amdgcn_raw_buffer_store_b128(f0, rs_ws, so, 0, 16);
      __builtin_amdgcn_raw_buffer_store_b128(f1, rs_ws, so + 1024, 0, 16);
    }
    {
      const int tm = w & 1, kh = w >> 1;
      TS(11);
      VMCNT(0);
      if (lane == 0) gstore_flag(ws, WS_OFLAG + (tb * 32 + sl * 4 + w) * 4, tag);
      TS(5);
      await(C_WO, 4);
      u32x4 wf[16][2];
      const u32 wa = L_C + (224 * kh + 4 * h + q4) * 112 + g1 * 32 + 8 * p4;
      const u32 wa0 = wa + 64 * kh, wa1 = wa + 64 * (1 - kh);
#pragma unroll
      for (int i = 0; i < 16; ++i) {
        const u32 a = (28 * (i >> 1) + 16 * (i & 1)) * 112;
        wf[i][0] = trfrag(wa0 + a, wa0 + a + 896); wf[i][1] = trfrag(wa1 + a, wa1 + a + 896);
      }
#pragma unroll
      for (int i = 0; i < 16; ++i) PIN2(wf[i][0], wf[i][1]);
      if (w == 0) { poll_words(ws, WS_OFLAG + tb * 128, 32, tag, lane); arrive(C_OF, lane); }
      else await(C_OF, 1);
      TS(6);
      u32x4 of[16];
      const u32 so = WS_OF + ((tb * 2 + tm) * 16 + 8 * kh) * 2048;
#pragma unroll
      for (int i = 0; i < 16; ++i) of[i] = __builtin_amdgcn_raw_buffer_load_b128(rs_ws, vo + (i & 3) * 1024, so + (i >> 2) * 4096, H_AUX);
      SBAR();
      f32x16 a0 = {}, a1 = {};
#pragma unroll
      for (int i = 0; i < 16; ++i) {
        a0 = MFMA(frag(of[i]), frag(wf[i][0]), a0);
        a1 = MFMA(frag(of[i]), frag(wf[i][1]), a1);
      }
      MPIN2(a0, a1);
      TS(12);
      const f32x16 snd = a1;
      f32x16 own = a0;
      const u32 aw = w * L_PRIV + L_OS + vo, ar = (w ^ 2) * L_PRIV + L_OS + vo;
#pragma unroll
      for (int g = 0; g < 4; ++g) ldsw128(aw + g * 1024, f4(snd, g));
      arrive(C_P, lane);
      await(C_P, 4);
#pragma unroll
      for (int g = 0; g < 4; ++g) add4(own, g, ldsr128(ar + g * 1024));
      if (32 * kh + r < 56) {
        const u32 ov = ((tb * 64 + 32 * tm + 4 * h) * 896 + 448 + 56 * sl + 32 * kh + r) * 4;
#pragma unroll
        for (int i = 0; i < 16; ++i) __builtin_amdgcn_raw_buffer_store_b32(__float_as_uint(rbf(own[i])), rs_out, ov, ((i & 3) + 8 * (i >> 2)) * 3584, OUT_AUX);
      }
    }
    TS(7);
    TSW();
  } else {
    __builtin_amdgcn_s_setprio(AUX_PRIO);
    const int q = wv - 4;
    const u32 PA = q * L_PRIV;
    const int part = q >> 1, half = q & 1;
    u32 tag;
    auto stage_C = [&]() {
      if (!(ABL & 2) && lane < 56) {
        const char* src = (const char*)(part ? P.Cim : P.Cre) + (size_t)sl * 56 * 896 + half * 448;
        const int hi = lane >= 28, cs = lane - 28 * hi;
        u32 cvo[4];
#pragma unroll
        for (int t = 0; t < 4; ++t) cvo[t] = hi * 896 + ((cs ^ t) << 4);
#pragma unroll
        for (int i = 0; i < 28; ++i) { glds16(src + i * 1792, cvo[(i >> 1) & 3], PA + i * 896); VMTHR(); }
      }
    };
    auto stage_wo = [&]() {
      if (!(ABL & 4) && lane < 56) {
        const char* src = (const char*)P.wo + sl * 112 + q * 14 * 7168;
#pragma unroll
        for (int i = 0; i < 14; ++i) { glds16(src + i * 7168, vo7, L_C + (q * 14 + i) * 896); VMTHR(); }
        if (q == 3) glds16(src, vo7, L_C + 56 * 896);
      }
    };
    auto kv_wait = [&]() {
      const float kn = poll_kv(ws, (b >> 3) % KV_REPL, tag, lane);
      if (FENCE) { __builtin_amdgcn_fence(__ATOMIC_ACQUIRE, "agent"); VMCNT(0); }
      if (lane < 32) *(LAS float*)(size_t)(L_KN + 4 * lane) = kn;
      arrive(C_KV, lane);
    };
    u32x4 cf[2][14];
    auto read_C = [&]() {
#pragma unroll
      for (int tn = 0; tn < 2; ++tn) {
        int row = 32 * (tn ^ part) + r; row = row > 55 ? 55 : row;
        const int t = (row >> 2) & 3;
        const u32 a0 = PA + row * 448 + ((h ^ t) << 4), a1 = PA + row * 448 + (((2 + h) ^ t) << 4);
#pragma unroll
        for (int j = 0; j < 14; ++j) cf[tn][j] = ldsr128(((j & 1) ? a1 : a0) + 64 * (j >> 1));
      }
    };
    if (sl == 7) {
      const int kind = q >> 1, tm = q & 1;
      if (lane < 56) {
        const char* src = (const char*)(kind ? P.wv : P.wk) + tm * 14 * 896;
#pragma unroll
        for (int j = 0; j < 14; ++j) glds16(src + j * 896, vo, L_C + (q * 14 + j) * 896);
      }
      asm volatile("s_waitcnt vmcnt(0) lgkmcnt(0)\n\ts_barrier" ::: "memory");
      TS(1);
      const u32 wa = L_C + kind * 25088 + (8 * h + q4) * 56 + g1 * 32 + 8 * p4;
      f32x16 acc;
      if (kind == 0) acc = proj_tile<true, 56, false>(32 * tm + r, h, wa, lane);
      else acc = proj_tile<false, 56, false>(32 * tm + r, h, wa, lane);
      TS(3);
      u32x4 f0 = {cvtpk(acc[0], acc[1]), cvtpk(acc[2], acc[3]), cvtpk(acc[4], acc[5]), cvtpk(acc[6], acc[7])};
      u32x4 f1 = {cvtpk(acc[8], acc[9]), cvtpk(acc[10], acc[11]), cvtpk(acc[12], acc[13]), cvtpk(acc[14], acc[15])};
      if (kind == 0) {
        if (h) { f1.z = 0x3F803F80u; f1.w = 0x00003F80u; }
      } else if (r >= 28) {
        const u32 c = r == 28 ? 0x3F803F80u : 0u;
        f0 = (u32x4){c, c, c, c}; f1 = f0;
      }
      const u32 so = WS_KVF + (tb * 2 + tm) * 4096 + kind * 2048 + vo;
      __builtin_amdgcn_raw_buffer_store_b128(f0, rs_ws, so, 0, 16);
      __builtin_amdgcn_raw_buffer_store_b128(f1, rs_ws, so + 1024, 0, 16);
      SBAR();
      tag = ldsu(L_MISC + 4 * C_EPOCH) + 1u;
      if (kind == 0) {
        float n2 = 0.f;
#pragma unroll
        for (int i = 0; i < 16; ++i) { if (i < 12) n2 = fmaf(acc[i], acc[i], n2); else n2 = h ? n2 : fmaf(acc[i], acc[i], n2); }
        n2 += swap32(n2);
        n2 = wave_max_nonneg(n2);
        if (lane == 0) asm volatile("ds_max_u32 %0, %1" :: "v"(L_MISC + 4 * C_KMAX), "v"(n2) : "memory");
      }
      TS(4);
      VMCNT(0);
      TS(5);
      u32 old = 0;
      if (lane == 0) asm volatile("s_waitcnt lgkmcnt(0)\n\tds_add_rtn_u32 %0, %1, %2\n\ts_waitcnt lgkmcnt(0)" : "=&v"(old) : "v"(L_MISC + 4 * C_KVP), "v"(1u) : "memory");
      if (__builtin_amdgcn_readfirstlane(old) == 3) {
        const u32 km = ldsu(L_MISC + 4 * C_KMAX);
        if (lane < KV_REPL) __hip_atomic_store((GAS u64*)(ws + WS_KVFLAG + lane * 256 + tb * 8), (u64)tag | ((u64)km << 32), __ATOMIC_RELAXED, __HIP_MEMORY_SCOPE_AGENT);
      }
      TS(2);
      arrive(C_AGG, lane);
      if (q == 0) kv_wait();
      await(C_AGG, 4);
      await(C_QDONE, 4);
      stage_wo();
    } else {
      const int tb2 = tb >> 1, cg = (tb & 1) * 7 + sl, ch = 32 * cg + r;
      u32 vlre = P.lre[ch], vlim = P.lim[ch], vldt = P.ldt[ch];
      u32x4 blast_re = *(const u32x4*)(P.Bre + (size_t)ch * 448 + 432 + 8 * h);
      u32x4 blast_im = *(const u32x4*)(P.Bim + (size_t)ch * 448 + 432 + 8 * h);
      asm volatile("s_waitcnt vmcnt(0) lgkmcnt(0)\n\ts_barrier" ::: "memory");
      asm volatile("" : "+v"(vlre), "+v"(vlim), "+v"(vldt), "+v"(blast_re), "+v"(blast_im) :: "memory");
      await(C_WQ, 4);
      const char* bsrc = (const char*)(part ? P.Bim : P.Bre) + (size_t)(cg * 32 + half * 16) * 896;
      const u32 vo1 = (lane ^ 1) * 16;
      if (lane < 54) {
#pragma unroll
        for (int i = 0; i < 16; ++i)
          if (i < 6 || q != 3) glds16(bsrc + i * 896, i < 8 ? vo : vo1, L_C + (q * 16 + i) * 864);
      }
      tag = ldsu(L_MISC + 4 * C_EPOCH) + 1u;
      float lr, li, cr, ci;
      {
        const float dt = rbf(expf(__uint_as_float(vldt << 16))), a = __uint_as_float(vlre << 16), bb = __uint_as_float(vlim << 16);
        const float er = expf(dt * a);
        float sn, cs;
        sincosf(dt * bb, &sn, &cs);
        lr = er * cs; li = er * sn;
        const float nr = lr - 1.f, ni = li, den = 2.0f / (a * a + bb * bb);
        cr = (nr * a + ni * bb) * den; ci = (ni * a - nr * bb) * den;
      }
      float l16r = lr, l16i = li;
#pragma unroll
      for (int i = 0; i < 4; ++i) cmul(l16r, l16i, l16r, l16i, l16r, l16i);
      float l32r, l32i, l128r, l128i;
      cmul(l16r, l16i, l16r, l16i, l32r, l32i);
      cmul(l32r, l32i, l32r, l32i, l128r, l128i);
      cmul(l128r, l128i, l128r, l128i, l128r, l128i);
      PIN4(lr, li, cr, ci); PIN4(l16r, l16i, l32r, l32i); PIN2(l128r, l128i);
      const char* xsrc = (const char*)P.Ax + (size_t)(tb2 * 128 + q * 32) * 896;
      const u32 xv0 = (lane >> 3) * 896 + (((lane & 7) ^ (lane >> 4)) << 4), xv1 = xv0 ^ 64;
      const int rho = 16 * ((r >> 2) & 1) + (r & 3) + 4 * (r >> 3), sw = (rho >> 1) & 7;
      u32 xa[4];
#pragma unroll
      for (int bb = 0; bb < 4; ++bb) xa[bb] = PA + rho * 128 + (((2 * bb + h) ^ sw) << 4);
      const u32 ba = L_C + r * 864 + ((h ^ ((r >> 3) & 1)) << 4);
      PIN4(xa[0], xa[1], xa[2], xa[3]);
      TS(1);
      if (q == 0) kv_wait();
      await(C_QDONE, 4);
      if (q == 3 && lane < 54) {
#pragma unroll
        for (int i = 6; i < 16; ++i) glds16(bsrc + i * 896, i < 8 ? vo : vo1, L_C + (48 + i) * 864);
      }
#pragma unroll
      for (int c = 0; c < 6; ++c)
#pragma unroll
        for (int j = 0; j < 4; ++j) glds16(xsrc + c * 128 + j * 7168, (j & 1) ? xv1 : xv0, PA + c * 4096 + j * 1024);
      VMCNT(24);
      arrive(C_BFULL, lane);
      await(C_BFULL, 4);
      TS(2);
      f32x16 ur = {}, ui = {};
      PIN2(ur, ui);
      __builtin_amdgcn_s_setprio(G1_PRIO);
      {
        u32x4 xf[28], br[28], bi[28];
#pragma unroll
        for (int ks = 0; ks < 28 + 4; ++ks) {
          if (ks >= 4) {
            const int k = ks - 4;
            ur = MFMA(frag(xf[k]), frag(br[k]), ur);
            ui = MFMA(frag(xf[k]), frag(bi[k]), ui);
            SBAR();
            if (k == 3) {
#pragma unroll
              for (int j = 0; j < 4; ++j) glds16(xsrc + 6 * 128 + j * 7168, (j & 1) ? xv1 : xv0, PA + j * 1024);
            }
          }
          if (ks < 28) {
            if (ks == 0) VMCNT(20); else if (ks == 4) VMCNT(16); else if (ks == 8) VMCNT(16); else if (ks == 12) VMCNT(12);
            else if (ks == 16) VMCNT(8); else if (ks == 20) VMCNT(4); else if (ks == 24) VMCNT(0);
            xf[ks] = ldsr128(xa[ks & 3] + ((ks >> 2) % 6) * 4096);
            br[ks] = ks < 27 ? ldsr128(ba + 32 * ks) : blast_re;
            bi[ks] = ks < 27 ? ldsr128(ba + 27648 + 32 * ks) : blast_im;
            SBAR();
          }
        }
      }
      MPIN2(ur, ui);
      __builtin_amdgcn_s_setprio(AUX_PRIO);
      TS(3);
      float ar = 0.f, ai = 0.f;
#pragma unroll
      for (int i = 0; i < 16; ++i) {
        float tr, ti;
        cmul(cr, ci, ur[i], ui[i], tr, ti);
        ur[i] = tr; ui[i] = ti;
        cstep(lr, li, ar, ai, tr, ti);
      }
      const float or_ = swap32(ar), oi_ = swap32(ai);
      const float lor = h ? or_ : ar, loi = h ? oi_ : ai;
      float a32r = lor, a32i = loi;
      cstep(l16r, l16i, a32r, a32i, h ? ar : or_, h ? ai : oi_);
      if (q < 3 && h == 0) *(LAS f32x2*)(size_t)(L_AGG + q * 256 + r * 8) = (f32x2){a32r, a32i};
      arrive(C_AGG, lane);
      await(C_AGG, 4);
      f32x2 wagg[3];
#pragma unroll
      for (int j = 0; j < 3; ++j) wagg[j] = *(LAS f32x2*)(size_t)(L_AGG + j * 256 + r * 8);
      const u32 aggb = WS_AGG + cg * 8192;
      if (q == 3 && tb2 < 15) {
        float tr = wagg[0].x, ti = wagg[0].y;
        cstep(l32r, l32i, tr, ti, wagg[1].x, wagg[1].y);
        cstep(l32r, l32i, tr, ti, wagg[2].x, wagg[2].y);
        cstep(l32r, l32i, tr, ti, a32r, a32i);
        __hip_atomic_store((GAS u64*)(ws + aggb + tb2 * 512 + r * 16 + h * 8), (u64)__float_as_uint(h ? ti : tr) | ((u64)tag << 32), __ATOMIC_RELAXED, __HIP_MEMORY_SCOPE_AGENT);
      }
      stage_wo();
      TS(4);
      float hr = 0.f, hi = 0.f;
      if (!(ABL & 1) && tb2 > 0) {
        u32x4 g[8];
        u32 gv[8];
#pragma unroll
        for (int j = 0; j < 8; ++j) gv[j] = r * 16 + ((8 * h + j < tb2) ? (8 * h + j) * 512 : 0);
#pragma unroll 1
        for (u32 spin = 0; spin < (1u << 17); ++spin) {
          asm volatile("" ::: "memory");
          u32 bad = 0;
#pragma unroll
          for (int j = 0; j < 8; ++j) g[j] = __builtin_amdgcn_raw_buffer_load_b128(rs_ws, gv[j], aggb, 16);
#pragma unroll
          for (int j = 0; j < 8; ++j) bad |= (g[j].y ^ tag) | (g[j].w ^ tag);
          if (__all(bad == 0)) break;
          __builtin_amdgcn_s_sleep(4);
        }
        asm volatile("" ::: "memory");
#pragma unroll
        for (int j = 0; j < 8; ++j)
          if (j < tb2) cstep(l128r, l128i, hr, hi, __uint_as_float(g[j].x), __uint_as_float(g[j].z));
        const float sr = swap32(hr), si = swap32(hi);
        if (h) { hr = sr; hi = si; }
        if (tb2 > 8) {
#pragma unroll
          for (int j = 0; j < 7; ++j)
            if (8 + j < tb2) cstep(l128r, l128i, hr, hi, __uint_as_float(g[j].x), __uint_as_float(g[j].z));
          const float tr = swap32(hr), ti = swap32(hi);
          if (!h) { hr = tr; hi = ti; }
        }
      }
#pragma unroll
      for (int j = 0; j < 3; ++j)
        if (j < q) cstep(l32r, l32i, hr, hi, wagg[j].x, wagg[j].y);
      if (h) cstep(l16r, l16i, hr, hi, lor, loi);
      TS(5);
      u32 pr[8], pi[8];
#pragma unroll
      for (int i = 0; i < 16; i += 2) {
        cstep(lr, li, hr, hi, ur[i], ui[i]);
        const float r0 = hr, i0 = hi;
        cstep(lr, li, hr, hi, ur[i + 1], ui[i + 1]);
        pr[i >> 1] = cvtpk(r0, hr); pi[i >> 1] = cvtpk(-i0, -hi);
      }
      const u32 ta = PA + r * 80 + 32 * h;
      ldsw128(ta, (u32x4){pr[0], pr[1], pr[2], pr[3]}); ldsw128(ta + 16, (u32x4){pr[4], pr[5], pr[6], pr[7]});
      ldsw128(ta + 2560, (u32x4){pi[0], pi[1], pi[2], pi[3]}); ldsw128(ta + 2560 + 16, (u32x4){pi[4], pi[5], pi[6], pi[7]});
      LGKM0();
      const u32 tr = PA + (8 * h + q4) * 80 + g1 * 32 + 8 * p4;
      const int tbh = 2 * tb2 + (q >> 1);
      const u32 so = WS_HF + ((tbh * 2 + (q & 1)) * 56 + 2 * cg) * 1024 + vo;
#pragma unroll
      for (int pt = 0; pt < 2; ++pt)
#pragma unroll
        for (int s = 0; s < 2; ++s) {
          const u32 a = tr + pt * 2560 + s * 1280;
          if (!(ABL & 16)) __builtin_amdgcn_raw_buffer_store_b128(trfrag(a, a + 320), rs_ws, so + pt * 28 * 1024 + s * 1024, 0, 16);
        }
      VMCNT(0);
      if (lane == 0) gstore_flag(ws, WS_HFLAG + (tbh * 32 + cg * 2 + (q & 1)) * 4, tag);
      TS(6);
    }
    VMCNT(0);
    arrive(C_WO, lane);
    stage_C();
    const int tm = q & 1, tn = q >> 1;
    int col = 32 * tn + r; const bool valid = col < 56; col = valid ? col : 55;
    const int d = 56 * sl + col, t0 = tb * 64 + 32 * tm + 4 * h;
    float skip[16];
    {
      u16 xs[16];
#pragma unroll
      for (int i = 0; i < 16; ++i) xs[i] = (ABL & 32) ? (u16)i : P.Ax[(size_t)(t0 + (i & 3) + 8 * (i >> 2)) * 448 + d];
      const float dsk = bf2f(P.Dsk[d]);
#pragma unroll
      for (int i = 0; i < 16; i += 2) { float a = bf2f(xs[i]) * dsk, c = bf2f(xs[i + 1]) * dsk; PIN(a); skip[i >> 1] = __uint_as_float(cvtpk(a, c)); }
#pragma unroll
      for (int i = 0; i < 8; ++i) PIN(skip[i]);
    }
    VMCNT(0);
    read_C();
#pragma unroll
    for (int j = 0; j < 14; ++j) PIN2(cf[0][j], cf[1][j]);
    arrive(C_CREG, lane);
    TS(9);
    if (ABL & 8) { TSW(); if (tb == 31 && sl == 0 && q == 0) { for (int i = 0; i < 1000; ++i) __builtin_amdgcn_s_sleep(1); if (lane == 0) gstore_flag(ws, WS_EPOCH, tag); } return; }
    if (q == 0) { poll_words(ws, WS_HFLAG + tb * 128, 28, tag, lane); arrive(C_HF, lane); }
    else await(C_HF, 1);
    TS(7);
    {
      f32x16 y[4];
      const u32 so = WS_HF + ((tb * 2 + tm) * 56 + part * 28 + half * 14) * 1024, so1 = WS_HF + ((tb * 2 + (tm ^ 1)) * 56 + part * 28 + half * 14) * 1024;
      u32x4 hf[14];
#pragma unroll
      for (int j = 0; j < 14; ++j) hf[j] = __builtin_amdgcn_raw_buffer_load_b128(rs_ws, vo + (j & 3) * 1024, so + (j >> 2) * 4096, H_AUX);
      SBAR();
#pragma unroll
      for (int t = 0; t < 4; ++t) y[t] = (f32x16){};
      PIN4(y[0], y[1], y[2], y[3]);
      __builtin_amdgcn_s_setprio(AUX2_PRIO);
#pragma unroll
      for (int j = 0; j < 14; ++j) {
        y[0] = MFMA(frag(hf[j]), frag(cf[0][j]), y[0]);
        y[2] = MFMA(frag(hf[j]), frag(cf[1][j]), y[2]);
        SBAR();
        hf[j] = __builtin_amdgcn_raw_buffer_load_b128(rs_ws, vo + (j & 3) * 1024, so1 + (j >> 2) * 4096, H_AUX);
        SBAR();
      }
#pragma unroll
      for (int j = 0; j < 14; ++j) {
        y[1] = MFMA(frag(hf[j]), frag(cf[0][j]), y[1]);
        y[3] = MFMA(frag(hf[j]), frag(cf[1][j]), y[3]);
      }
      MPIN4(y[0], y[1], y[2], y[3]);
      __builtin_amdgcn_s_setprio(AUX3_PRIO);
#pragma unroll
      for (int t = 1; t < 4; ++t) {
#pragma unroll
        for (int g = 0; g < 4; ++g) ldsw128(PA + (t - 1) * 4096 + g * 1024 + vo, f4(y[t], g));
      }
      arrive(C_Y, lane);
      await(C_Y, 4);
      f32x16 own = y[0];
#pragma unroll
      for (int t = 1; t < 4; ++t) {
#pragma unroll
        for (int g = 0; g < 4; ++g) add4(own, g, ldsr128((q ^ t) * L_PRIV + (t - 1) * 4096 + g * 1024 + vo));
      }
      if (valid) {
        const u32 ov = (t0 * 896 + d) * 4;
#pragma unroll
        for (int i = 0; i < 16; ++i) {
          const u32 pk = __float_as_uint(skip[i >> 1]);
          __builtin_amdgcn_raw_buffer_store_b32(__float_as_uint(own[i] + __uint_as_float((i & 1) ? (pk & 0xffff0000u) : (pk << 16))), rs_out, ov, ((i & 3) + 8 * (i >> 2)) * 3584, OUT_AUX);
        }
      }
    }
    TS(8);
    TSW();
    if (tb == 31 && sl == 0 && q == 0) {
      await(C_KV, 1);
      if (lane == 0) gstore_flag(ws, WS_EPOCH, tag);
    }
  }
}

extern "C" void kernel_launch(void* const* d_in, const int* in_sizes, int n_in, void* d_out, int out_size, void* d_ws, size_t ws_size,
                              hipStream_t stream) {
  Params P;
  P.Ax = (const u16*)d_in[0]; P.lre = (const u16*)d_in[1]; P.lim = (const u16*)d_in[2]; P.Bre = (const u16*)d_in[3];
  P.Bim = (const u16*)d_in[4]; P.Cre = (const u16*)d_in[5]; P.Cim = (const u16*)d_in[6]; P.ldt = (const u16*)d_in[7];
  P.Dsk = (const u16*)d_in[8]; P.Bx = (const u16*)d_in[9]; P.wq = (const u16*)d_in[10]; P.wk = (const u16*)d_in[11];
  P.wv = (const u16*)d_in[12]; P.wo = (const u16*)d_in[13];
  P.out = (float*)d_out; P.ws = (char*)d_ws;
  (void)hipFuncSetAttribute((const void*)fused_kernel, hipFuncAttributeMaxDynamicSharedMemorySize, LDS_BYTES);
  fused_kernel<<<256, 512, LDS_BYTES, stream>>>(P);
}
```

```cpp
#define PROBE 13
#include <hip/hip_runtime.h>

typedef unsigned short u16;
typedef unsigned int u32;
typedef unsigned long long u64;
using bf16x8 = __attribute__((ext_vector_type(8))) short;
using f32x16 = __attribute__((ext_vector_type(16))) float;
using u32x4 = __attribute__((ext_vector_type(4))) unsigned;
using u32x2 = __attribute__((ext_vector_type(2))) unsigned;
typedef short v4i16_t __attribute__((ext_vector_type(4)));
typedef __bf16 bf16x2_t __attribute__((ext_vector_type(2)));
typedef float f32x2 __attribute__((ext_vector_type(2)));
#define LAS __attribute__((address_space(3)))
#define GAS __attribute__((address_space(1)))
#define DI __device__ __forceinline__
#define MFMA(a, b, c) __builtin_amdgcn_mfma_f32_32x32x16_bf16((a), (b), (c), 0, 0, 0)
#define SBAR() __builtin_amdgcn_sched_barrier(0)
#define PIN(x) asm volatile("" : "+v"(x))
#define PIN2(a, b) asm volatile("" : "+v"(a), "+v"(b))
#define PIN4(a, b, c, d) asm volatile("" : "+v"(a), "+v"(b), "+v"(c), "+v"(d))
#define MPIN(x) asm volatile("s_nop 7\n\ts_nop 7" : "+v"(x))
#define MPIN2(a, b) asm volatile("s_nop 7\n\ts_nop 7" : "+v"(a), "+v"(b))
#define MPIN4(a, b, c, d) asm volatile("s_nop 7\n\ts_nop 7" : "+v"(a), "+v"(b), "+v"(c), "+v"(d))
#define VMCNT(n) asm volatile("s_waitcnt vmcnt(" #n ")" ::: "memory")
#define LGKM0() asm volatile("s_waitcnt lgkmcnt(0)" ::: "memory")
#define EX(x) __builtin_amdgcn_exp2f(x)

#define NOLOAD 0
#define THR 12
#define KV_AUX 16
#define H_AUX 16
#define FENCE 0
#define ABL 0
#define G1_PRIO 0
#define AUX3_PRIO 2
#define QEARLY 1
#define OUT_AUX 17
#define KV_DEPTH 4
#define AUX_PRIO 2
#define AUX2_PRIO 0
#define GATE 0
#define KV_REPL 32

constexpr u32 WS_EPOCH = 0;
constexpr u32 WS_HFLAG = 4096;
constexpr u32 WS_OFLAG = 8192;
constexpr u32 WS_KVFLAG = 12288;
constexpr u32 WS_AGG = 32768;
constexpr u32 WS_KVF = WS_AGG + 131072;
constexpr u32 WS_OF = WS_KVF + 262144;
constexpr u32 WS_HF = WS_OF + 2097152;
constexpr u32 WS_END = WS_HF + 32 * 2 * 56 * 1024;
constexpr u32 WS_DBG = 8u << 20;

constexpr u32 L_XB = 0;
constexpr u32 L_WQ = 57344;
constexpr u32 L_C = 107520;
constexpr u32 L_MISC = L_C + 55296;
constexpr u32 L_AGG = L_MISC + 128;
constexpr u32 L_KN = L_MISC + 896;
constexpr u32 L_PRIV = 26880;
constexpr u32 L_OS = 12288;
constexpr u32 L_QX = L_C + 54 * 864;
constexpr u32 LDS_BYTES = 163840;
enum { C_WQ = 0, C_Q2, C_QDONE, C_BFULL, C_AGG, C_WO, C_O, C_MAX, C_P, C_Y, C_FB, C_KVP, C_KMAX, C_KV, C_OF, C_HF, C_WQ2, C_CREG, C_EPOCH = 31 };

struct Params {
  const u16 *Ax, *lre, *lim, *Bre, *Bim, *Cre, *Cim, *ldt, *Dsk, *Bx, *wq, *wk, *wv, *wo;
  float* out;
  char* ws;
};

DI float bf2f(u16 v) { return __uint_as_float(((u32)v) << 16); }
DI u32 cvtpk(float a, float b) { return __builtin_bit_cast(u32, __builtin_convertvector((f32x2){a, b}, bf16x2_t)); }
DI float rbf(float a) { return __uint_as_float(cvtpk(a, 0.f) << 16); }
DI bf16x8 frag(u32x4 v) { return __builtin_bit_cast(bf16x8, v); }
DI void glds16(const void* sbase, u32 voff, u32 lds_base) {
  asm volatile("s_mov_b32 m0, %2\n\ts_nop 0\n\tglobal_load_lds_dwordx4 %0, %1" :: "v"(voff), "s"(sbase), "s"(lds_base) : "memory");
}
DI u32x4 ldsr128(u32 a) { return *(const LAS u32x4*)(size_t)a; }
DI void ldsw128(u32 a, u32x4 v) { *(LAS u32x4*)(size_t)a = v; }
DI u32 ldsu(u32 a) { return (u32)__builtin_amdgcn_readfirstlane(*(volatile LAS u32*)(size_t)a); }
DI u32x2 ldstr(u32 a) { return __builtin_bit_cast(u32x2, __builtin_amdgcn_ds_read_tr16_b64_v4i16((LAS v4i16_t*)(size_t)a)); }
DI u32x4 trfrag(u32 a0, u32 a1) { u32x2 lo = ldstr(a0), hi = ldstr(a1); return (u32x4){lo.x, lo.y, hi.x, hi.y}; }
DI u32x4 f4(const f32x16& v, int g) {
  return (u32x4){__float_as_uint(v[4 * g]), __float_as_uint(v[4 * g + 1]), __float_as_uint(v[4 * g + 2]), __float_as_uint(v[4 * g + 3])};
}
DI void add4(f32x16& v, int g, u32x4 a) {
  v[4 * g] += __uint_as_float(a.x); v[4 * g + 1] += __uint_as_float(a.y); v[4 * g + 2] += __uint_as_float(a.z); v[4 * g + 3] += __uint_as_float(a.w);
}
DI void arrive(int c, int lane) {
  asm volatile("s_waitcnt lgkmcnt(0)" ::: "memory");
  if (lane == 0) asm volatile("ds_add_u32 %0, %1" :: "v"(L_MISC + 4 * c), "v"(1u) : "memory");
}
DI void await(int c, u32 target) {
#pragma unroll 1
  for (u32 spin = 0; spin < (1u << 22); ++spin) {
    asm volatile("" ::: "memory");
    if (ldsu(L_MISC + 4 * c) >= target) break;
    __builtin_amdgcn_s_sleep(1);
  }
  asm volatile("" ::: "memory");
}
DI float swap32(float v) {
  auto r = __builtin_amdgcn_permlane32_swap(__float_as_uint(v), __float_as_uint(v), false, false);
  return __uint_as_float((threadIdx.x & 32) ? r[0] : r[1]);
}
DI float wave_max_nonneg(float v) {
  u32 x = __float_as_uint(v);
  x = max(x, (u32)__builtin_amdgcn_update_dpp(0, (int)x, 0x111, 0xf, 0xf, false));
  x = max(x, (u32)__builtin_amdgcn_update_dpp(0, (int)x, 0x112, 0xf, 0xf, false));
  x = max(x, (u32)__builtin_amdgcn_update_dpp(0, (int)x, 0x114, 0xf, 0xf, false));
  x = max(x, (u32)__builtin_amdgcn_update_dpp(0, (int)x, 0x118, 0xf, 0xf, false));
  x = max(x, (u32)__builtin_amdgcn_update_dpp(0, (int)x, 0x142, 0xa, 0xf, false));
  x = max(x, (u32)__builtin_amdgcn_update_dpp(0, (int)x, 0x143, 0xc, 0xf, false));
  return __uint_as_float((u32)__builtin_amdgcn_readlane((int)x, 63));
}
DI void gstore_flag(char* ws, u32 off, u32 v) { __hip_atomic_store((GAS u32*)(ws + off), v, __ATOMIC_RELAXED, __HIP_MEMORY_SCOPE_AGENT); }
DI void poll_words(char* ws, u32 off, int n, u32 tag, int lane) {
  const int i = lane < n ? lane : 0;
#pragma unroll 1
  for (u32 spin = 0; spin < (1u << 17); ++spin) {
    asm volatile("" ::: "memory");
    u32 v = __hip_atomic_load((GAS u32*)(ws + off) + i, __ATOMIC_RELAXED, __HIP_MEMORY_SCOPE_AGENT);
    if (__all(v == tag)) break;
    __builtin_amdgcn_s_sleep(1);
  }
  asm volatile("" ::: "memory");
}
DI float poll_kv(char* ws, int rep, u32 tag, int lane) {
  u64 g;
#pragma unroll 1
  for (u32 spin = 0; spin < (1u << 17); ++spin) {
    asm volatile("" ::: "memory");
    g = __hip_atomic_load((GAS u64*)(ws + WS_KVFLAG + rep * 256) + (lane & 31), __ATOMIC_RELAXED, __HIP_MEMORY_SCOPE_AGENT);
    if (__all((u32)g == tag)) break;
    __builtin_amdgcn_s_sleep(1);
  }
  asm volatile("" ::: "memory");
  return __uint_as_float((u32)(g >> 32));
}
DI void cmul(float ar, float ai, float br, float bi, float& cr, float& ci) {
  float x, y;
  asm("v_mul_f32 %0, %2, %4\n\tv_mul_f32 %1, %2, %5\n\tv_fma_f32 %0, -%3, %5, %0\n\tv_fma_f32 %1, %3, %4, %1"
      : "=&v"(x), "=&v"(y) : "v"(ar), "v"(ai), "v"(br), "v"(bi));
  cr = x; ci = y;
}
DI void cstep(float ar, float ai, float& hr, float& hi, float ur, float ui) {
  float x, y;
  asm("v_fma_f32 %0, -%3, %5, %6\n\tv_fma_f32 %1, %3, %4, %7\n\tv_fma_f32 %0, %2, %4, %0\n\tv_fma_f32 %1, %2, %5, %1"
      : "=&v"(x), "=&v"(y) : "v"(ar), "v"(ai), "v"(hr), "v"(hi), "v"(ur), "v"(ui));
  hr = x; hi = y;
}
DI void set_pad(u32x4& f, float m, int h) {
  const float a = rbf(-m), b = rbf(-m - a), c = rbf(-m - a - b);
  if (h) { f.z = cvtpk(a, b); f.w = cvtpk(c, 0.f); }
}

#define TS(i) do { if ((i) == PROBE) asm volatile("s_memrealtime %0\n\ts_waitcnt lgkmcnt(0)" : "=s"(tprobe) :: "memory"); } while (0)
#define TSD() u64 tprobe = 0
#define TSW() do {} while (0)
#define LPRIO 0
#define PUP() do {} while (0)
#define PDN() SBAR()
#define STR_(x) #x
#define VMTHR_(n) asm volatile("s_waitcnt vmcnt(" STR_(n) ")" ::: "memory")
#define VMTHR() VMTHR_(THR)

template <bool W_IS_A, int WROW, bool MID>
DI f32x16 proj_tile(int row, int h, u32 wa, int lane) {
  const int sw = (row >> 1) & 7;
  u32 xa[4];
#pragma unroll
  for (int bb = 0; bb < 4; ++bb) xa[bb] = L_XB + row * 128 + (((2 * bb + h) ^ sw) << 4);
  u32x4 xf[28], wf[28];
  f32x16 acc = {};
#pragma unroll
  for (int ks = 0; ks < 28 + 5; ++ks) {
    if (ks >= 5) {
      const int k = ks - 5;
      acc = W_IS_A ? MFMA(frag(wf[k]), frag(xf[k]), acc) : MFMA(frag(xf[k]), frag(wf[k]), acc);
      SBAR();
    }
    if (ks < 28) {
      if (MID && ks == 14) {
        asm volatile("s_nop 7\n\ts_nop 7\n\ts_waitcnt vmcnt(0)" ::: "memory");
        if (lane == 0) asm volatile("ds_add_u32 %0, %1" :: "v"(L_MISC + 4 * C_WQ2), "v"(1u) : "memory");
        await(C_WQ2, 4);
      }
      xf[ks] = ldsr128(xa[ks & 3] + 8192 * (ks >> 2));
      wf[ks] = trfrag(wa + ks * 16 * WROW, wa + ks * 16 * WROW + 4 * WROW);
      SBAR();
    }
  }
  MPIN(acc);
  return acc;
}

__global__ void __launch_bounds__(512, 2) fused_kernel(Params P) {
  extern __shared__ __attribute__((aligned(16))) char lds_dyn[];
  const int tid = threadIdx.x, lane = tid & 63, wv = __builtin_amdgcn_readfirstlane(tid >> 6);
  const int r = lane & 31, h = lane >> 5;
  const int b = blockIdx.x;
  const int tb = (b & 7) * 4 + (b >> 6), sl = (b >> 3) & 7;
  char* ws = P.ws;
  TSD();
  TS(0);
  {
    const int row = 8 * wv + (lane >> 3);
    const u32 vo = row * 896 + (((lane & 7) ^ ((row >> 1) & 7)) << 4);
    const char* src = (const char*)P.Bx + (size_t)tb * 64 * 896;
#pragma unroll
    for (int c = 0; c < 7; ++c) glds16(src + c * 128, vo, L_XB + c * 8192 + wv * 1024);
  }
  if (tid < 31) *(LAS u32*)(size_t)(L_MISC + 4 * tid) = 0;
  if (tid == 0) asm volatile("s_mov_b32 m0, %2\n\ts_nop 0\n\tglobal_load_lds_dword %0, %1 sc1" :: "v"(0), "s"(ws), "s"(L_MISC + 4 * C_EPOCH) : "memory");
  const auto rs_ws = __builtin_amdgcn_make_buffer_rsrc((void*)ws, 0, WS_END, 0x00020000);
  const auto rs_out = __builtin_amdgcn_make_buffer_rsrc((void*)P.out, 0, 2048 * 896 * 4, 0x00020000);
  const int g1 = (lane >> 4) & 1, q4 = (lane >> 2) & 3, p4 = lane & 3;
  const int l7 = lane / 7;
  const u32 vo7 = l7 * 896 + (lane - 7 * l7) * 16;
  const u32 vo = lane * 16;

  if (wv < 4) {
    const int w = wv;
    const char* wqsrc = (const char*)P.wq + sl * 112 + w * 7 * 7168;
    if (sl != 7) {
      if (lane < 56) {
#pragma unroll
        for (int j = 0; j < 14; ++j) glds16(wqsrc + (j % 7 + 28 * (j / 7)) * 7168, vo7, L_WQ + (w * 7 + j % 7 + 28 * (j / 7)) * 896);
      }
      asm volatile("s_waitcnt vmcnt(14) lgkmcnt(0)\n\ts_barrier" ::: "memory");
    } else {
      asm volatile("s_waitcnt vmcnt(0) lgkmcnt(0)\n\ts_barrier" ::: "memory");
      if (lane < 56) {
#pragma unroll
        for (int j = 0; j < 14; ++j) glds16(wqsrc + (j % 7 + 28 * (j / 7)) * 7168, vo7, L_WQ + (w * 7 + j % 7 + 28 * (j / 7)) * 896);
      }
    }
    TS(13);
    const u32 tag = ldsu(L_MISC + 4 * C_EPOCH) + 1u;
    VMCNT(7);
    TS(14);
    arrive(C_WQ, lane);
    await(C_WQ, 4);
    TS(1);
    {
      f32x16 acc = proj_tile<true, 112, true>(32 * (w & 1) + r, h, L_WQ + (8 * h + q4) * 112 + (w >> 1) * 56 + g1 * 32 + 8 * p4, lane);
      TS(8);
      const float qs = 0.27264060f;
      float n2 = 0.f;
#pragma unroll
      for (int i = 0; i < 16; ++i) { acc[i] *= qs; if (i < 12) n2 = fmaf(acc[i], acc[i], n2); else n2 = h ? n2 : fmaf(acc[i], acc[i], n2); }
      n2 += swap32(n2);
      u32x4 f0 = {cvtpk(acc[0], acc[1]), cvtpk(acc[2], acc[3]), cvtpk(acc[4], acc[5]), cvtpk(acc[6], acc[7])};
      u32x4 f1 = {cvtpk(acc[8], acc[9]), cvtpk(acc[10], acc[11]), cvtpk(acc[12], acc[13]), cvtpk(acc[14], acc[15])};
      if (h) { f1.z = 0; f1.w = __float_as_uint(__builtin_amdgcn_sqrtf(n2)); }
      if (sl == 7) await(C_AGG, 4);
      ldsw128(L_QX + w * 2048 + vo, f0);
      ldsw128(L_QX + w * 2048 + 1024 + vo, f1);
      arrive(C_Q2, lane);
    }
    u32x4 qf[4][2];
    if (QEARLY) {
      await(C_Q2, 4);
#pragma unroll
      for (int t = 0; t < 4; ++t) { const u32 a = L_QX + ((w + t) & 3) * 2048 + vo; qf[t][0] = ldsr128(a); qf[t][1] = ldsr128(a + 1024); }
      arrive(C_QDONE, lane);
    }
    TS(2);
    await(C_KV, 1);
    TS(3);
    const u32 kvbase = WS_KVF + w * 65536;
    const int rot = b >> 3;
#define KVOFF(it) (kvbase + ((((it) + rot) & 15) << 12))
#define LDK(it, s) __builtin_amdgcn_raw_buffer_load_b128(rs_ws, vo + 1024 * (s), KVOFF(it), KV_AUX)
#define LDV(it, s) __builtin_amdgcn_raw_buffer_load_b128(rs_ws, vo + 2048 + 1024 * (s), KVOFF(it), KV_AUX)
    f32x16 o[4];
    for (int attempt = 0; attempt < 2; ++attempt) {
      u32x4 kf[KV_DEPTH][2], vf[KV_DEPTH][2];
#pragma unroll
      for (int d = 0; d < KV_DEPTH; ++d) {
        kf[d][0] = LDK(d, 0); kf[d][1] = LDK(d, 1);
        if (d < KV_DEPTH - 1) { vf[d][0] = LDV(d, 0); vf[d][1] = LDV(d, 1); }
      }
      vf[KV_DEPTH - 1][0] = (u32x4){0, 0, 0, 0}; vf[KV_DEPTH - 1][1] = (u32x4){0, 0, 0, 0};
      SBAR();
      if (attempt == 0) {
        if (!QEARLY) {
        await(C_Q2, 4);
#pragma unroll
        for (int t = 0; t < 4; ++t) { const u32 a = L_QX + ((w + t) & 3) * 2048 + vo; qf[t][0] = ldsr128(a); qf[t][1] = ldsr128(a + 1024); }
        arrive(C_QDONE, lane);
        }
        const float kmax = __builtin_amdgcn_sqrtf(wave_max_nonneg(*(LAS float*)(size_t)(L_KN + 4 * r)));
#pragma unroll
        for (int t = 0; t < 4; ++t) set_pad(qf[t][1], __uint_as_float(qf[t][1].w) * kmax, h);
      }
#pragma unroll
      for (int t = 0; t < 4; ++t) { o[t] = (f32x16){}; PIN(o[t]); PIN(qf[t][0]); PIN(qf[t][1]); }
      SBAR();
      const f32x16 zero = {};
      f32x16 sc = MFMA(frag(kf[0][0]), frag(qf[0][0]), zero);
      sc = MFMA(frag(kf[0][1]), frag(qf[0][1]), sc);
      TS(9);
      float e0 = 0.f, e1 = 0.f, e2 = 0.f, e3 = 0.f;
      u32x4 p1 = {0, 0, 0, 0};
      for (int it0 = 0; it0 < 16; it0 += KV_DEPTH) {
#pragma unroll
        for (int d = 0; d < KV_DEPTH; ++d) {
#pragma unroll
          for (int qi = 0; qi < 4; ++qi) {
            const int dn = qi == 3 ? (d + 1) % KV_DEPTH : d, qn = (qi + 1) & 3;
            const int dp = qi == 0 ? (d + KV_DEPTH - 1) % KV_DEPTH : d, qp = (qi + 3) & 3;
            f32x16 sn; u32x4 p0;
            float c0, c1, c2, c3;
            sn = MFMA(frag(kf[dn][0]), frag(qf[qn][0]), zero); SBAR();
            p1.z = cvtpk(e0, e1); p1.w = cvtpk(e2, e3); PIN(p1); SBAR();
            c0 = EX(sc[0]); c1 = EX(sc[1]); c2 = EX(sc[2]); c3 = EX(sc[3]); PIN4(c0, c1, c2, c3); SBAR();
            o[qp] = MFMA(frag(vf[dp][1]), frag(p1), o[qp]); SBAR();
            p0.x = cvtpk(c0, c1); p0.y = cvtpk(c2, c3); PIN2(p0.x, p0.y); SBAR();
            c0 = EX(sc[4]); c1 = EX(sc[5]);
            if (!NOLOAD && qi == 0) { PIN2(c0, c1); SBAR(); PUP(); vf[dp][0] = LDV(it0 + d - 1 + KV_DEPTH, 0); PDN(); }
            if (!NOLOAD && qi == 1) { PIN2(c0, c1); SBAR(); const int dq = (d + KV_DEPTH - 1) % KV_DEPTH; PUP(); vf[dq][1] = LDV(it0 + d - 1 + KV_DEPTH, 1); PDN(); }
            c2 = EX(sc[6]); c3 = EX(sc[7]); PIN4(c0, c1, c2, c3); SBAR();
            sn = MFMA(frag(kf[dn][1]), frag(qf[qn][1]), sn); SBAR();
            p0.z = cvtpk(c0, c1); p0.w = cvtpk(c2, c3); PIN(p0); SBAR();
            c0 = EX(sc[8]); c1 = EX(sc[9]);
            if (!NOLOAD && qi == 2) { PIN2(c0, c1); SBAR(); PUP(); kf[d][0] = LDK(it0 + d + KV_DEPTH, 0); PDN(); }
            c2 = EX(sc[10]); c3 = EX(sc[11]); PIN4(c0, c1, c2, c3); SBAR();
            o[qi] = MFMA(frag(vf[d][0]), frag(p0), o[qi]); SBAR();
            p1.x = cvtpk(c0, c1); p1.y = cvtpk(c2, c3); PIN2(p1.x, p1.y); SBAR();
            e0 = EX(sc[12]); e1 = EX(sc[13]);
            if (!NOLOAD && qi == 2) { PIN2(e0, e1); SBAR(); PUP(); kf[d][1] = LDK(it0 + d + KV_DEPTH, 1); PDN(); }
            e2 = EX(sc[14]); e3 = EX(sc[15]); PIN4(e0, e1, e2, e3); SBAR();
            sc = sn;
          }
        }
      }
      p1.z = cvtpk(e0, e1); p1.w = cvtpk(e2, e3);
      o[3] = MFMA(frag(vf[KV_DEPTH - 1][1]), frag(p1), o[3]);
      MPIN4(o[0], o[1], o[2], o[3]);
      TS(4);
      if (attempt == 0) {
        bool bad = false;
#pragma unroll
        for (int t = 0; t < 4; ++t) bad |= !(o[t][12] >= 0x1p-80f && o[t][12] <= 0x1p100f);
        if (__any(bad && h)) { if (lane == 0) *(LAS u32*)(size_t)(L_MISC + 4 * C_FB) = 1u; }
        await(C_CREG, 4);
      }
#pragma unroll
      for (int t = 1; t < 4; ++t) {
#pragma unroll
        for (int g = 0; g < 4; ++g) ldsw128(w * L_PRIV + L_OS + (t - 1) * 4096 + g * 1024 + vo, f4(o[t], g));
      }
      arrive(C_O, lane);
      await(C_O, 4 * (attempt + 1));
      TS(10);
      const u32 fb = attempt == 0 ? ldsu(L_MISC + 4 * C_FB) : 0u;
      if (!fb) break;
      {
        float mx[4];
#pragma unroll
        for (int t = 0; t < 4; ++t) { mx[t] = -3.0e38f; if (h) { qf[t][1].z = 0; qf[t][1].w = 0; } }
#pragma unroll 1
        for (int it = 0; it < 16; ++it) {
          const u32x4 k0 = LDK(it, 0), k1 = LDK(it, 1);
#pragma unroll
          for (int t = 0; t < 4; ++t) {
            f32x16 s = MFMA(frag(k0), frag(qf[t][0]), zero);
            s = MFMA(frag(k1), frag(qf[t][1]), s);
#pragma unroll
            for (int i = 0; i < 16; ++i) mx[t] = fmaxf(mx[t], s[i]);
          }
        }
        const u32 ma = L_C + 51200;
#pragma unroll
        for (int t = 0; t < 4; ++t) { mx[t] = fmaxf(mx[t], swap32(mx[t])); *(LAS float*)(size_t)(ma + (w * 4 + ((w + t) & 3)) * 256 + lane * 4) = mx[t]; }
        arrive(C_MAX, lane);
        await(C_MAX, 4);
#pragma unroll
        for (int t = 0; t < 4; ++t) {
          float m = -3.0e38f;
#pragma unroll
          for (int ww = 0; ww < 4; ++ww) m = fmaxf(m, *(LAS float*)(size_t)(ma + (ww * 4 + ((w + t) & 3)) * 256 + lane * 4));
          set_pad(qf[t][1], m, h);
        }
      }
    }
    {
      f32x16 own = o[0];
#pragma unroll
      for (int pp = 1; pp < 4; ++pp) {
        const u32 a = ((w + pp) & 3) * L_PRIV + L_OS + (3 - pp) * 4096 + vo;
#pragma unroll
        for (int g = 0; g < 4; ++g) add4(own, g, ldsr128(a + g * 1024));
      }
      const float other = swap32(own[12]);
      const float inv = __builtin_amdgcn_rcpf(h ? own[12] : other);
      u32x4 f0 = {cvtpk(own[0] * inv, own[1] * inv), cvtpk(own[2] * inv, own[3] * inv), cvtpk(own[4] * inv, own[5] * inv), cvtpk(own[6] * inv, own[7] * inv)};
      u32x4 f1 = {cvtpk(own[8] * inv, own[9] * inv), cvtpk(own[10] * inv, own[11] * inv), cvtpk(own[12] * inv, own[13] * inv), cvtpk(own[14] * inv, own[15] * inv)};
      if (h) { f1.z = 0; f1.w = 0; }
      const u32 so = WS_OF + ((tb * 2 + (w & 1)) * 16 + 2 * sl + (w >> 1)) * 2048 + vo;
      __builtin_amdgcn_raw_buffer_store_b128(f0, rs_ws, so, 0, 16);
      __builtin_amdgcn_raw_buffer_store_b128(f1, rs_ws, so + 1024, 0, 16);
    }
    {
      const int tm = w & 1, kh = w >> 1;
      TS(11);
      VMCNT(0);
      if (lane == 0) gstore_flag(ws, WS_OFLAG + (tb * 32 + sl * 4 + w) * 4, tag);
      TS(5);
      await(C_WO, 4);
      u32x4 wf[16][2];
      const u32 wa = L_C + (224 * kh + 4 * h + q4) * 112 + g1 * 32 + 8 * p4;
      const u32 wa0 = wa + 64 * kh, wa1 = wa + 64 * (1 - kh);
#pragma unroll
      for (int i = 0; i < 16; ++i) {
        const u32 a = (28 * (i >> 1) + 16 * (i & 1)) * 112;
        wf[i][0] = trfrag(wa0 + a, wa0 + a + 896); wf[i][1] = trfrag(wa1 + a, wa1 + a + 896);
      }
#pragma unroll
      for (int i = 0; i < 16; ++i) PIN2(wf[i][0], wf[i][1]);
      if (w == 0) { poll_words(ws, WS_OFLAG + tb * 128, 32, tag, lane); arrive(C_OF, lane); }
      else await(C_OF, 1);
      TS(6);
      u32x4 of[16];
      const u32 so = WS_OF + ((tb * 2 + tm) * 16 + 8 * kh) * 2048;
#pragma unroll
      for (int i = 0; i < 16; ++i) of[i] = __builtin_amdgcn_raw_buffer_load_b128(rs_ws, vo + (i & 3) * 1024, so + (i >> 2) * 4096, H_AUX);
      SBAR();
      f32x16 a0 = {}, a1 = {};
#pragma unroll
      for (int i = 0; i < 16; ++i) {
        a0 = MFMA(frag(of[i]), frag(wf[i][0]), a0);
        a1 = MFMA(frag(of[i]), frag(wf[i][1]), a1);
      }
      MPIN2(a0, a1);
      TS(12);
      const f32x16 snd = a1;
      f32x16 own = a0;
      const u32 aw = w * L_PRIV + L_OS + vo, ar = (w ^ 2) * L_PRIV + L_OS + vo;
#pragma unroll
      for (int g = 0; g < 4; ++g) ldsw128(aw + g * 1024, f4(snd, g));
      arrive(C_P, lane);
      await(C_P, 4);
#pragma unroll
      for (int g = 0; g < 4; ++g) add4(own, g, ldsr128(ar + g * 1024));
      if (32 * kh + r < 56) {
        const u32 ov = ((tb * 64 + 32 * tm + 4 * h) * 896 + 448 + 56 * sl + 32 * kh + r) * 4;
#pragma unroll
        for (int i = 0; i < 16; ++i) __builtin_amdgcn_raw_buffer_store_b32(__float_as_uint(rbf(own[i])), rs_out, ov, ((i & 3) + 8 * (i >> 2)) * 3584, OUT_AUX);
      }
    }
    TS(7);
    TSW();
    while (__builtin_amdgcn_s_memrealtime() - tprobe < 3000) __builtin_amdgcn_s_sleep(2);
  } else {
    __builtin_amdgcn_s_setprio(AUX_PRIO);
    const int q = wv - 4;
    const u32 PA = q * L_PRIV;
    const int part = q >> 1, half = q & 1;
    u32 tag;
    auto stage_C = [&]() {
      if (!(ABL & 2) && lane < 56) {
        const char* src = (const char*)(part ? P.Cim : P.Cre) + (size_t)sl * 56 * 896 + half * 448;
        const int hi = lane >= 28, cs = lane - 28 * hi;
        u32 cvo[4];
#pragma unroll
        for (int t = 0; t < 4; ++t) cvo[t] = hi * 896 + ((cs ^ t) << 4);
#pragma unroll
        for (int i = 0; i < 28; ++i) { glds16(src + i * 1792, cvo[(i >> 1) & 3], PA + i * 896); VMTHR(); }
      }
    };
    auto stage_wo = [&]() {
      if (!(ABL & 4) && lane < 56) {
        const char* src = (const char*)P.wo + sl * 112 + q * 14 * 7168;
#pragma unroll
        for (int i = 0; i < 14; ++i) { glds16(src + i * 7168, vo7, L_C + (q * 14 + i) * 896); VMTHR(); }
        if (q == 3) glds16(src, vo7, L_C + 56 * 896);
      }
    };
    auto kv_wait = [&]() {
      const float kn = poll_kv(ws, (b >> 3) % KV_REPL, tag, lane);
      if (FENCE) { __builtin_amdgcn_fence(__ATOMIC_ACQUIRE, "agent"); VMCNT(0); }
      if (lane < 32) *(LAS float*)(size_t)(L_KN + 4 * lane) = kn;
      arrive(C_KV, lane);
    };
    u32x4 cf[2][14];
    auto read_C = [&]() {
#pragma unroll
      for (int tn = 0; tn < 2; ++tn) {
        int row = 32 * (tn ^ part) + r; row = row > 55 ? 55 : row;
        const int t = (row >> 2) & 3;
        const u32 a0 = PA + row * 448 + ((h ^ t) << 4), a1 = PA + row * 448 + (((2 + h) ^ t) << 4);
#pragma unroll
        for (int j = 0; j < 14; ++j) cf[tn][j] = ldsr128(((j & 1) ? a1 : a0) + 64 * (j >> 1));
      }
    };
    if (sl == 7) {
      const int kind = q >> 1, tm = q & 1;
      if (lane < 56) {
        const char* src = (const char*)(kind ? P.wv : P.wk) + tm * 14 * 896;
#pragma unroll
        for (int j = 0; j < 14; ++j) glds16(src + j * 896, vo, L_C + (q * 14 + j) * 896);
      }
      asm volatile("s_waitcnt vmcnt(0) lgkmcnt(0)\n\ts_barrier" ::: "memory");
      TS(1);
      const u32 wa = L_C + kind * 25088 + (8 * h + q4) * 56 + g1 * 32 + 8 * p4;
      f32x16 acc;
      if (kind == 0) acc = proj_tile<true, 56, false>(32 * tm + r, h, wa, lane);
      else acc = proj_tile<false, 56, false>(32 * tm + r, h, wa, lane);
      TS(3);
      u32x4 f0 = {cvtpk(acc[0], acc[1]), cvtpk(acc[2], acc[3]), cvtpk(acc[4], acc[5]), cvtpk(acc[6], acc[7])};
      u32x4 f1 = {cvtpk(acc[8], acc[9]), cvtpk(acc[10], acc[11]), cvtpk(acc[12], acc[13]), cvtpk(acc[14], acc[15])};
      if (kind == 0) {
        if (h) { f1.z = 0x3F803F80u; f1.w = 0x00003F80u; }
      } else if (r >= 28) {
        const u32 c = r == 28 ? 0x3F803F80u : 0u;
        f0 = (u32x4){c, c, c, c}; f1 = f0;
      }
      const u32 so = WS_KVF + (tb * 2 + tm) * 4096 + kind * 2048 + vo;
      __builtin_amdgcn_raw_buffer_store_b128(f0, rs_ws, so, 0, 16);
      __builtin_amdgcn_raw_buffer_store_b128(f1, rs_ws, so + 1024, 0, 16);
      SBAR();
      tag = ldsu(L_MISC + 4 * C_EPOCH) + 1u;
      if (kind == 0) {
        float n2 = 0.f;
#pragma unroll
        for (int i = 0; i < 16; ++i) { if (i < 12) n2 = fmaf(acc[i], acc[i], n2); else n2 = h ? n2 : fmaf(acc[i], acc[i], n2); }
        n2 += swap32(n2);
        n2 = wave_max_nonneg(n2);
        if (lane == 0) asm volatile("ds_max_u32 %0, %1" :: "v"(L_MISC + 4 * C_KMAX), "v"(n2) : "memory");
      }
      TS(4);
      VMCNT(0);
      TS(5);
      u32 old = 0;
      if (lane == 0) asm volatile("s_waitcnt lgkmcnt(0)\n\tds_add_rtn_u32 %0, %1, %2\n\ts_waitcnt lgkmcnt(0)" : "=&v"(old) : "v"(L_MISC + 4 * C_KVP), "v"(1u) : "memory");
      if (__builtin_amdgcn_readfirstlane(old) == 3) {
        const u32 km = ldsu(L_MISC + 4 * C_KMAX);
        if (lane < KV_REPL) __hip_atomic_store((GAS u64*)(ws + WS_KVFLAG + lane * 256 + tb * 8), (u64)tag | ((u64)km << 32), __ATOMIC_RELAXED, __HIP_MEMORY_SCOPE_AGENT);
      }
      TS(2);
      arrive(C_AGG, lane);
      if (q == 0) kv_wait();
      await(C_AGG, 4);
      await(C_QDONE, 4);
      stage_wo();
    } else {
      const int tb2 = tb >> 1, cg = (tb & 1) * 7 + sl, ch = 32 * cg + r;
      u32 vlre = P.lre[ch], vlim = P.lim[ch], vldt = P.ldt[ch];
      u32x4 blast_re = *(const u32x4*)(P.Bre + (size_t)ch * 448 + 432 + 8 * h);
      u32x4 blast_im = *(const u32x4*)(P.Bim + (size_t)ch * 448 + 432 + 8 * h);
      asm volatile("s_waitcnt vmcnt(0) lgkmcnt(0)\n\ts_barrier" ::: "memory");
      asm volatile("" : "+v"(vlre), "+v"(vlim), "+v"(vldt), "+v"(blast_re), "+v"(blast_im) :: "memory");
      await(C_WQ, 4);
      const char* bsrc = (const char*)(part ? P.Bim : P.Bre) + (size_t)(cg * 32 + half * 16) * 896;
      const u32 vo1 = (lane ^ 1) * 16;
      if (lane < 54) {
#pragma unroll
        for (int i = 0; i < 16; ++i)
          if (i < 6 || q != 3) glds16(bsrc + i * 896, i < 8 ? vo : vo1, L_C + (q * 16 + i) * 864);
      }
      tag = ldsu(L_MISC + 4 * C_EPOCH) + 1u;
      float lr, li, cr, ci;
      {
        const float dt = rbf(expf(__uint_as_float(vldt << 16))), a = __uint_as_float(vlre << 16), bb = __uint_as_float(vlim << 16);
        const float er = expf(dt * a);
        float sn, cs;
        sincosf(dt * bb, &sn, &cs);
        lr = er * cs; li = er * sn;
        const float nr = lr - 1.f, ni = li, den = 2.0f / (a * a + bb * bb);
        cr = (nr * a + ni * bb) * den; ci = (ni * a - nr * bb) * den;
      }
      float l16r = lr, l16i = li;
#pragma unroll
      for (int i = 0; i < 4; ++i) cmul(l16r, l16i, l16r, l16i, l16r, l16i);
      float l32r, l32i, l128r, l128i;
      cmul(l16r, l16i, l16r, l16i, l32r, l32i);
      cmul(l32r, l32i, l32r, l32i, l128r, l128i);
      cmul(l128r, l128i, l128r, l128i, l128r, l128i);
      PIN4(lr, li, cr, ci); PIN4(l16r, l16i, l32r, l32i); PIN2(l128r, l128i);
      const char* xsrc = (const char*)P.Ax + (size_t)(tb2 * 128 + q * 32) * 896;
      const u32 xv0 = (lane >> 3) * 896 + (((lane & 7) ^ (lane >> 4)) << 4), xv1 = xv0 ^ 64;
      const int rho = 16 * ((r >> 2) & 1) + (r & 3) + 4 * (r >> 3), sw = (rho >> 1) & 7;
      u32 xa[4];
#pragma unroll
      for (int bb = 0; bb < 4; ++bb) xa[bb] = PA + rho * 128 + (((2 * bb + h) ^ sw) << 4);
      const u32 ba = L_C + r * 864 + ((h ^ ((r >> 3) & 1)) << 4);
      PIN4(xa[0], xa[1], xa[2], xa[3]);
      TS(1);
      if (q == 0) kv_wait();
      await(C_QDONE, 4);
      if (q == 3 && lane < 54) {
#pragma unroll
        for (int i = 6; i < 16; ++i) glds16(bsrc + i * 896, i < 8 ? vo : vo1, L_C + (48 + i) * 864);
      }
#pragma unroll
      for (int c = 0; c < 6; ++c)
#pragma unroll
        for (int j = 0; j < 4; ++j) glds16(xsrc + c * 128 + j * 7168, (j & 1) ? xv1 : xv0, PA + c * 4096 + j * 1024);
      VMCNT(24);
      arrive(C_BFULL, lane);
      await(C_BFULL, 4);
      TS(2);
      f32x16 ur = {}, ui = {};
      PIN2(ur, ui);
      __builtin_amdgcn_s_setprio(G1_PRIO);
      {
        u32x4 xf[28], br[28], bi[28];
#pragma unroll
        for (int ks = 0; ks < 28 + 4; ++ks) {
          if (ks >= 4) {
            const int k = ks - 4;
            ur = MFMA(frag(xf[k]), frag(br[k]), ur);
            ui = MFMA(frag(xf[k]), frag(bi[k]), ui);
            SBAR();
            if (k == 3) {
#pragma unroll
              for (int j = 0; j < 4; ++j) glds16(xsrc + 6 * 128 + j * 7168, (j & 1) ? xv1 : xv0, PA + j * 1024);
            }
          }
          if (ks < 28) {
            if (ks == 0) VMCNT(20); else if (ks == 4) VMCNT(16); else if (ks == 8) VMCNT(16); else if (ks == 12) VMCNT(12);
            else if (ks == 16) VMCNT(8); else if (ks == 20) VMCNT(4); else if (ks == 24) VMCNT(0);
            xf[ks] = ldsr128(xa[ks & 3] + ((ks >> 2) % 6) * 4096);
            br[ks] = ks < 27 ? ldsr128(ba + 32 * ks) : blast_re;
            bi[ks] = ks < 27 ? ldsr128(ba + 27648 + 32 * ks) : blast_im;
            SBAR();
          }
        }
      }
      MPIN2(ur, ui);
      __builtin_amdgcn_s_setprio(AUX_PRIO);
      TS(3);
      float ar = 0.f, ai = 0.f;
#pragma unroll
      for (int i = 0; i < 16; ++i) {
        float tr, ti;
        cmul(cr, ci, ur[i], ui[i], tr, ti);
        ur[i] = tr; ui[i] = ti;
        cstep(lr, li, ar, ai, tr, ti);
      }
      const float or_ = swap32(ar), oi_ = swap32(ai);
      const float lor = h ? or_ : ar, loi = h ? oi_ : ai;
      float a32r = lor, a32i = loi;
      cstep(l16r, l16i, a32r, a32i, h ? ar : or_, h ? ai : oi_);
      if (q < 3 && h == 0) *(LAS f32x2*)(size_t)(L_AGG + q * 256 + r * 8) = (f32x2){a32r, a32i};
      arrive(C_AGG, lane);
      await(C_AGG, 4);
      f32x2 wagg[3];
#pragma unroll
      for (int j = 0; j < 3; ++j) wagg[j] = *(LAS f32x2*)(size_t)(L_AGG + j * 256 + r * 8);
      const u32 aggb = WS_AGG + cg * 8192;
      if (q == 3 && tb2 < 15) {
        float tr = wagg[0].x, ti = wagg[0].y;
        cstep(l32r, l32i, tr, ti, wagg[1].x, wagg[1].y);
        cstep(l32r, l32i, tr, ti, wagg[2].x, wagg[2].y);
        cstep(l32r, l32i, tr, ti, a32r, a32i);
        __hip_atomic_store((GAS u64*)(ws + aggb + tb2 * 512 + r * 16 + h * 8), (u64)__float_as_uint(h ? ti : tr) | ((u64)tag << 32), __ATOMIC_RELAXED, __HIP_MEMORY_SCOPE_AGENT);
      }
      stage_wo();
      TS(4);
      float hr = 0.f, hi = 0.f;
      if (!(ABL & 1) && tb2 > 0) {
        u32x4 g[8];
        u32 gv[8];
#pragma unroll
        for (int j = 0; j < 8; ++j) gv[j] = r * 16 + ((8 * h + j < tb2) ? (8 * h + j) * 512 : 0);
#pragma unroll 1
        for (u32 spin = 0; spin < (1u << 17); ++spin) {
          asm volatile("" ::: "memory");
          u32 bad = 0;
#pragma unroll
          for (int j = 0; j < 8; ++j) g[j] = __builtin_amdgcn_raw_buffer_load_b128(rs_ws, gv[j], aggb, 16);
#pragma unroll
          for (int j = 0; j < 8; ++j) bad |= (g[j].y ^ tag) | (g[j].w ^ tag);
          if (__all(bad == 0)) break;
          __builtin_amdgcn_s_sleep(4);
        }
        asm volatile("" ::: "memory");
#pragma unroll
        for (int j = 0; j < 8; ++j)
          if (j < tb2) cstep(l128r, l128i, hr, hi, __uint_as_float(g[j].x), __uint_as_float(g[j].z));
        const float sr = swap32(hr), si = swap32(hi);
        if (h) { hr = sr; hi = si; }
        if (tb2 > 8) {
#pragma unroll
          for (int j = 0; j < 7; ++j)
            if (8 + j < tb2) cstep(l128r, l128i, hr, hi, __uint_as_float(g[j].x), __uint_as_float(g[j].z));
          const float tr = swap32(hr), ti = swap32(hi);
          if (!h) { hr = tr; hi = ti; }
        }
      }
#pragma unroll
      for (int j = 0; j < 3; ++j)
        if (j < q) cstep(l32r, l32i, hr, hi, wagg[j].x, wagg[j].y);
      if (h) cstep(l16r, l16i, hr, hi, lor, loi);
      TS(5);
      u32 pr[8], pi[8];
#pragma unroll
      for (int i = 0; i < 16; i += 2) {
        cstep(lr, li, hr, hi, ur[i], ui[i]);
        const float r0 = hr, i0 = hi;
        cstep(lr, li, hr, hi, ur[i + 1], ui[i + 1]);
        pr[i >> 1] = cvtpk(r0, hr); pi[i >> 1] = cvtpk(-i0, -hi);
      }
      const u32 ta = PA + r * 80 + 32 * h;
      ldsw128(ta, (u32x4){pr[0], pr[1], pr[2], pr[3]}); ldsw128(ta + 16, (u32x4){pr[4], pr[5], pr[6], pr[7]});
      ldsw128(ta + 2560, (u32x4){pi[0], pi[1], pi[2], pi[3]}); ldsw128(ta + 2560 + 16, (u32x4){pi[4], pi[5], pi[6], pi[7]});
      LGKM0();
      const u32 tr = PA + (8 * h + q4) * 80 + g1 * 32 + 8 * p4;
      const int tbh = 2 * tb2 + (q >> 1);
      const u32 so = WS_HF + ((tbh * 2 + (q & 1)) * 56 + 2 * cg) * 1024 + vo;
#pragma unroll
      for (int pt = 0; pt < 2; ++pt)
#pragma unroll
        for (int s = 0; s < 2; ++s) {
          const u32 a = tr + pt * 2560 + s * 1280;
          if (!(ABL & 16)) __builtin_amdgcn_raw_buffer_store_b128(trfrag(a, a + 320), rs_ws, so + pt * 28 * 1024 + s * 1024, 0, 16);
        }
      VMCNT(0);
      if (lane == 0) gstore_flag(ws, WS_HFLAG + (tbh * 32 + cg * 2 + (q & 1)) * 4, tag);
      TS(6);
    }
    VMCNT(0);
    arrive(C_WO, lane);
    stage_C();
    const int tm = q & 1, tn = q >> 1;
    int col = 32 * tn + r; const bool valid = col < 56; col = valid ? col : 55;
    const int d = 56 * sl + col, t0 = tb * 64 + 32 * tm + 4 * h;
    float skip[16];
    {
      u16 xs[16];
#pragma unroll
      for (int i = 0; i < 16; ++i) xs[i] = (ABL & 32) ? (u16)i : P.Ax[(size_t)(t0 + (i & 3) + 8 * (i >> 2)) * 448 + d];
      const float dsk = bf2f(P.Dsk[d]);
#pragma unroll
      for (int i = 0; i < 16; i += 2) { float a = bf2f(xs[i]) * dsk, c = bf2f(xs[i + 1]) * dsk; PIN(a); skip[i >> 1] = __uint_as_float(cvtpk(a, c)); }
#pragma unroll
      for (int i = 0; i < 8; ++i) PIN(skip[i]);
    }
    VMCNT(0);
    read_C();
#pragma unroll
    for (int j = 0; j < 14; ++j) PIN2(cf[0][j], cf[1][j]);
    arrive(C_CREG, lane);
    TS(9);
    if (ABL & 8) { TSW(); if (tb == 31 && sl == 0 && q == 0) { for (int i = 0; i < 1000; ++i) __builtin_amdgcn_s_sleep(1); if (lane == 0) gstore_flag(ws, WS_EPOCH, tag); } return; }
    if (q == 0) { poll_words(ws, WS_HFLAG + tb * 128, 28, tag, lane); arrive(C_HF, lane); }
    else await(C_HF, 1);
    TS(7);
    {
      f32x16 y[4];
      const u32 so = WS_HF + ((tb * 2 + tm) * 56 + part * 28 + half * 14) * 1024, so1 = WS_HF + ((tb * 2 + (tm ^ 1)) * 56 + part * 28 + half * 14) * 1024;
      u32x4 hf[14];
#pragma unroll
      for (int j = 0; j < 14; ++j) hf[j] = __builtin_amdgcn_raw_buffer_load_b128(rs_ws, vo + (j & 3) * 1024, so + (j >> 2) * 4096, H_AUX);
      SBAR();
#pragma unroll
      for (int t = 0; t < 4; ++t) y[t] = (f32x16){};
      PIN4(y[0], y[1], y[2], y[3]);
      __builtin_amdgcn_s_setprio(AUX2_PRIO);
#pragma unroll
      for (int j = 0; j < 14; ++j) {
        y[0] = MFMA(frag(hf[j]), frag(cf[0][j]), y[0]);
        y[2] = MFMA(frag(hf[j]), frag(cf[1][j]), y[2]);
        SBAR();
        hf[j] = __builtin_amdgcn_raw_buffer_load_b128(rs_ws, vo + (j & 3) * 1024, so1 + (j >> 2) * 4096, H_AUX);
        SBAR();
      }
#pragma unroll
      for (int j = 0; j < 14; ++j) {
        y[1] = MFMA(frag(hf[j]), frag(cf[0][j]), y[1]);
        y[3] = MFMA(frag(hf[j]), frag(cf[1][j]), y[3]);
      }
      MPIN4(y[0], y[1], y[2], y[3]);
      __builtin_amdgcn_s_setprio(AUX3_PRIO);
#pragma unroll
      for (int t = 1; t < 4; ++t) {
#pragma unroll
        for (int g = 0; g < 4; ++g) ldsw128(PA + (t - 1) * 4096 + g * 1024 + vo, f4(y[t], g));
      }
      arrive(C_Y, lane);
      await(C_Y, 4);
      f32x16 own = y[0];
#pragma unroll
      for (int t = 1; t < 4; ++t) {
#pragma unroll
        for (int g = 0; g < 4; ++g) add4(own, g, ldsr128((q ^ t) * L_PRIV + (t - 1) * 4096 + g * 1024 + vo));
      }
      if (valid) {
        const u32 ov = (t0 * 896 + d) * 4;
#pragma unroll
        for (int i = 0; i < 16; ++i) {
          const u32 pk = __float_as_uint(skip[i >> 1]);
          __builtin_amdgcn_raw_buffer_store_b32(__float_as_uint(own[i] + __uint_as_float((i & 1) ? (pk & 0xffff0000u) : (pk << 16))), rs_out, ov, ((i & 3) + 8 * (i >> 2)) * 3584, OUT_AUX);
        }
      }
    }
    TS(8);
    TSW();
    if (tb == 31 && sl == 0 && q == 0) {
      await(C_KV, 1);
      if (lane == 0) gstore_flag(ws, WS_EPOCH, tag);
    }
  }
}

extern "C" void kernel_launch(void* const* d_in, const int* in_sizes, int n_in, void* d_out, int out_size, void* d_ws, size_t ws_size,
                              hipStream_t stream) {
  Params P;
  P.Ax = (const u16*)d_in[0]; P.lre = (const u16*)d_in[1]; P.lim = (const u16*)d_in[2]; P.Bre = (const u16*)d_in[3];
  P.Bim = (const u16*)d_in[4]; P.Cre = (const u16*)d_in[5]; P.Cim = (const u16*)d_in[6]; P.ldt = (const u16*)d_in[7];
  P.Dsk = (const u16*)d_in[8]; P.Bx = (const u16*)d_in[9]; P.wq = (const u16*)d_in[10]; P.wk = (const u16*)d_in[11];
  P.wv = (const u16*)d_in[12]; P.wo = (const u16*)d_in[13];
  P.out = (float*)d_out; P.ws = (char*)d_ws;
  (void)hipFuncSetAttribute((const void*)fused_kernel, hipFuncAttributeMaxDynamicSharedMemorySize, LDS_BYTES);
  fused_kernel<<<256, 512, LDS_BYTES, stream>>>(P);
}
```

```cpp
#include <hip/hip_runtime.h>

typedef unsigned short u16;
typedef unsigned int u32;
typedef unsigned long long u64;
using bf16x8 = __attribute__((ext_vector_type(8))) short;
using f32x16 = __attribute__((ext_vector_type(16))) float;
using u32x4 = __attribute__((ext_vector_type(4))) unsigned;
using u32x2 = __attribute__((ext_vector_type(2))) unsigned;
typedef short v4i16_t __attribute__((ext_vector_type(4)));
typedef __bf16 bf16x2_t __attribute__((ext_vector_type(2)));
typedef float f32x2 __attribute__((ext_vector_type(2)));
#define LAS __attribute__((address_space(3)))
#define GAS __attribute__((address_space(1)))
#define DI __device__ __forceinline__
#define MFMA(a, b, c) __builtin_amdgcn_mfma_f32_32x32x16_bf16((a), (b), (c), 0, 0, 0)
#define SBAR() __builtin_amdgcn_sched_barrier(0)
#define PIN(x) asm volatile("" : "+v"(x))
#define PIN2(a, b) asm volatile("" : "+v"(a), "+v"(b))
#define PIN4(a, b, c, d) asm volatile("" : "+v"(a), "+v"(b), "+v"(c), "+v"(d))
#define MPIN(x) asm volatile("s_nop 7\n\ts_nop 7" : "+v"(x))
#define MPIN2(a, b) asm volatile("s_nop 7\n\ts_nop 7" : "+v"(a), "+v"(b))
#define MPIN4(a, b, c, d) asm volatile("s_nop 7\n\ts_nop 7" : "+v"(a), "+v"(b), "+v"(c), "+v"(d))
#define VMCNT(n) asm volatile("s_waitcnt vmcnt(" #n ")" ::: "memory")
#define LGKM0() asm volatile("s_waitcnt lgkmcnt(0)" ::: "memory")
#define EX(x) __builtin_amdgcn_exp2f(x)

#define NOLOAD 0
#define THR 4
#define KV_AUX 16
#define H_AUX 16
#define FENCE 0
#define ABL 0
#define G1_PRIO 0
#define AUX3_PRIO 2
#define QEARLY 1
#define OUT_AUX 2
#define POLL_SLEEP 2
#define WARM 1
#define PREF 0
#define XEARLY 2
#define XD 4
#define CEARLY 3
#define KV_DEPTH 6
#define AUX_PRIO 2
#define AUX2_PRIO 0
#define GATE 0
#define KV_REPL 32

constexpr u32 WS_EPOCH = 0;
constexpr u32 WS_HFLAG = 7u << 20;
constexpr u32 WS_OFLAG = WS_HFLAG + 131072;
constexpr u32 WS_KVFLAG = WS_OFLAG + 131072;
constexpr u32 WS_AGG = 32768;
constexpr u32 WS_KVF = WS_AGG + 131072;
constexpr u32 WS_OF = WS_KVF + 262144;
constexpr u32 WS_HF = WS_OF + 2097152;
constexpr u32 WS_END = WS_KVFLAG + 32768;
constexpr u32 WS_DBG = 8u << 20;

constexpr u32 L_XB = 0;
constexpr u32 L_WQ = 57344;
constexpr u32 L_C = 107520;
constexpr u32 L_MISC = L_C + 55296;
constexpr u32 L_AGG = L_MISC + 128;
constexpr u32 L_KN = L_MISC + 896;
constexpr u32 L_CARRY = L_C + 20480;
constexpr u32 L_PRIV = 26880;
constexpr u32 L_WO_OFF = 13440;
constexpr u32 L_QX = L_C + 54 * 864;
constexpr u32 LDS_BYTES = 163840;
enum { C_WQ = 0, C_Q2, C_QDONE, C_BFULL, C_AGG, C_WO, C_O, C_MAX, C_P, C_Y, C_FB, C_KVP, C_KMAX, C_KV, C_OF, C_HF, C_WQ2, C_HD, C_KVL, C_KVR, C_KVD, C_C3, C_CARRY, C_EPOCH = 31 };

struct Params {
  const u16 *Ax, *lre, *lim, *Bre, *Bim, *Cre, *Cim, *ldt, *Dsk, *Bx, *wq, *wk, *wv, *wo;
  float* out;
  char* ws;
};

DI float bf2f(u16 v) { return __uint_as_float(((u32)v) << 16); }
DI u32 cvtpk(float a, float b) { return __builtin_bit_cast(u32, __builtin_convertvector((f32x2){a, b}, bf16x2_t)); }
DI float rbf(float a) { return __uint_as_float(cvtpk(a, 0.f) << 16); }
DI bf16x8 frag(u32x4 v) { return __builtin_bit_cast(bf16x8, v); }
DI void glds16(const void* sbase, u32 voff, u32 lds_base) {
  asm volatile("s_mov_b32 m0, %2\n\ts_nop 0\n\tglobal_load_lds_dwordx4 %0, %1" :: "v"(voff), "s"(sbase), "s"(lds_base) : "memory");
}
DI void glds4(const void* sbase, u32 voff, u32 lds_base) {
  asm volatile("s_mov_b32 m0, %2\n\ts_nop 0\n\tglobal_load_lds_dword %0, %1" :: "v"(voff), "s"(sbase), "s"(lds_base) : "memory");
}
template <int N> DI void vmwait() { asm volatile("s_waitcnt vmcnt(%0)" :: "i"(N) : "memory"); }
DI void vmwait_rt(int n) {
  switch (n) {
    case 0: vmwait<0>(); break; case 4: vmwait<4>(); break; case 8: vmwait<8>(); break; case 12: vmwait<12>(); break;
    case 16: vmwait<16>(); break; case 20: vmwait<20>(); break; default: vmwait<0>(); break;
  }
}
DI u32x4 ldsr128(u32 a) { return *(const LAS u32x4*)(size_t)a; }
DI void ldsw128(u32 a, u32x4 v) { *(LAS u32x4*)(size_t)a = v; }
DI u32 ldsu(u32 a) { return (u32)__builtin_amdgcn_readfirstlane(*(volatile LAS u32*)(size_t)a); }
DI u32x2 ldstr(u32 a) { return __builtin_bit_cast(u32x2, __builtin_amdgcn_ds_read_tr16_b64_v4i16((LAS v4i16_t*)(size_t)a)); }
DI u32x4 trfrag(u32 a0, u32 a1) { u32x2 lo = ldstr(a0), hi = ldstr(a1); return (u32x4){lo.x, lo.y, hi.x, hi.y}; }
DI u32x4 f4(const f32x16& v, int g) {
  return (u32x4){__float_as_uint(v[4 * g]), __float_as_uint(v[4 * g + 1]), __float_as_uint(v[4 * g + 2]), __float_as_uint(v[4 * g + 3])};
}
DI void add4(f32x16& v, int g, u32x4 a) {
  v[4 * g] += __uint_as_float(a.x); v[4 * g + 1] += __uint_as_float(a.y); v[4 * g + 2] += __uint_as_float(a.z); v[4 * g + 3] += __uint_as_float(a.w);
}
DI void arrive(int c, int lane) {
  asm volatile("s_waitcnt lgkmcnt(0)" ::: "memory");
  if (lane == 0) asm volatile("ds_add_u32 %0, %1" :: "v"(L_MISC + 4 * c), "v"(1u) : "memory");
}
DI void await(int c, u32 target) {
#pragma unroll 1
  for (u32 spin = 0; spin < (1u << 22); ++spin) {
    asm volatile("" ::: "memory");
    if (ldsu(L_MISC + 4 * c) >= target) break;
    __builtin_amdgcn_s_sleep(1);
  }
  asm volatile("" ::: "memory");
}
DI float swap32(float v, bool hi) {
  auto r = __builtin_amdgcn_permlane32_swap(__float_as_uint(v), __float_as_uint(v), false, false);
  return __uint_as_float(hi ? r[0] : r[1]);
}
DI float wave_max_nonneg(float v) {
  u32 x = __float_as_uint(v);
  x = max(x, (u32)__builtin_amdgcn_update_dpp(0, (int)x, 0x111, 0xf, 0xf, false));
  x = max(x, (u32)__builtin_amdgcn_update_dpp(0, (int)x, 0x112, 0xf, 0xf, false));
  x = max(x, (u32)__builtin_amdgcn_update_dpp(0, (int)x, 0x114, 0xf, 0xf, false));
  x = max(x, (u32)__builtin_amdgcn_update_dpp(0, (int)x, 0x118, 0xf, 0xf, false));
  x = max(x, (u32)__builtin_amdgcn_update_dpp(0, (int)x, 0x142, 0xa, 0xf, false));
  x = max(x, (u32)__builtin_amdgcn_update_dpp(0, (int)x, 0x143, 0xc, 0xf, false));
  return __uint_as_float((u32)__builtin_amdgcn_readlane((int)x, 63));
}
DI void gstore_flag(char* ws, u32 off, u32 v) { __hip_atomic_store((GAS u32*)(ws + off), v, __ATOMIC_RELAXED, __HIP_MEMORY_SCOPE_AGENT); }
DI void poll_words(char* ws, u32 off, int n, u32 tag, int lane) {
  GAS u32* p = (GAS u32*)(ws + off) + (lane < n ? lane : 0) * 32;
  u32 a = __hip_atomic_load(p, __ATOMIC_RELAXED, __HIP_MEMORY_SCOPE_AGENT);
#pragma unroll 1
  for (u32 spin = 0; spin < (1u << 16); ++spin) {
    __builtin_amdgcn_s_sleep(POLL_SLEEP);
    u32 c = __hip_atomic_load(p, __ATOMIC_RELAXED, __HIP_MEMORY_SCOPE_AGENT);
    SBAR();
    if (__all(a == tag)) break;
    __builtin_amdgcn_s_sleep(POLL_SLEEP);
    a = __hip_atomic_load(p, __ATOMIC_RELAXED, __HIP_MEMORY_SCOPE_AGENT);
    SBAR();
    if (__all(c == tag)) break;
  }
  asm volatile("" ::: "memory");
}
DI float poll_kv(char* ws, int rep, u32 tag, int lane) {
  GAS u64* p = (GAS u64*)(ws + WS_KVFLAG + rep * 4096) + (lane & 31) * 16;
  u64 a = __hip_atomic_load(p, __ATOMIC_RELAXED, __HIP_MEMORY_SCOPE_AGENT), g = 0;
#pragma unroll 1
  for (u32 spin = 0; spin < (1u << 16); ++spin) {
    __builtin_amdgcn_s_sleep(POLL_SLEEP);
    u64 c = __hip_atomic_load(p, __ATOMIC_RELAXED, __HIP_MEMORY_SCOPE_AGENT);
    SBAR();
    g = a;
    if (__all((u32)a == tag)) break;
    __builtin_amdgcn_s_sleep(POLL_SLEEP);
    a = __hip_atomic_load(p, __ATOMIC_RELAXED, __HIP_MEMORY_SCOPE_AGENT);
    SBAR();
    g = c;
    if (__all((u32)c == tag)) break;
  }
  asm volatile("" ::: "memory");
  return __uint_as_float((u32)(g >> 32));
}
DI void cmul(float ar, float ai, float br, float bi, float& cr, float& ci) {
  float x, y;
  asm("v_mul_f32 %0, %2, %4\n\tv_mul_f32 %1, %2, %5\n\tv_fma_f32 %0, -%3, %5, %0\n\tv_fma_f32 %1, %3, %4, %1"
      : "=&v"(x), "=&v"(y) : "v"(ar), "v"(ai), "v"(br), "v"(bi));
  cr = x; ci = y;
}
DI void cstep(float ar, float ai, float& hr, float& hi, float ur, float ui) {
  float x, y;
  asm("v_fma_f32 %0, -%3, %5, %6\n\tv_fma_f32 %1, %3, %4, %7\n\tv_fma_f32 %0, %2, %4, %0\n\tv_fma_f32 %1, %2, %5, %1"
      : "=&v"(x), "=&v"(y) : "v"(ar), "v"(ai), "v"(hr), "v"(hi), "v"(ur), "v"(ui));
  hr = x; hi = y;
}
DI void set_pad(u32x4& f, float m, int h) {
  const float a = rbf(-m), b = rbf(-m - a), c = rbf(-m - a - b);
  if (h) { f.z = cvtpk(a, b); f.w = cvtpk(c, 0.f); }
}

#define TS(i) do {} while (0)
#define TSD() do {} while (0)
#define TSW() do {} while (0)
#define LPRIO 0
#define PUP() do {} while (0)
#define PDN() SBAR()
#define STR_(x) #x
#define VMTHR_(n) asm volatile("s_waitcnt vmcnt(" STR_(n) ")" ::: "memory")
#define VMTHR() VMTHR_(THR)

template <bool W_IS_A, int WROW, bool MID, class F>
DI f32x16 proj_tile(int row, int h, u32 wa, int lane, F mid) {
  const int sw = (row >> 1) & 7;
  u32 xa[4];
#pragma unroll
  for (int bb = 0; bb < 4; ++bb) xa[bb] = L_XB + row * 128 + (((2 * bb + h) ^ sw) << 4);
  u32x4 xf[28], wf[28];
  f32x16 acc = {};
#pragma unroll
  for (int ks = 0; ks < 28 + 5; ++ks) {
    if (ks >= 5) {
      const int k = ks - 5;
      acc = W_IS_A ? MFMA(frag(wf[k]), frag(xf[k]), acc) : MFMA(frag(xf[k]), frag(wf[k]), acc);
      SBAR();
    }
    if (ks < 28) {
      if (MID && ks == 14) {
        asm volatile("s_nop 7\n\ts_nop 7" ::: "memory");
        mid();
        VMCNT(0);
        if (lane == 0) asm volatile("ds_add_u32 %0, %1" :: "v"(L_MISC + 4 * C_WQ2), "v"(1u) : "memory");
        await(C_WQ2, 4);
      }
      xf[ks] = ldsr128(xa[ks & 3] + 8192 * (ks >> 2));
      wf[ks] = trfrag(wa + ks * 16 * WROW, wa + ks * 16 * WROW + 4 * WROW);
      SBAR();
    }
  }
  MPIN(acc);
  return acc;
}

__global__ void __launch_bounds__(512, 2) fused_kernel(Params P) {
  extern __shared__ __attribute__((aligned(16))) char lds_dyn[];
  asm volatile("" : "+s"(P.Ax), "+s"(P.lre), "+s"(P.lim), "+s"(P.Bre), "+s"(P.Bim), "+s"(P.Cre), "+s"(P.Cim), "+s"(P.ldt));
  asm volatile("" : "+s"(P.Dsk), "+s"(P.Bx), "+s"(P.wq), "+s"(P.wk), "+s"(P.wv), "+s"(P.wo), "+s"(P.out), "+s"(P.ws));
  const int tid = threadIdx.x, lane = tid & 63, wv = __builtin_amdgcn_readfirstlane(tid >> 6);
  const int r = lane & 31, h = lane >> 5;
  const int b = blockIdx.x;
  const int tb = (b & 7) * 4 + (b >> 6), sl = (b >> 3) & 7;
  char* ws = P.ws;
  const int tl = b >> 6;
  TSD();
  TS(0);
  if (tid == 0) asm volatile("s_mov_b32 m0, %2\n\ts_nop 0\n\tglobal_load_lds_dword %0, %1 sc1" :: "v"(0), "s"(ws), "s"(L_MISC + 4 * C_EPOCH) : "memory");
  const u32 vo = lane * 16;
  {
    const int row = 8 * wv + (lane >> 3);
    const u32 xv = row * 896 + (((lane & 7) ^ ((row >> 1) & 7)) << 4);
    const char* src = (const char*)P.Bx + (size_t)tb * 64 * 896;
#pragma unroll
    for (int j = 0; j < 7; ++j) {
      int c = j + sl; c = c >= 7 ? c - 7 : c;
      glds16(src + c * 128, xv, L_XB + c * 8192 + wv * 1024);
    }
  }
  if (tid < 31) *(LAS u32*)(size_t)(L_MISC + 4 * tid) = 0;
  const auto rs_ws = __builtin_amdgcn_make_buffer_rsrc((void*)ws, 0, WS_END, 0x00020000);
  const auto rs_out = __builtin_amdgcn_make_buffer_rsrc((void*)P.out, 0, 2048 * 896 * 4, 0x00020000);
  const int g1 = (lane >> 4) & 1, q4 = (lane >> 2) & 3, p4 = lane & 3;
  const int l7 = lane / 7;
  const u32 vo7 = l7 * 896 + (lane - 7 * l7) * 16;

  if (wv < 4) {
    const int w = wv;
    const char* wqsrc = (const char*)P.wq + sl * 112 + w * 7 * 7168;
    auto stage_wq = [&](int hf) {
      if (lane < 56) {
#pragma unroll
        for (int j = 0; j < 7; ++j) {
          int i = j + 2 * tl; i = (i >= 7 ? i - 7 : i) + 28 * hf;
          glds16(wqsrc + i * 7168, vo7, L_WQ + (w * 7 + i) * 896);
        }
      }
    };
    if (sl != 7) {
      stage_wq(0); stage_wq(1);
      asm volatile("s_waitcnt vmcnt(14) lgkmcnt(0)\n\ts_barrier" ::: "memory");
    } else {
      stage_wq(0);
      asm volatile("s_waitcnt vmcnt(7) lgkmcnt(0)\n\ts_barrier" ::: "memory");
    }
    const u32 tag = ldsu(L_MISC + 4 * C_EPOCH) + 1u;
    if (sl != 7) VMCNT(7); else VMCNT(0);
    arrive(C_WQ, lane);
    await(C_WQ, 4);
    TS(15);
    {
      f32x16 acc = proj_tile<true, 112, true>(32 * (w & 1) + r, h, L_WQ + (8 * h + q4) * 112 + (w >> 1) * 56 + g1 * 32 + 8 * p4, lane,
                                            [&]() { if (sl == 7) { await(C_KVD, 4); stage_wq(1); } });
      TS(8);
      const float qs = 0.27264060f;
      float n2 = 0.f;
#pragma unroll
      for (int i = 0; i < 16; ++i) { acc[i] *= qs; if (i < 12) n2 = fmaf(acc[i], acc[i], n2); else n2 = h ? n2 : fmaf(acc[i], acc[i], n2); }
      n2 += swap32(n2, h);
      u32x4 f0 = {cvtpk(acc[0], acc[1]), cvtpk(acc[2], acc[3]), cvtpk(acc[4], acc[5]), cvtpk(acc[6], acc[7])};
      u32x4 f1 = {cvtpk(acc[8], acc[9]), cvtpk(acc[10], acc[11]), cvtpk(acc[12], acc[13]), cvtpk(acc[14], acc[15])};
      if (h) { f1.z = 0; f1.w = __float_as_uint(__builtin_amdgcn_sqrtf(n2)); }
      ldsw128(L_QX + w * 2048 + vo, f0);
      ldsw128(L_QX + w * 2048 + 1024 + vo, f1);
      arrive(C_Q2, lane);
    }
    u32x4 qf[4][2];
    if (QEARLY) {
      await(C_Q2, 4);
#pragma unroll
      for (int t = 0; t < 4; ++t) { const u32 a = L_QX + ((w + t) & 3) * 2048 + vo; qf[t][0] = ldsr128(a); qf[t][1] = ldsr128(a + 1024); }
      arrive(C_QDONE, lane);
    }
    TS(2);
    if (w == 0) {
      const float kn = wave_max_nonneg(poll_kv(ws, b & 7, tag, lane));
      if (lane == 0) *(LAS float*)(size_t)L_KN = kn;
      arrive(C_KV, lane);
    } else {
      await(C_KV, 1);
    }
    TS(3);
    const u32 kvbase = WS_KVF + w * 65536;
    const int rot = b >> 3;
#define KVOFF(it) (kvbase + ((((it) + rot) & 15) << 12))
#define LDK(it, s) __builtin_amdgcn_raw_buffer_load_b128(rs_ws, vo + 1024 * (s), KVOFF(it), KV_AUX)
#define LDV(it, s) __builtin_amdgcn_raw_buffer_load_b128(rs_ws, vo + 2048 + 1024 * (s), KVOFF(it), KV_AUX)
#define NEXT(x) asm volatile("s_add_u32 %0, %0, 0x1000\n\ts_and_b32 %0, %0, 0xf000" : "+s"(x) :: "scc")
#define LDK2(s_) __builtin_amdgcn_raw_buffer_load_b128(rs_kv, vo + 1024 * (s_), koff, KV_AUX)
#define LDV2(s_) __builtin_amdgcn_raw_buffer_load_b128(rs_kv, vo + 2048 + 1024 * (s_), voff, KV_AUX)
    const auto rs_kv = __builtin_amdgcn_make_buffer_rsrc((void*)(ws + kvbase), 0, 65536, 0x00020000);
    f32x16 o[4];
    for (int attempt = 0; attempt < 2; ++attempt) {
      u32x4 kf[KV_DEPTH][2], vf[KV_DEPTH][2];
#pragma unroll
      for (int d = 0; d < KV_DEPTH; ++d) {
        kf[d][0] = LDK(d, 0); kf[d][1] = LDK(d, 1);
        if (d < KV_DEPTH - 1) { vf[d][0] = LDV(d, 0); vf[d][1] = LDV(d, 1); }
      }
      u32 koff = ((KV_DEPTH - 1 + rot) & 15) << 12, voff = ((KV_DEPTH - 2 + rot) & 15) << 12;
      SBAR();
      if (attempt == 0) {
        arrive(C_KVL, lane);
        if (!QEARLY) {
        await(C_Q2, 4);
#pragma unroll
        for (int t = 0; t < 4; ++t) { const u32 a = L_QX + ((w + t) & 3) * 2048 + vo; qf[t][0] = ldsr128(a); qf[t][1] = ldsr128(a + 1024); }
        arrive(C_QDONE, lane);
        }
        const float kmax = __builtin_amdgcn_sqrtf(*(LAS float*)(size_t)L_KN);
#pragma unroll
        for (int t = 0; t < 4; ++t) set_pad(qf[t][1], __uint_as_float(qf[t][1].w) * kmax, h);
      }
#pragma unroll
      for (int t = 0; t < 4; ++t) { PIN(qf[t][0]); PIN(qf[t][1]); }
      SBAR();
      const f32x16 zero = {};
      f32x16 sc = MFMA(frag(kf[0][0]), frag(qf[0][0]), zero);
      sc = MFMA(frag(kf[0][1]), frag(qf[0][1]), sc);
      TS(9);
      float e0 = 0.f, e1 = 0.f, e2 = 0.f, e3 = 0.f;
      u32x4 p1 = {0, 0, 0, 0};
#pragma unroll
      for (int it = 0; it < 16; ++it) {
        const int d = it % KV_DEPTH, dprev = (it + KV_DEPTH - 1) % KV_DEPTH;
#pragma unroll
        for (int qi = 0; qi < 4; ++qi) {
          const int dn = qi == 3 ? (it + 1) % KV_DEPTH : d, qn = (qi + 1) & 3;
          const int dp = qi == 0 ? dprev : d, qp = (qi + 3) & 3;
          const bool last = it == 15 && qi == 3;
          f32x16 sn; u32x4 p0;
          float c0, c1, c2, c3;
          if (!last) sn = MFMA(frag(kf[dn][0]), frag(qf[qn][0]), zero);
          SBAR();
          p1.z = cvtpk(e0, e1); p1.w = cvtpk(e2, e3); PIN(p1); SBAR();
          c0 = EX(sc[0]); c1 = EX(sc[1]); c2 = EX(sc[2]); c3 = EX(sc[3]); PIN4(c0, c1, c2, c3); SBAR();
          if (it > 0 || qi > 0) o[qp] = MFMA(frag(vf[dp][1]), frag(p1), o[qp]);
          SBAR();
          p0.x = cvtpk(c0, c1); p0.y = cvtpk(c2, c3); PIN2(p0.x, p0.y); SBAR();
          c0 = EX(sc[4]); c1 = EX(sc[5]);
          if (!NOLOAD && qi < 2 && it - 1 + KV_DEPTH < 16) { PIN2(c0, c1); SBAR(); if (qi == 0) NEXT(voff); vf[dprev][qi] = LDV2(qi); SBAR(); }
          c2 = EX(sc[6]); c3 = EX(sc[7]); PIN4(c0, c1, c2, c3); SBAR();
          if (!last) sn = MFMA(frag(kf[dn][1]), frag(qf[qn][1]), sn);
          SBAR();
          p0.z = cvtpk(c0, c1); p0.w = cvtpk(c2, c3); PIN(p0); SBAR();
          c0 = EX(sc[8]); c1 = EX(sc[9]);
          if (!NOLOAD && qi == 2 && it + KV_DEPTH < 16) { PIN2(c0, c1); SBAR(); NEXT(koff); kf[d][0] = LDK2(0); SBAR(); }
          c2 = EX(sc[10]); c3 = EX(sc[11]); PIN4(c0, c1, c2, c3); SBAR();
          o[qi] = MFMA(frag(vf[d][0]), frag(p0), it == 0 ? zero : o[qi]); SBAR();
          p1.x = cvtpk(c0, c1); p1.y = cvtpk(c2, c3); PIN2(p1.x, p1.y); SBAR();
          e0 = EX(sc[12]); e1 = EX(sc[13]);
          if (!NOLOAD && qi == 2 && it + KV_DEPTH < 16) { PIN2(e0, e1); SBAR(); kf[d][1] = LDK2(1); SBAR(); }
          e2 = EX(sc[14]); e3 = EX(sc[15]); PIN4(e0, e1, e2, e3); SBAR();
          if (!last) sc = sn;
        }
      }
      p1.z = cvtpk(e0, e1); p1.w = cvtpk(e2, e3);
      o[3] = MFMA(frag(vf[15 % KV_DEPTH][1]), frag(p1), o[3]);
      MPIN4(o[0], o[1], o[2], o[3]);
      TS(4);
      u32 lv;
      asm volatile("v_mbcnt_lo_u32_b32 %0, -1, 0\n\tv_mbcnt_hi_u32_b32 %0, -1, %0\n\tv_lshlrev_b32 %0, 4, %0" : "=v"(lv));
      if (attempt == 0) {
        bool bad = false;
#pragma unroll
        for (int t = 0; t < 4; ++t) bad |= !(o[t][12] >= 0x1p-80f && o[t][12] <= 0x1p100f);
        if (__any(bad && lv >= 512)) { if (lv == 0) *(LAS u32*)(size_t)(L_MISC + 4 * C_FB) = 1u; }
        await(C_HD, 4);
      }
#pragma unroll
      for (int t = 1; t < 4; ++t) {
#pragma unroll
        for (int g = 0; g < 4; ++g) ldsw128(L_C + w * 12288 + (t - 1) * 4096 + g * 1024 + lv, f4(o[t], g));
      }
      arrive(C_O, lv);
      await(C_O, 4 * (attempt + 1));
      TS(10);
      const u32 fb = attempt == 0 ? ldsu(L_MISC + 4 * C_FB) : 0u;
      if (!fb) break;
      {
        float mx[4];
#pragma unroll
        for (int t = 0; t < 4; ++t) { mx[t] = -3.0e38f; if (lv >= 512) { qf[t][1].z = 0; qf[t][1].w = 0; } }
#pragma unroll 1
        for (int it = 0; it < 16; ++it) {
          const u32x4 k0 = __builtin_amdgcn_raw_buffer_load_b128(rs_kv, lv, it << 12, KV_AUX), k1 = __builtin_amdgcn_raw_buffer_load_b128(rs_kv, lv + 1024, it << 12, KV_AUX);
#pragma unroll
          for (int t = 0; t < 4; ++t) {
            f32x16 s = MFMA(frag(k0), frag(qf[t][0]), zero);
            s = MFMA(frag(k1), frag(qf[t][1]), s);
#pragma unroll
            for (int i = 0; i < 16; ++i) mx[t] = fmaxf(mx[t], s[i]);
          }
        }
        const u32 ma = L_C + 49152;
#pragma unroll
        for (int t = 0; t < 4; ++t) { mx[t] = fmaxf(mx[t], swap32(mx[t], lv >= 512)); *(LAS float*)(size_t)(ma + (w * 4 + ((w + t) & 3)) * 256 + (lv >> 2)) = mx[t]; }
        arrive(C_MAX, lv);
        await(C_MAX, 4);
#pragma unroll
        for (int t = 0; t < 4; ++t) {
          float m = -3.0e38f;
#pragma unroll
          for (int ww = 0; ww < 4; ++ww) m = fmaxf(m, *(LAS float*)(size_t)(ma + (ww * 4 + ((w + t) & 3)) * 256 + (lv >> 2)));
          set_pad(qf[t][1], m, lv >= 512);
        }
      }
    }
    {
    int lane;
    asm volatile("v_mbcnt_lo_u32_b32 %0, -1, 0\n\tv_mbcnt_hi_u32_b32 %0, -1, %0" : "=v"(lane));
    const int r = lane & 31, h = lane >> 5, g1 = (lane >> 4) & 1, q4 = (lane >> 2) & 3, p4 = lane & 3;
    const u32 vo = lane * 16;
    {
      f32x16 own = o[0];
#pragma unroll
      for (int pp = 1; pp < 4; ++pp) {
        const u32 a = L_C + ((w + pp) & 3) * 12288 + (3 - pp) * 4096 + vo;
#pragma unroll
        for (int g = 0; g < 4; ++g) add4(own, g, ldsr128(a + g * 1024));
      }
      const float other = swap32(own[12], h);
      const float inv = __builtin_amdgcn_rcpf(h ? own[12] : other);
      u32x4 f0 = {cvtpk(own[0] * inv, own[1] * inv), cvtpk(own[2] * inv, own[3] * inv), cvtpk(own[4] * inv, own[5] * inv), cvtpk(own[6] * inv, own[7] * inv)};
      u32x4 f1 = {cvtpk(own[8] * inv, own[9] * inv), cvtpk(own[10] * inv, own[11] * inv), cvtpk(own[12] * inv, own[13] * inv), cvtpk(own[14] * inv, own[15] * inv)};
      if (h) { f1.z = 0; f1.w = 0; }
      const u32 so = WS_OF + ((tb * 2 + (w & 1)) * 16 + 2 * sl + (w >> 1)) * 2048 + vo;
      __builtin_amdgcn_raw_buffer_store_b128(f0, rs_ws, so, 0, 16);
      __builtin_amdgcn_raw_buffer_store_b128(f1, rs_ws, so + 1024, 0, 16);
    }
    {
      const int tm = w & 1, kh = w >> 1;
      TS(11);
      VMCNT(0);
      if (lane == 0) gstore_flag(ws, WS_OFLAG + (tb * 32 + sl * 4 + w) * 128, tag);
      TS(5);
      await(C_WO, 4);
      u32x4 wf[16][2];
      const u32 wa = 2 * kh * L_PRIV + L_WO_OFF + (4 * h + q4) * 112 + g1 * 32 + 8 * p4;
      const u32 wa0 = wa + 64 * kh, wa1 = wa + 64 * (1 - kh);
#pragma unroll
      for (int i = 0; i < 16; ++i) {
        const u32 a = (i >> 3) * L_PRIV + (28 * ((i >> 1) & 3) + 16 * (i & 1)) * 112;
        wf[i][0] = trfrag(wa0 + a, wa0 + a + 896); wf[i][1] = trfrag(wa1 + a, wa1 + a + 896);
      }
#pragma unroll
      for (int i = 0; i < 16; ++i) PIN2(wf[i][0], wf[i][1]);
      if (w == 0) { poll_words(ws, WS_OFLAG + tb * 4096, 32, tag, lane); arrive(C_OF, lane); }
      else await(C_OF, 1);
      TS(6);
      u32x4 of[16];
      const u32 so = WS_OF + ((tb * 2 + tm) * 16 + 8 * kh) * 2048;
#pragma unroll
      for (int i = 0; i < 16; ++i) of[i] = __builtin_amdgcn_raw_buffer_load_b128(rs_ws, vo + (i & 3) * 1024, so + (i >> 2) * 4096, H_AUX);
      SBAR();
      f32x16 a0 = {}, a1 = {};
#pragma unroll
      for (int i = 0; i < 16; ++i) {
        a0 = MFMA(frag(of[i]), frag(wf[i][0]), a0);
        a1 = MFMA(frag(of[i]), frag(wf[i][1]), a1);
      }
      MPIN2(a0, a1);
      TS(12);
      const f32x16 snd = a1;
      f32x16 own = a0;
      const u32 aw = L_C + w * 4096 + vo, ar = L_C + (w ^ 2) * 4096 + vo;
#pragma unroll
      for (int g = 0; g < 4; ++g) ldsw128(aw + g * 1024, f4(snd, g));
      arrive(C_P, lane);
      await(C_P, 4);
#pragma unroll
      for (int g = 0; g < 4; ++g) add4(own, g, ldsr128(ar + g * 1024));
      if (32 * kh + r < 56) {
        const u32 ov = ((tb * 64 + 32 * tm + 4 * h) * 896 + 448 + 56 * sl + 32 * kh + r) * 4;
#pragma unroll
        for (int i = 0; i < 16; ++i) __builtin_amdgcn_raw_buffer_store_b32(__float_as_uint(rbf(own[i])), rs_out, ov, ((i & 3) + 8 * (i >> 2)) * 3584, OUT_AUX);
      }
    }
    }
    TS(7);
    TSW();
  } else {
    __builtin_amdgcn_s_setprio(AUX_PRIO);
    const int q = wv - 4;
    const u32 PA = q * L_PRIV;
    const int part = q >> 1, half = q & 1;
    if (sl == 7 && lane < 56) {
      const char* src = (const char*)((q >> 1) ? P.wv : P.wk) + (q & 1) * 14 * 896;
#pragma unroll
      for (int j = 0; j < 14; ++j) {
        int i = j + 4 * tl; i = i >= 14 ? i - 14 : i;
        glds16(src + i * 896, vo, L_C + (q * 14 + i) * 896);
      }
    }
    asm volatile("s_waitcnt vmcnt(0) lgkmcnt(0)\n\ts_barrier" ::: "memory");
    const u32 tag = ldsu(L_MISC + 4 * C_EPOCH) + 1u;
    TS(13);
    if (sl == 7) {
      const int kind = q >> 1, ktm = q & 1;
      const u32 wa = L_C + kind * 25088 + (8 * h + q4) * 56 + g1 * 32 + 8 * p4;
      f32x16 acc;
      if (kind == 0) acc = proj_tile<true, 56, false>(32 * ktm + r, h, wa, lane, []() {});
      else acc = proj_tile<false, 56, false>(32 * ktm + r, h, wa, lane, []() {});
      u32x4 f0 = {cvtpk(acc[0], acc[1]), cvtpk(acc[2], acc[3]), cvtpk(acc[4], acc[5]), cvtpk(acc[6], acc[7])};
      u32x4 f1 = {cvtpk(acc[8], acc[9]), cvtpk(acc[10], acc[11]), cvtpk(acc[12], acc[13]), cvtpk(acc[14], acc[15])};
      if (kind == 0) {
        if (h) { f1.z = 0x3F803F80u; f1.w = 0x00003F80u; }
      } else if (r >= 28) {
        const u32 c = r == 28 ? 0x3F803F80u : 0u;
        f0 = (u32x4){c, c, c, c}; f1 = f0;
      }
      const u32 so = WS_KVF + (tb * 2 + ktm) * 4096 + kind * 2048 + vo;
      __builtin_amdgcn_raw_buffer_store_b128(f0, rs_ws, so, 0, 16);
      __builtin_amdgcn_raw_buffer_store_b128(f1, rs_ws, so + 1024, 0, 16);
      SBAR();
      TS(12);
      if (kind == 0) {
        float n2 = 0.f;
#pragma unroll
        for (int i = 0; i < 16; ++i) { if (i < 12) n2 = fmaf(acc[i], acc[i], n2); else n2 = h ? n2 : fmaf(acc[i], acc[i], n2); }
        n2 += swap32(n2, h);
        n2 = wave_max_nonneg(n2);
        if (lane == 0) asm volatile("ds_max_u32 %0, %1" :: "v"(L_MISC + 4 * C_KMAX), "v"(n2) : "memory");
      }
      VMCNT(0);
      u32 old = 0;
      if (lane == 0) asm volatile("s_waitcnt lgkmcnt(0)\n\tds_add_rtn_u32 %0, %1, %2\n\ts_waitcnt lgkmcnt(0)" : "=&v"(old) : "v"(L_MISC + 4 * C_KVP), "v"(1u) : "memory");
      if (__builtin_amdgcn_readfirstlane(old) == 3) {
        const u32 km = ldsu(L_MISC + 4 * C_KMAX);
        if (lane < 8) __hip_atomic_store((GAS u64*)(ws + WS_KVFLAG + lane * 4096 + tb * 128), (u64)tag | ((u64)km << 32), __ATOMIC_RELAXED, __HIP_MEMORY_SCOPE_AGENT);
      }
      TS(14);
      arrive(C_KVD, lane);
      await(C_KVD, 4);
    } else if (PREF) {
      const int j = (b >> 3) - tl;
      const char* p0 = (const char*)(q == 0 ? P.Bre : q == 1 ? P.Bim : q == 2 ? P.Cre : P.Cim) + j * 14336;
      const u32 o2 = lane < 48 ? 8192 + lane * 128 : 0;
      glds4(p0, lane * 128, L_AGG + 512);
      glds4(p0, o2, L_AGG + 512);
      if (q == 0) glds4((const char*)P.Ax + (size_t)(b & 7) * 229376 + j * 8192, lane * 128, L_AGG + 512);
      if (q == 1) glds4((const char*)P.wo + j * 14336, lane * 128, L_AGG + 512);
      if (q == 2) glds4((const char*)P.wo + j * 14336, o2, L_AGG + 512);
    }

    u32 cva;
    {
      const int hi = lane >= 28, cs = lane - 28 * hi;
      cva = hi * 896 + (cs << 4);
    }
    auto stage_C4 = [&](int qq, int g) {
      const char* csrc = (const char*)((qq >> 1) ? P.Cim : P.Cre) + (size_t)sl * 56 * 896 + (qq & 1) * 448;
      int p0 = 4 * g + 8 * tl; p0 = p0 >= 28 ? p0 - 28 : p0;
      const u32 va = cva ^ ((p0 & 4) << 3), vb = va ^ 16;
      if (lane < 56) {
#pragma unroll
        for (int j = 0; j < 4; ++j) { glds16(csrc + (p0 + j) * 1792, j < 2 ? va : vb, qq * L_PRIV + (p0 + j) * 896); VMTHR(); }
      }
    };
    const int tm = (q ^ sl) & 1, tn = q >> 1;
    const bool valid = 32 * tn + r < 56;
    const int dcol = 56 * sl + (valid ? 32 * tn + r : 55), t0 = tb * 64 + 32 * tm + 4 * h;
    if (WARM && q == 3) {
      const int k = lane & 7;
      const u32 o1 = k < 4 ? WS_KVF + k * 65536 : k < 6 ? WS_OF + tb * 65536 + (k - 4) * 65024 : k == 6 ? WS_HFLAG : WS_AGG + (((tb & 1) * 7 + sl) & 15) * 8192;
      const u32 o2 = WS_HF + (tb & ~1) * 114688 + (k & 3) * 65536;
      if (lane < 16) {
        asm volatile("s_mov_b32 m0, %2\n\ts_nop 0\n\tglobal_load_lds_dword %0, %1 sc1" :: "v"(lane < 8 ? o1 : o2), "s"(ws), "s"(L_AGG + 512) : "memory");
        glds4((const char*)P.out + (size_t)tb * 229376, (lane & 3) * 65536, L_AGG + 512);
      }
    }
    u32x4 cf[2][14];
    auto read_C = [&]() {
#pragma unroll
      for (int t = 0; t < 2; ++t) {
        int row = 32 * (t ^ part) + r; row = row > 55 ? 55 : row;
        const int x = (row >> 2) & 3;
        const u32 a0 = PA + row * 448 + ((h ^ x) << 4), a1 = PA + row * 448 + (((2 + h) ^ x) << 4);
#pragma unroll
        for (int j = 0; j < 14; ++j) cf[t][j] = ldsr128(((j & 1) ? a1 : a0) + 64 * (j >> 1));
      }
#pragma unroll
      for (int j = 0; j < 14; ++j) PIN2(cf[0][j], cf[1][j]);
      LGKM0();
    };
    if (sl == 7) {
      arrive(C_HD, lane);
      await(C_KVL, 4);
#pragma unroll 1
      for (int g = 0; g < 7; ++g) stage_C4(q, g);
      VMCNT(0);
      read_C();
    } else {
      const int tb2 = tb >> 1, cg = (tb & 1) * 7 + sl, ch = 32 * cg + r;
      const u32 vo1 = (lane ^ 1) * 16;
      float lr, li, cr, ci;
      if (q < 3 && lane < 54) {
#pragma unroll
        for (int j = 0; j < 18; ++j) {
          int i = j + 9 * (tb2 & 1); i = i >= 18 ? i - 18 : i;
          const int row = q * 18 + i;
          glds16((const char*)(row >= 32 ? P.Bim : P.Bre) + (size_t)(cg * 32 + (row & 31)) * 896, (row & 8) ? vo1 : vo, L_C + row * 864);
        }
      }
      u32 vlre = P.lre[ch], vlim = P.lim[ch], vldt = P.ldt[ch];
      u32x4 blast_re = *(const u32x4*)(P.Bre + (size_t)ch * 448 + 432 + 8 * h);
      u32x4 blast_im = *(const u32x4*)(P.Bim + (size_t)ch * 448 + 432 + 8 * h);
      asm volatile("" : "+v"(vlre), "+v"(vlim), "+v"(vldt), "+v"(blast_re), "+v"(blast_im) :: "memory");
      {
        const float dt = rbf(expf(__uint_as_float(vldt << 16))), a = __uint_as_float(vlre << 16), bb = __uint_as_float(vlim << 16);
        const float er = expf(dt * a);
        float sn, cs;
        sincosf(dt * bb, &sn, &cs);
        lr = er * cs; PIN(lr); li = er * sn;
        const float nr = lr - 1.f, ni = li, den = 2.0f / (a * a + bb * bb);
        cr = (nr * a + ni * bb) * den; PIN(cr); ci = (ni * a - nr * bb) * den;
      }
      float l16r = lr, l16i = li;
#pragma unroll
      for (int i = 0; i < 4; ++i) cmul(l16r, l16i, l16r, l16i, l16r, l16i);
      float l32r, l32i, l128r, l128i;
      cmul(l16r, l16i, l16r, l16i, l32r, l32i);
      cmul(l32r, l32i, l32r, l32i, l128r, l128i);
      cmul(l128r, l128i, l128r, l128i, l128r, l128i);
      PIN4(lr, li, cr, ci); PIN4(l16r, l16i, l32r, l32i); PIN2(l128r, l128i);
      const char* xsrc = (const char*)P.Ax + (size_t)(tb2 * 128 + q * 32) * 896;
      const u32 xv0 = (lane >> 3) * 896 + (((lane & 7) ^ (lane >> 4)) << 4), xv1 = xv0 ^ 64;
      const int rho = 16 * ((r >> 2) & 1) + (r & 3) + 4 * (r >> 3), sw = (rho >> 1) & 7;
      u32 xa[4];
#pragma unroll
      for (int bb = 0; bb < 4; ++bb) xa[bb] = PA + rho * 128 + (((2 * bb + h) ^ sw) << 4);
      const int xrot = cg % 6;
      u32 ba[7];
#pragma unroll
      for (int c = 0; c < 7; ++c) {
        int kc = c + xrot; kc = c == 6 ? 6 : kc >= 6 ? kc - 6 : kc;
        ba[c] = L_C + r * 864 + ((h ^ ((r >> 3) & 1)) << 4) + 128 * kc;
        PIN(ba[c]);
      }
      PIN4(xa[0], xa[1], xa[2], xa[3]);
      TS(1);
      await(C_QDONE, 4);
      if (q == 3 && lane < 54) {
#pragma unroll
        for (int i = 22; i < 32; ++i) glds16((const char*)P.Bim + (size_t)(cg * 32 + i) * 896, (i & 8) ? vo1 : vo, L_C + (32 + i) * 864);
      }
      auto xdma = [&](int c) {
#pragma unroll
        for (int j = 0; j < 4; ++j) {
          int kc = c + xrot; kc = c == 6 ? 6 : kc >= 6 ? kc - 6 : kc;
          glds16(xsrc + kc * 128 + j * 7168, (j & 1) ? xv1 : xv0, PA + (c % 6) * 4096 + j * 1024);
        }
      };
      xdma(0); xdma(1);
      vmwait<8>();
      arrive(C_BFULL, lane);
      await(C_BFULL, 4);
      TS(2);
      f32x16 ur = {}, ui = {};
      PIN2(ur, ui);
      __builtin_amdgcn_s_setprio(G1_PRIO);
      {
        u32x4 xf[28], br[28], bi[28];
#pragma unroll
        for (int ks = 0; ks < 28 + 4; ++ks) {
          if (ks >= 4) {
            const int k = ks - 4;
            ur = MFMA(frag(xf[k]), frag(br[k]), ur);
            ui = MFMA(frag(xf[k]), frag(bi[k]), ui);
            SBAR();
          }
          if (ks < 28) {
            if ((ks & 3) == 0) {
              const int c = ks >> 2;
              const int issued = c < 2 ? 1 : (XD + c - 2 > 6 ? 6 : XD + c - 2);
              vmwait_rt(4 * (issued - c));
            }
            if (ks == 7) {
              asm volatile("s_nop 7\n\ts_nop 7" ::: "memory");
              await(C_KVL, 4);
#pragma unroll
              for (int c = 2; c <= XD; ++c) xdma(c);
            }
            if ((ks & 3) == 3 && ks > 7 && XD + (ks >> 2) - 1 <= 6) xdma(XD + (ks >> 2) - 1);
            xf[ks] = ldsr128(xa[ks & 3] + ((ks >> 2) % 6) * 4096);
            br[ks] = ks < 27 ? ldsr128(ba[ks >> 2] + 32 * (ks & 3)) : blast_re;
            bi[ks] = ks < 27 ? ldsr128(ba[ks >> 2] + 27648 + 32 * (ks & 3)) : blast_im;
            SBAR();
          }
        }
      }
      MPIN2(ur, ui);
      __builtin_amdgcn_s_setprio(AUX_PRIO);
      TS(3);
      float ar = 0.f, ai = 0.f;
#pragma unroll
      for (int i = 0; i < 16; ++i) {
        float tr, ti;
        cmul(cr, ci, ur[i], ui[i], tr, ti);
        ur[i] = tr; ui[i] = ti;
        cstep(lr, li, ar, ai, tr, ti);
      }
      const float or_ = swap32(ar, h), oi_ = swap32(ai, h);
      const float lor = h ? or_ : ar, loi = h ? oi_ : ai;
      float a32r = lor, a32i = loi;
      cstep(l16r, l16i, a32r, a32i, h ? ar : or_, h ? ai : oi_);
      if (q < 3 && h == 0) *(LAS f32x2*)(size_t)(L_AGG + q * 256 + r * 8) = (f32x2){a32r, a32i};
      arrive(C_AGG, lane);
      await(C_AGG, 4);
      f32x2 wagg[3];
#pragma unroll
      for (int j = 0; j < 3; ++j) wagg[j] = *(LAS f32x2*)(size_t)(L_AGG + j * 256 + r * 8);
      const u32 aggb = WS_AGG + cg * 8192;
      if (q == 3 && tb2 < 15) {
        float tr = wagg[0].x, ti = wagg[0].y;
        cstep(l32r, l32i, tr, ti, wagg[1].x, wagg[1].y);
        cstep(l32r, l32i, tr, ti, wagg[2].x, wagg[2].y);
        cstep(l32r, l32i, tr, ti, a32r, a32i);
        __hip_atomic_store((GAS u64*)(ws + aggb + tb2 * 512 + r * 16 + h * 8), (u64)__float_as_uint(h ? ti : tr) | ((u64)tag << 32), __ATOMIC_RELAXED, __HIP_MEMORY_SCOPE_AGENT);
      }
      TS(4);
      float hr = 0.f, hi = 0.f;
      if (q < 3) {
#pragma unroll 1
        for (int g = q; g < 7; g += 3) stage_C4(3, g);
        stage_C4(q, 0); stage_C4(q, 1);
        vmwait<8>();
        arrive(C_C3, lane);
#pragma unroll 1
        for (int g = 2; g < 7; ++g) stage_C4(q, g);
        VMCNT(0);
        read_C();
        TS(10);
        await(C_CARRY, 1);
        const f32x2 cv = *(LAS f32x2*)(size_t)(L_CARRY + r * 8);
        hr = cv.x; hi = cv.y;
      } else {
        await(C_C3, 3);
        read_C();
        if (tb2 > 0) {
          u32x4 g[8];
          u32 gv[8];
#pragma unroll
          for (int j = 0; j < 8; ++j) gv[j] = r * 16 + ((8 * h + j < tb2) ? (8 * h + j) * 512 : 0);
#pragma unroll 1
          for (u32 spin = 0; spin < (1u << 17); ++spin) {
            asm volatile("" ::: "memory");
            u32 bad = 0;
#pragma unroll
            for (int j = 0; j < 8; ++j) g[j] = __builtin_amdgcn_raw_buffer_load_b128(rs_ws, gv[j], aggb, 16);
#pragma unroll
            for (int j = 0; j < 8; ++j) bad |= (g[j].y ^ tag) | (g[j].w ^ tag);
            if (__all(bad == 0)) break;
            __builtin_amdgcn_s_sleep(4);
          }
          asm volatile("" ::: "memory");
#pragma unroll
          for (int j = 0; j < 8; ++j)
            if (j < tb2) cstep(l128r, l128i, hr, hi, __uint_as_float(g[j].x), __uint_as_float(g[j].z));
          const float sr = swap32(hr, h), si = swap32(hi, h);
          if (h) { hr = sr; hi = si; }
          if (tb2 > 8) {
#pragma unroll
            for (int j = 0; j < 7; ++j)
              if (8 + j < tb2) cstep(l128r, l128i, hr, hi, __uint_as_float(g[j].x), __uint_as_float(g[j].z));
            const float tr = swap32(hr, h), ti = swap32(hi, h);
            if (!h) { hr = tr; hi = ti; }
          }
        }
        if (h == 0) *(LAS f32x2*)(size_t)(L_CARRY + r * 8) = (f32x2){hr, hi};
        arrive(C_CARRY, lane);
        TS(10);
      }
#pragma unroll
      for (int j = 0; j < 3; ++j)
        if (j < q) cstep(l32r, l32i, hr, hi, wagg[j].x, wagg[j].y);
      if (h) cstep(l16r, l16i, hr, hi, lor, loi);
      TS(5);
      u32 pr[8], pi[8];
#pragma unroll
      for (int i = 0; i < 16; i += 2) {
        cstep(lr, li, hr, hi, ur[i], ui[i]);
        const float r0 = hr, i0 = hi;
        cstep(lr, li, hr, hi, ur[i + 1], ui[i + 1]);
        pr[i >> 1] = cvtpk(r0, hr); pi[i >> 1] = cvtpk(-i0, -hi);
      }
      const u32 ta = L_C + q * 5120 + r * 80 + 32 * h;
      ldsw128(ta, (u32x4){pr[0], pr[1], pr[2], pr[3]}); ldsw128(ta + 16, (u32x4){pr[4], pr[5], pr[6], pr[7]});
      ldsw128(ta + 2560, (u32x4){pi[0], pi[1], pi[2], pi[3]}); ldsw128(ta + 2560 + 16, (u32x4){pi[4], pi[5], pi[6], pi[7]});
      LGKM0();
      const u32 tr = L_C + q * 5120 + (8 * h + q4) * 80 + g1 * 32 + 8 * p4;
      const int tbh = 2 * tb2 + (q >> 1);
      const u32 so = WS_HF + ((tbh * 2 + (q & 1)) * 56 + 2 * cg) * 1024 + vo;
#pragma unroll
      for (int pt = 0; pt < 2; ++pt)
#pragma unroll
        for (int s = 0; s < 2; ++s) {
          const u32 a = tr + pt * 2560 + s * 1280;
          __builtin_amdgcn_raw_buffer_store_b128(trfrag(a, a + 320), rs_ws, so + pt * 28 * 1024 + s * 1024, 0, 16);
        }
      arrive(C_HD, lane);
      VMCNT(0);
      if (lane == 0) gstore_flag(ws, WS_HFLAG + (tbh * 32 + cg * 2 + (q & 1)) * 128, tag);
      TS(6);
    }
    TS(9);
    if (lane < 56) {
      const char* src = (const char*)P.wo + sl * 112 + q * 14 * 7168;
#pragma unroll
      for (int j = 0; j < 15; ++j) {
        int i = j + 4 * tl; i = i >= 15 ? i - 15 : i;
        glds16(src + ((i == 14 && q == 3) ? 13 : i) * 7168, vo7, PA + L_WO_OFF + 896 * i); VMTHR();
      }
    }
    VMCNT(0);
    arrive(C_WO, lane);
    TS(11);
    if (q == 0) { poll_words(ws, WS_HFLAG + tb * 4096, 28, tag, lane); arrive(C_HF, lane); }
    else await(C_HF, 1);
    TS(7);
    {
      f32x16 y[4];
      const u32 so = WS_HF + ((tb * 2 + tm) * 56 + part * 28 + half * 14) * 1024, so1 = WS_HF + ((tb * 2 + (tm ^ 1)) * 56 + part * 28 + half * 14) * 1024;
      u32x4 hf[14];
#pragma unroll
      for (int j = 0; j < 14; ++j) hf[j] = __builtin_amdgcn_raw_buffer_load_b128(rs_ws, vo + (j & 3) * 1024, so + (j >> 2) * 4096, H_AUX);
      SBAR();
#pragma unroll
      for (int t = 0; t < 4; ++t) y[t] = (f32x16){};
      PIN4(y[0], y[1], y[2], y[3]);
      __builtin_amdgcn_s_setprio(AUX2_PRIO);
#pragma unroll
      for (int j = 0; j < 14; ++j) {
        y[0] = MFMA(frag(hf[j]), frag(cf[0][j]), y[0]);
        y[2] = MFMA(frag(hf[j]), frag(cf[1][j]), y[2]);
        SBAR();
        hf[j] = __builtin_amdgcn_raw_buffer_load_b128(rs_ws, vo + (j & 3) * 1024, so1 + (j >> 2) * 4096, H_AUX);
        SBAR();
      }
#pragma unroll
      for (int j = 0; j < 14; ++j) {
        y[1] = MFMA(frag(hf[j]), frag(cf[0][j]), y[1]);
        y[3] = MFMA(frag(hf[j]), frag(cf[1][j]), y[3]);
      }
      u16 xs[16];
      {
        const auto rs_ax = __builtin_amdgcn_make_buffer_rsrc((void*)P.Ax, 0, 2048 * 896, 0x00020000);
        const u32 xv = (t0 * 448 + dcol) * 2;
#pragma unroll
        for (int i = 0; i < 16; ++i) xs[i] = __builtin_amdgcn_raw_buffer_load_b16(rs_ax, xv + (i & 3) * 896, (i >> 2) * 7168, 0);
      }
      const u16 dsk16 = P.Dsk[dcol];
      MPIN4(y[0], y[1], y[2], y[3]);
      __builtin_amdgcn_s_setprio(AUX3_PRIO);
#pragma unroll
      for (int t = 1; t < 4; ++t) {
#pragma unroll
        for (int g = 0; g < 4; ++g) ldsw128(PA + (t - 1) * 4096 + g * 1024 + vo, f4(y[t], g));
      }
      arrive(C_Y, lane);
      await(C_Y, 4);
      f32x16 own = y[0];
#pragma unroll
      for (int t = 1; t < 4; ++t) {
#pragma unroll
        for (int g = 0; g < 4; ++g) add4(own, g, ldsr128((q ^ t) * L_PRIV + (t - 1) * 4096 + g * 1024 + vo));
      }
      if (valid) {
        const u32 ov = (t0 * 896 + dcol) * 4;
#pragma unroll
        for (int i = 0; i < 16; ++i) {
          float sk = bf2f(xs[i]) * bf2f(dsk16);
          PIN(sk);
          __builtin_amdgcn_raw_buffer_store_b32(__float_as_uint(own[i] + rbf(sk)), rs_out, ov, ((i & 3) + 8 * (i >> 2)) * 3584, OUT_AUX);
        }
      }
    }
    TS(8);
    TSW();
    if (tb == 31 && sl == 0 && q == 0) {
      await(C_KV, 1);
      if (lane == 0) gstore_flag(ws, WS_EPOCH, tag);
    }
  }
}

extern "C" void kernel_launch(void* const* d_in, const int* in_sizes, int n_in, void* d_out, int out_size, void* d_ws, size_t ws_size,
                              hipStream_t stream) {
  Params P;
  P.Ax = (const u16*)d_in[0]; P.lre = (const u16*)d_in[1]; P.lim = (const u16*)d_in[2]; P.Bre = (const u16*)d_in[3];
  P.Bim = (const u16*)d_in[4]; P.Cre = (const u16*)d_in[5]; P.Cim = (const u16*)d_in[6]; P.ldt = (const u16*)d_in[7];
  P.Dsk = (const u16*)d_in[8]; P.Bx = (const u16*)d_in[9]; P.wq = (const u16*)d_in[10]; P.wk = (const u16*)d_in[11];
  P.wv = (const u16*)d_in[12]; P.wo = (const u16*)d_in[13];
  P.out = (float*)d_out; P.ws = (char*)d_ws;
  (void)hipFuncSetAttribute((const void*)fused_kernel, hipFuncAttributeMaxDynamicSharedMemorySize, LDS_BYTES);
  fused_kernel<<<256, 512, LDS_BYTES, stream>>>(P);
}
```

```cpp
#include <hip/hip_runtime.h>

typedef unsigned short u16;
typedef unsigned int u32;
typedef unsigned long long u64;
using bf16x8 = __attribute__((ext_vector_type(8))) short;
using f32x16 = __attribute__((ext_vector_type(16))) float;
using u32x4 = __attribute__((ext_vector_type(4))) unsigned;
using u32x2 = __attribute__((ext_vector_type(2))) unsigned;
typedef short v4i16_t __attribute__((ext_vector_type(4)));
typedef __bf16 bf16x2_t __attribute__((ext_vector_type(2)));
typedef float f32x2 __attribute__((ext_vector_type(2)));
#define LAS __attribute__((address_space(3)))
#define GAS __attribute__((address_space(1)))
#define DI __device__ __forceinline__
#define MFMA(a, b, c) __builtin_amdgcn_mfma_f32_32x32x16_bf16((a), (b), (c), 0, 0, 0)
#define SBAR() __builtin_amdgcn_sched_barrier(0)
#define PIN(x) asm volatile("" : "+v"(x))
#define PIN2(a, b) asm volatile("" : "+v"(a), "+v"(b))
#define PIN4(a, b, c, d) asm volatile("" : "+v"(a), "+v"(b), "+v"(c), "+v"(d))
#define MPIN(x) asm volatile("s_nop 7\n\ts_nop 7" : "+v"(x))
#define MPIN2(a, b) asm volatile("s_nop 7\n\ts_nop 7" : "+v"(a), "+v"(b))
#define MPIN4(a, b, c, d) asm volatile("s_nop 7\n\ts_nop 7" : "+v"(a), "+v"(b), "+v"(c), "+v"(d))
#define VMCNT(n) asm volatile("s_waitcnt vmcnt(" #n ")" ::: "memory")
#define LGKM0() asm volatile("s_waitcnt lgkmcnt(0)" ::: "memory")
#define EX(x) __builtin_amdgcn_exp2f(x)

#define NOLOAD 0
#define THR 4
#define KV_AUX 16
#define H_AUX 16
#define FENCE 0
#define ABL 0
#define G1_PRIO 0
#define AUX3_PRIO 2
#define QEARLY 1
#define OUT_AUX 17
#define POLL_SLEEP 2
#define WARM 1
#define PREF 0
#define XEARLY 2
#define XD 4
#define CEARLY 3
#define ROLLED 0
#define KV_DEPTH 6
#define AUX_PRIO 2
#define AUX2_PRIO 0
#define GATE 0
#define KV_REPL 32

constexpr u32 WS_EPOCH = 0;
constexpr u32 WS_HFLAG = 7u << 20;
constexpr u32 WS_OFLAG = WS_HFLAG + 131072;
constexpr u32 WS_KVFLAG = WS_OFLAG + 131072;
constexpr u32 WS_AGG = 32768;
constexpr u32 WS_KVF = WS_AGG + 131072;
constexpr u32 WS_OF = WS_KVF + 262144;
constexpr u32 WS_HF = WS_OF + 2097152;
constexpr u32 WS_END = WS_KVFLAG + 32768;
constexpr u32 WS_DBG = 8u << 20;

constexpr u32 L_XB = 0;
constexpr u32 L_WQ = 57344;
constexpr u32 L_C = 107520;
constexpr u32 L_MISC = L_C + 55296;
constexpr u32 L_AGG = L_MISC + 128;
constexpr u32 L_KN = L_MISC + 896;
constexpr u32 L_CARRY = L_C + 20480;
constexpr u32 L_PRIV = 26880;
constexpr u32 L_WO_OFF = 13440;
constexpr u32 L_QX = L_C + 54 * 864;
constexpr u32 LDS_BYTES = 163840;
enum { C_WQ = 0, C_Q2, C_QDONE, C_BFULL, C_AGG, C_WO, C_O, C_MAX, C_P, C_Y, C_FB, C_KVP, C_KMAX, C_KV, C_OF, C_HF, C_WQ2, C_HD, C_KVL, C_KVR, C_KVD, C_C3, C_CARRY, C_EPOCH = 31 };

struct Params {
  const u16 *Ax, *lre, *lim, *Bre, *Bim, *Cre, *Cim, *ldt, *Dsk, *Bx, *wq, *wk, *wv, *wo;
  float* out;
  char* ws;
};

DI float bf2f(u16 v) { return __uint_as_float(((u32)v) << 16); }
DI u32 cvtpk(float a, float b) { return __builtin_bit_cast(u32, __builtin_convertvector((f32x2){a, b}, bf16x2_t)); }
DI float rbf(float a) { return __uint_as_float(cvtpk(a, 0.f) << 16); }
DI bf16x8 frag(u32x4 v) { return __builtin_bit_cast(bf16x8, v); }
DI void glds16(const void* sbase, u32 voff, u32 lds_base) {
  asm volatile("s_mov_b32 m0, %2\n\ts_nop 0\n\tglobal_load_lds_dwordx4 %0, %1" :: "v"(voff), "s"(sbase), "s"(lds_base) : "memory");
}
DI void glds4(const void* sbase, u32 voff, u32 lds_base) {
  asm volatile("s_mov_b32 m0, %2\n\ts_nop 0\n\tglobal_load_lds_dword %0, %1" :: "v"(voff), "s"(sbase), "s"(lds_base) : "memory");
}
template <int N> DI void vmwait() { asm volatile("s_waitcnt vmcnt(%0)" :: "i"(N) : "memory"); }
DI void vmwait_rt(int n) {
  switch (n) {
    case 0: vmwait<0>(); break; case 4: vmwait<4>(); break; case 8: vmwait<8>(); break; case 12: vmwait<12>(); break;
    case 16: vmwait<16>(); break; case 20: vmwait<20>(); break; default: vmwait<0>(); break;
  }
}
DI u32x4 ldsr128(u32 a) { return *(const LAS u32x4*)(size_t)a; }
DI void ldsw128(u32 a, u32x4 v) { *(LAS u32x4*)(size_t)a = v; }
DI u32 ldsu(u32 a) { return (u32)__builtin_amdgcn_readfirstlane(*(volatile LAS u32*)(size_t)a); }
DI u32x2 ldstr(u32 a) { return __builtin_bit_cast(u32x2, __builtin_amdgcn_ds_read_tr16_b64_v4i16((LAS v4i16_t*)(size_t)a)); }
DI u32x4 trfrag(u32 a0, u32 a1) { u32x2 lo = ldstr(a0), hi = ldstr(a1); return (u32x4){lo.x, lo.y, hi.x, hi.y}; }
DI u32x4 f4(const f32x16& v, int g) {
  return (u32x4){__float_as_uint(v[4 * g]), __float_as_uint(v[4 * g + 1]), __float_as_uint(v[4 * g + 2]), __float_as_uint(v[4 * g + 3])};
}
DI void add4(f32x16& v, int g, u32x4 a) {
  v[4 * g] += __uint_as_float(a.x); v[4 * g + 1] += __uint_as_float(a.y); v[4 * g + 2] += __uint_as_float(a.z); v[4 * g + 3] += __uint_as_float(a.w);
}
DI void arrive(int c, int lane) {
  asm volatile("s_waitcnt lgkmcnt(0)" ::: "memory");
  if (lane == 0) asm volatile("ds_add_u32 %0, %1" :: "v"(L_MISC + 4 * c), "v"(1u) : "memory");
}
DI void await(int c, u32 target) {
#pragma unroll 1
  for (u32 spin = 0; spin < (1u << 22); ++spin) {
    asm volatile("" ::: "memory");
    if (ldsu(L_MISC + 4 * c) >= target) break;
    __builtin_amdgcn_s_sleep(1);
  }
  asm volatile("" ::: "memory");
}
DI float swap32(float v, bool hi) {
  auto r = __builtin_amdgcn_permlane32_swap(__float_as_uint(v), __float_as_uint(v), false, false);
  return __uint_as_float(hi ? r[0] : r[1]);
}
DI float wave_max_nonneg(float v) {
  u32 x = __float_as_uint(v);
  x = max(x, (u32)__builtin_amdgcn_update_dpp(0, (int)x, 0x111, 0xf, 0xf, false));
  x = max(x, (u32)__builtin_amdgcn_update_dpp(0, (int)x, 0x112, 0xf, 0xf, false));
  x = max(x, (u32)__builtin_amdgcn_update_dpp(0, (int)x, 0x114, 0xf, 0xf, false));
  x = max(x, (u32)__builtin_amdgcn_update_dpp(0, (int)x, 0x118, 0xf, 0xf, false));
  x = max(x, (u32)__builtin_amdgcn_update_dpp(0, (int)x, 0x142, 0xa, 0xf, false));
  x = max(x, (u32)__builtin_amdgcn_update_dpp(0, (int)x, 0x143, 0xc, 0xf, false));
  return __uint_as_float((u32)__builtin_amdgcn_readlane((int)x, 63));
}
DI void gstore_flag(char* ws, u32 off, u32 v) { __hip_atomic_store((GAS u32*)(ws + off), v, __ATOMIC_RELAXED, __HIP_MEMORY_SCOPE_AGENT); }
DI void poll_words(char* ws, u32 off, int n, u32 tag, int lane) {
  GAS u32* p = (GAS u32*)(ws + off) + (lane < n ? lane : 0) * 32;
  u32 a = __hip_atomic_load(p, __ATOMIC_RELAXED, __HIP_MEMORY_SCOPE_AGENT);
#pragma unroll 1
  for (u32 spin = 0; spin < (1u << 16); ++spin) {
    __builtin_amdgcn_s_sleep(POLL_SLEEP);
    u32 c = __hip_atomic_load(p, __ATOMIC_RELAXED, __HIP_MEMORY_SCOPE_AGENT);
    SBAR();
    if (__all(a == tag)) break;
    __builtin_amdgcn_s_sleep(POLL_SLEEP);
    a = __hip_atomic_load(p, __ATOMIC_RELAXED, __HIP_MEMORY_SCOPE_AGENT);
    SBAR();
    if (__all(c == tag)) break;
  }
  asm volatile("" ::: "memory");
}
DI float poll_kv(char* ws, int rep, u32 tag, int lane) {
  GAS u64* p = (GAS u64*)(ws + WS_KVFLAG + rep * 4096) + (lane & 31) * 16;
  u64 a = __hip_atomic_load(p, __ATOMIC_RELAXED, __HIP_MEMORY_SCOPE_AGENT), g = 0;
#pragma unroll 1
  for (u32 spin = 0; spin < (1u << 16); ++spin) {
    __builtin_amdgcn_s_sleep(POLL_SLEEP);
    u64 c = __hip_atomic_load(p, __ATOMIC_RELAXED, __HIP_MEMORY_SCOPE_AGENT);
    SBAR();
    g = a;
    if (__all((u32)a == tag)) break;
    __builtin_amdgcn_s_sleep(POLL_SLEEP);
    a = __hip_atomic_load(p, __ATOMIC_RELAXED, __HIP_MEMORY_SCOPE_AGENT);
    SBAR();
    g = c;
    if (__all((u32)c == tag)) break;
  }
  asm volatile("" ::: "memory");
  return __uint_as_float((u32)(g >> 32));
}
DI void cmul(float ar, float ai, float br, float bi, float& cr, float& ci) {
  float x, y;
  asm("v_mul_f32 %0, %2, %4\n\tv_mul_f32 %1, %2, %5\n\tv_fma_f32 %0, -%3, %5, %0\n\tv_fma_f32 %1, %3, %4, %1"
      : "=&v"(x), "=&v"(y) : "v"(ar), "v"(ai), "v"(br), "v"(bi));
  cr = x; ci = y;
}
DI void cstep(float ar, float ai, float& hr, float& hi, float ur, float ui) {
  float x, y;
  asm("v_fma_f32 %0, -%3, %5, %6\n\tv_fma_f32 %1, %3, %4, %7\n\tv_fma_f32 %0, %2, %4, %0\n\tv_fma_f32 %1, %2, %5, %1"
      : "=&v"(x), "=&v"(y) : "v"(ar), "v"(ai), "v"(hr), "v"(hi), "v"(ur), "v"(ui));
  hr = x; hi = y;
}
DI void set_pad(u32x4& f, float m, int h) {
  const float a = rbf(-m), b = rbf(-m - a), c = rbf(-m - a - b);
  if (h) { f.z = cvtpk(a, b); f.w = cvtpk(c, 0.f); }
}

#define TS(i) do {} while (0)
#define TSD() do {} while (0)
#define TSW() do {} while (0)
#define LPRIO 0
#define PUP() do {} while (0)
#define PDN() SBAR()
#define STR_(x) #x
#define VMTHR_(n) asm volatile("s_waitcnt vmcnt(" STR_(n) ")" ::: "memory")
#define VMTHR() VMTHR_(THR)

template <bool W_IS_A, int WROW, bool MID, class F>
DI f32x16 proj_tile(int row, int h, u32 wa, int lane, F mid) {
  const int sw = (row >> 1) & 7;
  u32 xa[4];
#pragma unroll
  for (int bb = 0; bb < 4; ++bb) xa[bb] = L_XB + row * 128 + (((2 * bb + h) ^ sw) << 4);
  u32x4 xf[28], wf[28];
  f32x16 acc = {};
#pragma unroll
  for (int ks = 0; ks < 28 + 5; ++ks) {
    if (ks >= 5) {
      const int k = ks - 5;
      acc = W_IS_A ? MFMA(frag(wf[k]), frag(xf[k]), acc) : MFMA(frag(xf[k]), frag(wf[k]), acc);
      SBAR();
    }
    if (ks < 28) {
      if (MID && ks == 14) {
        asm volatile("s_nop 7\n\ts_nop 7" ::: "memory");
        mid();
        VMCNT(0);
        if (lane == 0) asm volatile("ds_add_u32 %0, %1" :: "v"(L_MISC + 4 * C_WQ2), "v"(1u) : "memory");
        await(C_WQ2, 4);
      }
      xf[ks] = ldsr128(xa[ks & 3] + 8192 * (ks >> 2));
      wf[ks] = trfrag(wa + ks * 16 * WROW, wa + ks * 16 * WROW + 4 * WROW);
      SBAR();
    }
  }
  MPIN(acc);
  return acc;
}

__global__ void __launch_bounds__(512, 2) fused_kernel(Params P) {
  extern __shared__ __attribute__((aligned(16))) char lds_dyn[];
  asm volatile("" : "+s"(P.Ax), "+s"(P.lre), "+s"(P.lim), "+s"(P.Bre), "+s"(P.Bim), "+s"(P.Cre), "+s"(P.Cim), "+s"(P.ldt));
  asm volatile("" : "+s"(P.Dsk), "+s"(P.Bx), "+s"(P.wq), "+s"(P.wk), "+s"(P.wv), "+s"(P.wo), "+s"(P.out), "+s"(P.ws));
  const int tid = threadIdx.x, lane = tid & 63, wv = __builtin_amdgcn_readfirstlane(tid >> 6);
  const int r = lane & 31, h = lane >> 5;
  const int b = blockIdx.x;
  const int tb = (b & 7) * 4 + (b >> 6), sl = (b >> 3) & 7;
  char* ws = P.ws;
  const int tl = b >> 6;
  TSD();
  TS(0);
  if (tid == 0) asm volatile("s_mov_b32 m0, %2\n\ts_nop 0\n\tglobal_load_lds_dword %0, %1 sc1" :: "v"(0), "s"(ws), "s"(L_MISC + 4 * C_EPOCH) : "memory");
  const u32 vo = lane * 16;
  {
    const int row = 8 * wv + (lane >> 3);
    const u32 xv = row * 896 + (((lane & 7) ^ ((row >> 1) & 7)) << 4);
    const char* src = (const char*)P.Bx + (size_t)tb * 64 * 896;
#pragma unroll
    for (int j = 0; j < 7; ++j) {
      int c = j + sl; c = c >= 7 ? c - 7 : c;
      glds16(src + c * 128, xv, L_XB + c * 8192 + wv * 1024);
    }
  }
  if (tid < 31) *(LAS u32*)(size_t)(L_MISC + 4 * tid) = 0;
  const auto rs_ws = __builtin_amdgcn_make_buffer_rsrc((void*)ws, 0, WS_END, 0x00020000);
  const auto rs_out = __builtin_amdgcn_make_buffer_rsrc((void*)P.out, 0, 2048 * 896 * 4, 0x00020000);
  const int g1 = (lane >> 4) & 1, q4 = (lane >> 2) & 3, p4 = lane & 3;
  const int l7 = lane / 7;
  const u32 vo7 = l7 * 896 + (lane - 7 * l7) * 16;

  if (wv < 4) {
    const int w = wv;
    const char* wqsrc = (const char*)P.wq + sl * 112 + w * 7 * 7168;
    auto stage_wq = [&](int hf) {
      if (lane < 56) {
#pragma unroll
        for (int j = 0; j < 7; ++j) {
          int i = j + 2 * tl; i = (i >= 7 ? i - 7 : i) + 28 * hf;
          glds16(wqsrc + i * 7168, vo7, L_WQ + (w * 7 + i) * 896);
        }
      }
    };
    if (sl != 7) {
      stage_wq(0); stage_wq(1);
      asm volatile("s_waitcnt vmcnt(14) lgkmcnt(0)\n\ts_barrier" ::: "memory");
    } else {
      stage_wq(0);
      asm volatile("s_waitcnt vmcnt(7) lgkmcnt(0)\n\ts_barrier" ::: "memory");
    }
    const u32 tag = ldsu(L_MISC + 4 * C_EPOCH) + 1u;
    if (sl != 7) VMCNT(7); else VMCNT(0);
    arrive(C_WQ, lane);
    await(C_WQ, 4);
    TS(15);
    {
      f32x16 acc = proj_tile<true, 112, true>(32 * (w & 1) + r, h, L_WQ + (8 * h + q4) * 112 + (w >> 1) * 56 + g1 * 32 + 8 * p4, lane,
                                            [&]() { if (sl == 7) { await(C_KVD, 4); stage_wq(1); } });
      TS(8);
      const float qs = 0.27264060f;
      float n2 = 0.f;
#pragma unroll
      for (int i = 0; i < 16; ++i) { acc[i] *= qs; if (i < 12) n2 = fmaf(acc[i], acc[i], n2); else n2 = h ? n2 : fmaf(acc[i], acc[i], n2); }
      n2 += swap32(n2, h);
      u32x4 f0 = {cvtpk(acc[0], acc[1]), cvtpk(acc[2], acc[3]), cvtpk(acc[4], acc[5]), cvtpk(acc[6], acc[7])};
      u32x4 f1 = {cvtpk(acc[8], acc[9]), cvtpk(acc[10], acc[11]), cvtpk(acc[12], acc[13]), cvtpk(acc[14], acc[15])};
      if (h) { f1.z = 0; f1.w = __float_as_uint(__builtin_amdgcn_sqrtf(n2)); }
      ldsw128(L_QX + w * 2048 + vo, f0);
      ldsw128(L_QX + w * 2048 + 1024 + vo, f1);
      arrive(C_Q2, lane);
    }
    u32x4 qf[4][2];
    if (QEARLY) {
      await(C_Q2, 4);
#pragma unroll
      for (int t = 0; t < 4; ++t) { const u32 a = L_QX + ((w + t) & 3) * 2048 + vo; qf[t][0] = ldsr128(a); qf[t][1] = ldsr128(a + 1024); }
      arrive(C_QDONE, lane);
    }
    TS(2);
    if (w == 0) {
      const float kn = wave_max_nonneg(poll_kv(ws, b & 7, tag, lane));
      if (lane == 0) *(LAS float*)(size_t)L_KN = kn;
      arrive(C_KV, lane);
    } else {
      await(C_KV, 1);
    }
    TS(3);
    const u32 kvbase = WS_KVF + w * 65536;
    const int rot = b >> 3;
#define KVOFF(it) (kvbase + ((((it) + rot) & 15) << 12))
#define LDK(it, s) __builtin_amdgcn_raw_buffer_load_b128(rs_ws, vo + 1024 * (s), KVOFF(it), KV_AUX)
#define LDV(it, s) __builtin_amdgcn_raw_buffer_load_b128(rs_ws, vo + 2048 + 1024 * (s), KVOFF(it), KV_AUX)
#define NEXT(x) asm volatile("s_add_u32 %0, %0, 0x1000\n\ts_and_b32 %0, %0, 0xf000" : "+s"(x) :: "scc")
#define LDK2(s_) __builtin_amdgcn_raw_buffer_load_b128(rs_kv, vo + 1024 * (s_), koff, KV_AUX)
#define LDV2(s_) __builtin_amdgcn_raw_buffer_load_b128(rs_kv, vo + 2048 + 1024 * (s_), voff, KV_AUX)
    const auto rs_kv = __builtin_amdgcn_make_buffer_rsrc((void*)(ws + kvbase), 0, 65536, 0x00020000);
    f32x16 o[4];
    for (int attempt = 0; attempt < 2; ++attempt) {
      u32x4 kf[KV_DEPTH][2], vf[KV_DEPTH][2];
#pragma unroll
      for (int d = 0; d < KV_DEPTH; ++d) {
        kf[d][0] = LDK(d, 0); kf[d][1] = LDK(d, 1);
        if (d < KV_DEPTH - 1) { vf[d][0] = LDV(d, 0); vf[d][1] = LDV(d, 1); }
      }
      u32 koff = ((KV_DEPTH - 1 + rot) & 15) << 12, voff = ((KV_DEPTH - 2 + rot) & 15) << 12;
      SBAR();
      if (attempt == 0) {
        arrive(C_KVL, lane);
        if (!QEARLY) {
        await(C_Q2, 4);
#pragma unroll
        for (int t = 0; t < 4; ++t) { const u32 a = L_QX + ((w + t) & 3) * 2048 + vo; qf[t][0] = ldsr128(a); qf[t][1] = ldsr128(a + 1024); }
        arrive(C_QDONE, lane);
        }
        const float kmax = __builtin_amdgcn_sqrtf(*(LAS float*)(size_t)L_KN);
#pragma unroll
        for (int t = 0; t < 4; ++t) set_pad(qf[t][1], __uint_as_float(qf[t][1].w) * kmax, h);
      }
#pragma unroll
      for (int t = 0; t < 4; ++t) { PIN(qf[t][0]); PIN(qf[t][1]); }
      SBAR();
      const f32x16 zero = {};
      f32x16 sc = MFMA(frag(kf[0][0]), frag(qf[0][0]), zero);
      sc = MFMA(frag(kf[0][1]), frag(qf[0][1]), sc);
      TS(9);
      float e0 = 0.f, e1 = 0.f, e2 = 0.f, e3 = 0.f;
      u32x4 p1 = {0, 0, 0, 0};
#pragma unroll
      for (int it = 0; it < 16; ++it) {
        const int d = it % KV_DEPTH, dprev = (it + KV_DEPTH - 1) % KV_DEPTH;
#pragma unroll
        for (int qi = 0; qi < 4; ++qi) {
          const int dn = qi == 3 ? (it + 1) % KV_DEPTH : d, qn = (qi + 1) & 3;
          const int dp = qi == 0 ? dprev : d, qp = (qi + 3) & 3;
          const bool last = it == 15 && qi == 3;
          f32x16 sn; u32x4 p0;
          float c0, c1, c2, c3;
          if (!last) sn = MFMA(frag(kf[dn][0]), frag(qf[qn][0]), zero);
          SBAR();
          p1.z = cvtpk(e0, e1); p1.w = cvtpk(e2, e3); PIN(p1); SBAR();
          c0 = EX(sc[0]); c1 = EX(sc[1]); c2 = EX(sc[2]); c3 = EX(sc[3]); PIN4(c0, c1, c2, c3); SBAR();
          if (it > 0 || qi > 0) o[qp] = MFMA(frag(vf[dp][1]), frag(p1), o[qp]);
          SBAR();
          p0.x = cvtpk(c0, c1); p0.y = cvtpk(c2, c3); PIN2(p0.x, p0.y); SBAR();
          c0 = EX(sc[4]); c1 = EX(sc[5]);
          if (!NOLOAD && qi < 2 && it - 1 + KV_DEPTH < 16) { PIN2(c0, c1); SBAR(); if (qi == 0) NEXT(voff); vf[dprev][qi] = LDV2(qi); SBAR(); }
          c2 = EX(sc[6]); c3 = EX(sc[7]); PIN4(c0, c1, c2, c3); SBAR();
          if (!last) sn = MFMA(frag(kf[dn][1]), frag(qf[qn][1]), sn);
          SBAR();
          p0.z = cvtpk(c0, c1); p0.w = cvtpk(c2, c3); PIN(p0); SBAR();
          c0 = EX(sc[8]); c1 = EX(sc[9]);
          if (!NOLOAD && qi == 2 && it + KV_DEPTH < 16) { PIN2(c0, c1); SBAR(); NEXT(koff); kf[d][0] = LDK2(0); SBAR(); }
          c2 = EX(sc[10]); c3 = EX(sc[11]); PIN4(c0, c1, c2, c3); SBAR();
          o[qi] = MFMA(frag(vf[d][0]), frag(p0), it == 0 ? zero : o[qi]); SBAR();
          p1.x = cvtpk(c0, c1); p1.y = cvtpk(c2, c3); PIN2(p1.x, p1.y); SBAR();
          e0 = EX(sc[12]); e1 = EX(sc[13]);
          if (!NOLOAD && qi == 2 && it + KV_DEPTH < 16) { PIN2(e0, e1); SBAR(); kf[d][1] = LDK2(1); SBAR(); }
          e2 = EX(sc[14]); e3 = EX(sc[15]); PIN4(e0, e1, e2, e3); SBAR();
          if (!last) sc = sn;
        }
      }
      p1.z = cvtpk(e0, e1); p1.w = cvtpk(e2, e3);
      o[3] = MFMA(frag(vf[15 % KV_DEPTH][1]), frag(p1), o[3]);
      MPIN4(o[0], o[1], o[2], o[3]);
      TS(4);
      u32 lv;
      asm volatile("v_mbcnt_lo_u32_b32 %0, -1, 0\n\tv_mbcnt_hi_u32_b32 %0, -1, %0\n\tv_lshlrev_b32 %0, 4, %0" : "=v"(lv));
      if (attempt == 0) {
        bool bad = false;
#pragma unroll
        for (int t = 0; t < 4; ++t) bad |= !(o[t][12] >= 0x1p-80f && o[t][12] <= 0x1p100f);
        if (__any(bad && lv >= 512)) { if (lv == 0) *(LAS u32*)(size_t)(L_MISC + 4 * C_FB) = 1u; }
        await(C_HD, 4);
      }
#pragma unroll
      for (int t = 1; t < 4; ++t) {
#pragma unroll
        for (int g = 0; g < 4; ++g) ldsw128(L_C + w * 12288 + (t - 1) * 4096 + g * 1024 + lv, f4(o[t], g));
      }
      arrive(C_O, lv);
      await(C_O, 4 * (attempt + 1));
      TS(10);
      const u32 fb = attempt == 0 ? ldsu(L_MISC + 4 * C_FB) : 0u;
      if (!fb) break;
      {
        float mx[4];
#pragma unroll
        for (int t = 0; t < 4; ++t) { mx[t] = -3.0e38f; if (lv >= 512) { qf[t][1].z = 0; qf[t][1].w = 0; } }
#pragma unroll 1
        for (int it = 0; it < 16; ++it) {
          const u32x4 k0 = __builtin_amdgcn_raw_buffer_load_b128(rs_kv, lv, it << 12, KV_AUX), k1 = __builtin_amdgcn_raw_buffer_load_b128(rs_kv, lv + 1024, it << 12, KV_AUX);
#pragma unroll
          for (int t = 0; t < 4; ++t) {
            f32x16 s = MFMA(frag(k0), frag(qf[t][0]), zero);
            s = MFMA(frag(k1), frag(qf[t][1]), s);
#pragma unroll
            for (int i = 0; i < 16; ++i) mx[t] = fmaxf(mx[t], s[i]);
          }
        }
        const u32 ma = L_C + 49152;
#pragma unroll
        for (int t = 0; t < 4; ++t) { mx[t] = fmaxf(mx[t], swap32(mx[t], lv >= 512)); *(LAS float*)(size_t)(ma + (w * 4 + ((w + t) & 3)) * 256 + (lv >> 2)) = mx[t]; }
        arrive(C_MAX, lv);
        await(C_MAX, 4);
#pragma unroll
        for (int t = 0; t < 4; ++t) {
          float m = -3.0e38f;
#pragma unroll
          for (int ww = 0; ww < 4; ++ww) m = fmaxf(m, *(LAS float*)(size_t)(ma + (ww * 4 + ((w + t) & 3)) * 256 + (lv >> 2)));
          set_pad(qf[t][1], m, lv >= 512);
        }
      }
    }
    {
    int lane;
    asm volatile("v_mbcnt_lo_u32_b32 %0, -1, 0\n\tv_mbcnt_hi_u32_b32 %0, -1, %0" : "=v"(lane));
    const int r = lane & 31, h = lane >> 5, g1 = (lane >> 4) & 1, q4 = (lane >> 2) & 3, p4 = lane & 3;
    const u32 vo = lane * 16;
    {
      f32x16 own = o[0];
#pragma unroll
      for (int pp = 1; pp < 4; ++pp) {
        const u32 a = L_C + ((w + pp) & 3) * 12288 + (3 - pp) * 4096 + vo;
#pragma unroll
        for (int g = 0; g < 4; ++g) add4(own, g, ldsr128(a + g * 1024));
      }
      const float other = swap32(own[12], h);
      const float inv = __builtin_amdgcn_rcpf(h ? own[12] : other);
      u32x4 f0 = {cvtpk(own[0] * inv, own[1] * inv), cvtpk(own[2] * inv, own[3] * inv), cvtpk(own[4] * inv, own[5] * inv), cvtpk(own[6] * inv, own[7] * inv)};
      u32x4 f1 = {cvtpk(own[8] * inv, own[9] * inv), cvtpk(own[10] * inv, own[11] * inv), cvtpk(own[12] * inv, own[13] * inv), cvtpk(own[14] * inv, own[15] * inv)};
      if (h) { f1.z = 0; f1.w = 0; }
      const u32 so = WS_OF + ((tb * 2 + (w & 1)) * 16 + 2 * sl + (w >> 1)) * 2048 + vo;
      __builtin_amdgcn_raw_buffer_store_b128(f0, rs_ws, so, 0, 16);
      __builtin_amdgcn_raw_buffer_store_b128(f1, rs_ws, so + 1024, 0, 16);
    }
    {
      const int tm = w & 1, kh = w >> 1;
      TS(11);
      VMCNT(0);
      if (lane == 0) gstore_flag(ws, WS_OFLAG + (tb * 32 + sl * 4 + w) * 128, tag);
      TS(5);
      await(C_WO, 4);
      u32x4 wf[16][2];
      const u32 wa = 2 * kh * L_PRIV + L_WO_OFF + (4 * h + q4) * 112 + g1 * 32 + 8 * p4;
      const u32 wa0 = wa + 64 * kh, wa1 = wa + 64 * (1 - kh);
#pragma unroll
      for (int i = 0; i < 16; ++i) {
        const u32 a = (i >> 3) * L_PRIV + (28 * ((i >> 1) & 3) + 16 * (i & 1)) * 112;
        wf[i][0] = trfrag(wa0 + a, wa0 + a + 896); wf[i][1] = trfrag(wa1 + a, wa1 + a + 896);
      }
#pragma unroll
      for (int i = 0; i < 16; ++i) PIN2(wf[i][0], wf[i][1]);
      if (w == 0) { poll_words(ws, WS_OFLAG + tb * 4096, 32, tag, lane); arrive(C_OF, lane); }
      else await(C_OF, 1);
      TS(6);
      u32x4 of[16];
      const u32 so = WS_OF + ((tb * 2 + tm) * 16 + 8 * kh) * 2048;
#pragma unroll
      for (int i = 0; i < 16; ++i) of[i] = __builtin_amdgcn_raw_buffer_load_b128(rs_ws, vo + (i & 3) * 1024, so + (i >> 2) * 4096, H_AUX);
      SBAR();
      f32x16 a0 = {}, a1 = {};
#pragma unroll
      for (int i = 0; i < 16; ++i) {
        a0 = MFMA(frag(of[i]), frag(wf[i][0]), a0);
        a1 = MFMA(frag(of[i]), frag(wf[i][1]), a1);
      }
      MPIN2(a0, a1);
      TS(12);
      const f32x16 snd = a1;
      f32x16 own = a0;
      const u32 aw = L_C + w * 4096 + vo, ar = L_C + (w ^ 2) * 4096 + vo;
#pragma unroll
      for (int g = 0; g < 4; ++g) ldsw128(aw + g * 1024, f4(snd, g));
      arrive(C_P, lane);
      await(C_P, 4);
#pragma unroll
      for (int g = 0; g < 4; ++g) add4(own, g, ldsr128(ar + g * 1024));
      if (32 * kh + r < 56) {
        const u32 ov = ((tb * 64 + 32 * tm + 4 * h) * 896 + 448 + 56 * sl + 32 * kh + r) * 4;
#pragma unroll
        for (int i = 0; i < 16; ++i) __builtin_amdgcn_raw_buffer_store_b32(__float_as_uint(rbf(own[i])), rs_out, ov, ((i & 3) + 8 * (i >> 2)) * 3584, OUT_AUX);
      }
    }
    }
    TS(7);
    TSW();
    { const u64 t0 = __hip_atomic_load((GAS u64*)(ws + 2048), __ATOMIC_RELAXED, __HIP_MEMORY_SCOPE_AGENT);
      const u64 lim = __builtin_amdgcn_s_memrealtime() + 6000;
      while (__builtin_amdgcn_s_memrealtime() - t0 < 4500 && __builtin_amdgcn_s_memrealtime() < lim) __builtin_amdgcn_s_sleep(2); }
  } else {
    __builtin_amdgcn_s_setprio(AUX_PRIO);
    const int q = wv - 4;
    const u32 PA = q * L_PRIV;
    const int part = q >> 1, half = q & 1;
    if (sl == 7 && lane < 56) {
      const char* src = (const char*)((q >> 1) ? P.wv : P.wk) + (q & 1) * 14 * 896;
#pragma unroll
      for (int j = 0; j < 14; ++j) {
        int i = j + 4 * tl; i = i >= 14 ? i - 14 : i;
        glds16(src + i * 896, vo, L_C + (q * 14 + i) * 896);
      }
    }
    asm volatile("s_waitcnt vmcnt(0) lgkmcnt(0)\n\ts_barrier" ::: "memory");
    const u32 tag = ldsu(L_MISC + 4 * C_EPOCH) + 1u;
    TS(13);
    if (sl == 7) {
      const int kind = q >> 1, ktm = q & 1;
      const u32 wa = L_C + kind * 25088 + (8 * h + q4) * 56 + g1 * 32 + 8 * p4;
      f32x16 acc;
      if (kind == 0) acc = proj_tile<true, 56, false>(32 * ktm + r, h, wa, lane, []() {});
      else acc = proj_tile<false, 56, false>(32 * ktm + r, h, wa, lane, []() {});
      u32x4 f0 = {cvtpk(acc[0], acc[1]), cvtpk(acc[2], acc[3]), cvtpk(acc[4], acc[5]), cvtpk(acc[6], acc[7])};
      u32x4 f1 = {cvtpk(acc[8], acc[9]), cvtpk(acc[10], acc[11]), cvtpk(acc[12], acc[13]), cvtpk(acc[14], acc[15])};
      if (kind == 0) {
        if (h) { f1.z = 0x3F803F80u; f1.w = 0x00003F80u; }
      } else if (r >= 28) {
        const u32 c = r == 28 ? 0x3F803F80u : 0u;
        f0 = (u32x4){c, c, c, c}; f1 = f0;
      }
      const u32 so = WS_KVF + (tb * 2 + ktm) * 4096 + kind * 2048 + vo;
      __builtin_amdgcn_raw_buffer_store_b128(f0, rs_ws, so, 0, 16);
      __builtin_amdgcn_raw_buffer_store_b128(f1, rs_ws, so + 1024, 0, 16);
      SBAR();
      TS(12);
      if (kind == 0) {
        float n2 = 0.f;
#pragma unroll
        for (int i = 0; i < 16; ++i) { if (i < 12) n2 = fmaf(acc[i], acc[i], n2); else n2 = h ? n2 : fmaf(acc[i], acc[i], n2); }
        n2 += swap32(n2, h);
        n2 = wave_max_nonneg(n2);
        if (lane == 0) asm volatile("ds_max_u32 %0, %1" :: "v"(L_MISC + 4 * C_KMAX), "v"(n2) : "memory");
      }
      VMCNT(0);
      u32 old = 0;
      if (lane == 0) asm volatile("s_waitcnt lgkmcnt(0)\n\tds_add_rtn_u32 %0, %1, %2\n\ts_waitcnt lgkmcnt(0)" : "=&v"(old) : "v"(L_MISC + 4 * C_KVP), "v"(1u) : "memory");
      if (__builtin_amdgcn_readfirstlane(old) == 3) {
        const u32 km = ldsu(L_MISC + 4 * C_KMAX);
        if (lane < 8) __hip_atomic_store((GAS u64*)(ws + WS_KVFLAG + lane * 4096 + tb * 128), (u64)tag | ((u64)km << 32), __ATOMIC_RELAXED, __HIP_MEMORY_SCOPE_AGENT);
      }
      TS(14);
      arrive(C_KVD, lane);
      await(C_KVD, 4);
    } else if (PREF) {
      const int j = (b >> 3) - tl;
      const char* p0 = (const char*)(q == 0 ? P.Bre : q == 1 ? P.Bim : q == 2 ? P.Cre : P.Cim) + j * 14336;
      const u32 o2 = lane < 48 ? 8192 + lane * 128 : 0;
      glds4(p0, lane * 128, L_AGG + 512);
      glds4(p0, o2, L_AGG + 512);
      if (q == 0) glds4((const char*)P.Ax + (size_t)(b & 7) * 229376 + j * 8192, lane * 128, L_AGG + 512);
      if (q == 1) glds4((const char*)P.wo + j * 14336, lane * 128, L_AGG + 512);
      if (q == 2) glds4((const char*)P.wo + j * 14336, o2, L_AGG + 512);
    }

    u32 cva;
    {
      const int hi = lane >= 28, cs = lane - 28 * hi;
      cva = hi * 896 + (cs << 4);
    }
    auto stage_C4 = [&](int qq, int g) {
      const char* csrc = (const char*)((qq >> 1) ? P.Cim : P.Cre) + (size_t)sl * 56 * 896 + (qq & 1) * 448;
      int p0 = 4 * g + 8 * tl; p0 = p0 >= 28 ? p0 - 28 : p0;
      const u32 va = cva ^ ((p0 & 4) << 3), vb = va ^ 16;
      if (lane < 56) {
#pragma unroll
        for (int j = 0; j < 4; ++j) { glds16(csrc + (p0 + j) * 1792, j < 2 ? va : vb, qq * L_PRIV + (p0 + j) * 896); VMTHR(); }
      }
    };
    const int tm = (q ^ sl) & 1, tn = q >> 1;
    const bool valid = 32 * tn + r < 56;
    const int dcol = 56 * sl + (valid ? 32 * tn + r : 55), t0 = tb * 64 + 32 * tm + 4 * h;
    if (WARM && q == 3) {
      const int k = lane & 7;
      const u32 o1 = k < 4 ? WS_KVF + k * 65536 : k < 6 ? WS_OF + tb * 65536 + (k - 4) * 65024 : k == 6 ? WS_HFLAG : WS_AGG + (((tb & 1) * 7 + sl) & 15) * 8192;
      const u32 o2 = WS_HF + (tb & ~1) * 114688 + (k & 3) * 65536;
      if (lane < 16) {
        asm volatile("s_mov_b32 m0, %2\n\ts_nop 0\n\tglobal_load_lds_dword %0, %1 sc1" :: "v"(lane < 8 ? o1 : o2), "s"(ws), "s"(L_AGG + 512) : "memory");
        glds4((const char*)P.out + (size_t)tb * 229376, (lane & 3) * 65536, L_AGG + 512);
      }
    }
    u32x4 cf[2][14];
    auto read_C = [&]() {
#pragma unroll
      for (int t = 0; t < 2; ++t) {
        int row = 32 * (t ^ part) + r; row = row > 55 ? 55 : row;
        const int x = (row >> 2) & 3;
        const u32 a0 = PA + row * 448 + ((h ^ x) << 4), a1 = PA + row * 448 + (((2 + h) ^ x) << 4);
#pragma unroll
        for (int j = 0; j < 14; ++j) cf[t][j] = ldsr128(((j & 1) ? a1 : a0) + 64 * (j >> 1));
      }
#pragma unroll
      for (int j = 0; j < 14; ++j) PIN2(cf[0][j], cf[1][j]);
      LGKM0();
    };
    if (sl == 7) {
      arrive(C_HD, lane);
      await(C_KVL, 4);
#pragma unroll 1
      for (int g = 0; g < 7; ++g) stage_C4(q, g);
      VMCNT(0);
      read_C();
    } else {
      const int tb2 = tb >> 1, cg = (tb & 1) * 7 + sl, ch = 32 * cg + r;
      const u32 vo1 = (lane ^ 1) * 16;
      float lr, li, cr, ci;
      if (q < 3 && lane < 54) {
#pragma unroll
        for (int j = 0; j < 18; ++j) {
          int i = j + 9 * (tb2 & 1); i = i >= 18 ? i - 18 : i;
          const int row = q * 18 + i;
          glds16((const char*)(row >= 32 ? P.Bim : P.Bre) + (size_t)(cg * 32 + (row & 31)) * 896, (row & 8) ? vo1 : vo, L_C + row * 864);
        }
      }
      u32 vlre = P.lre[ch], vlim = P.lim[ch], vldt = P.ldt[ch];
      u32x4 blast_re = *(const u32x4*)(P.Bre + (size_t)ch * 448 + 432 + 8 * h);
      u32x4 blast_im = *(const u32x4*)(P.Bim + (size_t)ch * 448 + 432 + 8 * h);
      asm volatile("" : "+v"(vlre), "+v"(vlim), "+v"(vldt), "+v"(blast_re), "+v"(blast_im) :: "memory");
      {
        const float dt = rbf(expf(__uint_as_float(vldt << 16))), a = __uint_as_float(vlre << 16), bb = __uint_as_float(vlim << 16);
        const float er = expf(dt * a);
        float sn, cs;
        sincosf(dt * bb, &sn, &cs);
        lr = er * cs; PIN(lr); li = er * sn;
        const float nr = lr - 1.f, ni = li, den = 2.0f / (a * a + bb * bb);
        cr = (nr * a + ni * bb) * den; PIN(cr); ci = (ni * a - nr * bb) * den;
      }
      float l16r = lr, l16i = li;
#pragma unroll
      for (int i = 0; i < 4; ++i) cmul(l16r, l16i, l16r, l16i, l16r, l16i);
      float l32r, l32i, l128r, l128i;
      cmul(l16r, l16i, l16r, l16i, l32r, l32i);
      cmul(l32r, l32i, l32r, l32i, l128r, l128i);
      cmul(l128r, l128i, l128r, l128i, l128r, l128i);
      PIN4(lr, li, cr, ci); PIN4(l16r, l16i, l32r, l32i); PIN2(l128r, l128i);
      const char* xsrc = (const char*)P.Ax + (size_t)(tb2 * 128 + q * 32) * 896;
      const u32 xv0 = (lane >> 3) * 896 + (((lane & 7) ^ (lane >> 4)) << 4), xv1 = xv0 ^ 64;
      const int rho = 16 * ((r >> 2) & 1) + (r & 3) + 4 * (r >> 3), sw = (rho >> 1) & 7;
      u32 xa[4];
#pragma unroll
      for (int bb = 0; bb < 4; ++bb) xa[bb] = PA + rho * 128 + (((2 * bb + h) ^ sw) << 4);
      const int xrot = cg % 6;
      u32 ba[7];
#pragma unroll
      for (int c = 0; c < 7; ++c) {
        int kc = c + xrot; kc = c == 6 ? 6 : kc >= 6 ? kc - 6 : kc;
        ba[c] = L_C + r * 864 + ((h ^ ((r >> 3) & 1)) << 4) + 128 * kc;
        PIN(ba[c]);
      }
      PIN4(xa[0], xa[1], xa[2], xa[3]);
      TS(1);
      await(C_QDONE, 4);
      if (q == 3 && lane < 54) {
#pragma unroll
        for (int i = 22; i < 32; ++i) glds16((const char*)P.Bim + (size_t)(cg * 32 + i) * 896, (i & 8) ? vo1 : vo, L_C + (32 + i) * 864);
      }
      auto xdma = [&](int c) {
#pragma unroll
        for (int j = 0; j < 4; ++j) {
          int kc = c + xrot; kc = c == 6 ? 6 : kc >= 6 ? kc - 6 : kc;
          glds16(xsrc + kc * 128 + j * 7168, (j & 1) ? xv1 : xv0, PA + (c % 6) * 4096 + j * 1024);
        }
      };
      xdma(0); xdma(1);
      vmwait<8>();
      arrive(C_BFULL, lane);
      await(C_BFULL, 4);
      TS(2);
      f32x16 ur = {}, ui = {};
      PIN2(ur, ui);
      __builtin_amdgcn_s_setprio(G1_PRIO);
      {
        u32x4 xf[28], br[28], bi[28];
#pragma unroll
        for (int ks = 0; ks < 28 + 4; ++ks) {
          if (ks >= 4) {
            const int k = ks - 4;
            ur = MFMA(frag(xf[k]), frag(br[k]), ur);
            ui = MFMA(frag(xf[k]), frag(bi[k]), ui);
            SBAR();
          }
          if (ks < 28) {
            if ((ks & 3) == 0) {
              const int c = ks >> 2;
              const int issued = c < 2 ? 1 : (XD + c - 2 > 6 ? 6 : XD + c - 2);
              vmwait_rt(4 * (issued - c));
            }
            if (ks == 7) {
              asm volatile("s_nop 7\n\ts_nop 7" ::: "memory");
              await(C_KVL, 4);
#pragma unroll
              for (int c = 2; c <= XD; ++c) xdma(c);
            }
            if ((ks & 3) == 3 && ks > 7 && XD + (ks >> 2) - 1 <= 6) xdma(XD + (ks >> 2) - 1);
            xf[ks] = ldsr128(xa[ks & 3] + ((ks >> 2) % 6) * 4096);
            br[ks] = ks < 27 ? ldsr128(ba[ks >> 2] + 32 * (ks & 3)) : blast_re;
            bi[ks] = ks < 27 ? ldsr128(ba[ks >> 2] + 27648 + 32 * (ks & 3)) : blast_im;
            SBAR();
          }
        }
      }
      MPIN2(ur, ui);
      __builtin_amdgcn_s_setprio(AUX_PRIO);
      TS(3);
      float ar = 0.f, ai = 0.f;
#pragma unroll
      for (int i = 0; i < 16; ++i) {
        float tr, ti;
        cmul(cr, ci, ur[i], ui[i], tr, ti);
        ur[i] = tr; ui[i] = ti;
        cstep(lr, li, ar, ai, tr, ti);
      }
      const float or_ = swap32(ar, h), oi_ = swap32(ai, h);
      const float lor = h ? or_ : ar, loi = h ? oi_ : ai;
      float a32r = lor, a32i = loi;
      cstep(l16r, l16i, a32r, a32i, h ? ar : or_, h ? ai : oi_);
      if (q < 3 && h == 0) *(LAS f32x2*)(size_t)(L_AGG + q * 256 + r * 8) = (f32x2){a32r, a32i};
      arrive(C_AGG, lane);
      await(C_AGG, 4);
      f32x2 wagg[3];
#pragma unroll
      for (int j = 0; j < 3; ++j) wagg[j] = *(LAS f32x2*)(size_t)(L_AGG + j * 256 + r * 8);
      const u32 aggb = WS_AGG + cg * 8192;
      if (q == 3 && tb2 < 15) {
        float tr = wagg[0].x, ti = wagg[0].y;
        cstep(l32r, l32i, tr, ti, wagg[1].x, wagg[1].y);
        cstep(l32r, l32i, tr, ti, wagg[2].x, wagg[2].y);
        cstep(l32r, l32i, tr, ti, a32r, a32i);
        __hip_atomic_store((GAS u64*)(ws + aggb + tb2 * 512 + r * 16 + h * 8), (u64)__float_as_uint(h ? ti : tr) | ((u64)tag << 32), __ATOMIC_RELAXED, __HIP_MEMORY_SCOPE_AGENT);
      }
      TS(4);
      float hr = 0.f, hi = 0.f;
      if (q < 3) {
#pragma unroll 1
        for (int g = q; g < 7; g += 3) stage_C4(3, g);
        stage_C4(q, 0); stage_C4(q, 1);
        vmwait<8>();
        arrive(C_C3, lane);
#pragma unroll 1
        for (int g = 2; g < 7; ++g) stage_C4(q, g);
        VMCNT(0);
        read_C();
        TS(10);
        await(C_CARRY, 1);
        const f32x2 cv = *(LAS f32x2*)(size_t)(L_CARRY + r * 8);
        hr = cv.x; hi = cv.y;
      } else {
        await(C_C3, 3);
        read_C();
        if (tb2 > 0) {
          u32x4 g[8];
          u32 gv[8];
#pragma unroll
          for (int j = 0; j < 8; ++j) gv[j] = r * 16 + ((8 * h + j < tb2) ? (8 * h + j) * 512 : 0);
#pragma unroll 1
          for (u32 spin = 0; spin < (1u << 17); ++spin) {
            asm volatile("" ::: "memory");
            u32 bad = 0;
#pragma unroll
            for (int j = 0; j < 8; ++j) g[j] = __builtin_amdgcn_raw_buffer_load_b128(rs_ws, gv[j], aggb, 16);
#pragma unroll
            for (int j = 0; j < 8; ++j) bad |= (g[j].y ^ tag) | (g[j].w ^ tag);
            if (__all(bad == 0)) break;
            __builtin_amdgcn_s_sleep(4);
          }
          asm volatile("" ::: "memory");
#pragma unroll
          for (int j = 0; j < 8; ++j)
            if (j < tb2) cstep(l128r, l128i, hr, hi, __uint_as_float(g[j].x), __uint_as_float(g[j].z));
          const float sr = swap32(hr, h), si = swap32(hi, h);
          if (h) { hr = sr; hi = si; }
          if (tb2 > 8) {
#pragma unroll
            for (int j = 0; j < 7; ++j)
              if (8 + j < tb2) cstep(l128r, l128i, hr, hi, __uint_as_float(g[j].x), __uint_as_float(g[j].z));
            const float tr = swap32(hr, h), ti = swap32(hi, h);
            if (!h) { hr = tr; hi = ti; }
          }
        }
        if (h == 0) *(LAS f32x2*)(size_t)(L_CARRY + r * 8) = (f32x2){hr, hi};
        arrive(C_CARRY, lane);
        TS(10);
      }
#pragma unroll
      for (int j = 0; j < 3; ++j)
        if (j < q) cstep(l32r, l32i, hr, hi, wagg[j].x, wagg[j].y);
      if (h) cstep(l16r, l16i, hr, hi, lor, loi);
      TS(5);
      u32 pr[8], pi[8];
#pragma unroll
      for (int i = 0; i < 16; i += 2) {
        cstep(lr, li, hr, hi, ur[i], ui[i]);
        const float r0 = hr, i0 = hi;
        cstep(lr, li, hr, hi, ur[i + 1], ui[i + 1]);
        pr[i >> 1] = cvtpk(r0, hr); pi[i >> 1] = cvtpk(-i0, -hi);
      }
      const u32 ta = L_C + q * 5120 + r * 80 + 32 * h;
      ldsw128(ta, (u32x4){pr[0], pr[1], pr[2], pr[3]}); ldsw128(ta + 16, (u32x4){pr[4], pr[5], pr[6], pr[7]});
      ldsw128(ta + 2560, (u32x4){pi[0], pi[1], pi[2], pi[3]}); ldsw128(ta + 2560 + 16, (u32x4){pi[4], pi[5], pi[6], pi[7]});
      LGKM0();
      const u32 tr = L_C + q * 5120 + (8 * h + q4) * 80 + g1 * 32 + 8 * p4;
      const int tbh = 2 * tb2 + (q >> 1);
      const u32 so = WS_HF + ((tbh * 2 + (q & 1)) * 56 + 2 * cg) * 1024 + vo;
#pragma unroll
      for (int pt = 0; pt < 2; ++pt)
#pragma unroll
        for (int s = 0; s < 2; ++s) {
          const u32 a = tr + pt * 2560 + s * 1280;
          __builtin_amdgcn_raw_buffer_store_b128(trfrag(a, a + 320), rs_ws, so + pt * 28 * 1024 + s * 1024, 0, 16);
        }
      arrive(C_HD, lane);
      VMCNT(0);
      if (lane == 0) gstore_flag(ws, WS_HFLAG + (tbh * 32 + cg * 2 + (q & 1)) * 128, tag);
      TS(6);
    }
    TS(9);
    if (lane < 56) {
      const char* src = (const char*)P.wo + sl * 112 + q * 14 * 7168;
#pragma unroll
      for (int j = 0; j < 15; ++j) {
        int i = j + 4 * tl; i = i >= 15 ? i - 15 : i;
        glds16(src + ((i == 14 && q == 3) ? 13 : i) * 7168, vo7, PA + L_WO_OFF + 896 * i); VMTHR();
      }
    }
    VMCNT(0);
    arrive(C_WO, lane);
    TS(11);
    if (q == 0) { poll_words(ws, WS_HFLAG + tb * 4096, 28, tag, lane); arrive(C_HF, lane); }
    else await(C_HF, 1);
    TS(7);
    {
      f32x16 y[4];
      const u32 so = WS_HF + ((tb * 2 + tm) * 56 + part * 28 + half * 14) * 1024, so1 = WS_HF + ((tb * 2 + (tm ^ 1)) * 56 + part * 28 + half * 14) * 1024;
      u32x4 hf[14];
#pragma unroll
      for (int j = 0; j < 14; ++j) hf[j] = __builtin_amdgcn_raw_buffer_load_b128(rs_ws, vo + (j & 3) * 1024, so + (j >> 2) * 4096, H_AUX);
      SBAR();
#pragma unroll
      for (int t = 0; t < 4; ++t) y[t] = (f32x16){};
      PIN4(y[0], y[1], y[2], y[3]);
      __builtin_amdgcn_s_setprio(AUX2_PRIO);
#pragma unroll
      for (int j = 0; j < 14; ++j) {
        y[0] = MFMA(frag(hf[j]), frag(cf[0][j]), y[0]);
        y[2] = MFMA(frag(hf[j]), frag(cf[1][j]), y[2]);
        SBAR();
        hf[j] = __builtin_amdgcn_raw_buffer_load_b128(rs_ws, vo + (j & 3) * 1024, so1 + (j >> 2) * 4096, H_AUX);
        SBAR();
      }
#pragma unroll
      for (int j = 0; j < 14; ++j) {
        y[1] = MFMA(frag(hf[j]), frag(cf[0][j]), y[1]);
        y[3] = MFMA(frag(hf[j]), frag(cf[1][j]), y[3]);
      }
      u16 xs[16];
      {
        const auto rs_ax = __builtin_amdgcn_make_buffer_rsrc((void*)P.Ax, 0, 2048 * 896, 0x00020000);
        const u32 xv = (t0 * 448 + dcol) * 2;
#pragma unroll
        for (int i = 0; i < 16; ++i) xs[i] = __builtin_amdgcn_raw_buffer_load_b16(rs_ax, xv + (i & 3) * 896, (i >> 2) * 7168, 0);
      }
      const u16 dsk16 = P.Dsk[dcol];
      MPIN4(y[0], y[1], y[2], y[3]);
      __builtin_amdgcn_s_setprio(AUX3_PRIO);
#pragma unroll
      for (int t = 1; t < 4; ++t) {
#pragma unroll
        for (int g = 0; g < 4; ++g) ldsw128(PA + (t - 1) * 4096 + g * 1024 + vo, f4(y[t], g));
      }
      arrive(C_Y, lane);
      await(C_Y, 4);
      f32x16 own = y[0];
#pragma unroll
      for (int t = 1; t < 4; ++t) {
#pragma unroll
        for (int g = 0; g < 4; ++g) add4(own, g, ldsr128((q ^ t) * L_PRIV + (t - 1) * 4096 + g * 1024 + vo));
      }
      if (valid) {
        const u32 ov = (t0 * 896 + dcol) * 4;
#pragma unroll
        for (int i = 0; i < 16; ++i) {
          float sk = bf2f(xs[i]) * bf2f(dsk16);
          PIN(sk);
          __builtin_amdgcn_raw_buffer_store_b32(__float_as_uint(own[i] + rbf(sk)), rs_out, ov, ((i & 3) + 8 * (i >> 2)) * 3584, OUT_AUX);
        }
      }
    }
    TS(8);
    TSW();
    if (tb == 31 && sl == 0 && q == 0) {
      await(C_KV, 1);
      if (lane == 0) gstore_flag(ws, WS_EPOCH, tag);
    }
  }
}

__global__ void __launch_bounds__(512, 2) dummy_kernel(char* ws) {
  asm volatile("v_mov_b32 v250, 0" ::: "v250");
  const u64 t = __builtin_amdgcn_s_memrealtime();
  if (blockIdx.x == 0 && threadIdx.x == 0) __hip_atomic_store((GAS u64*)(ws + 2048), t, __ATOMIC_RELAXED, __HIP_MEMORY_SCOPE_AGENT);
}
extern "C" void kernel_launch(void* const* d_in, const int* in_sizes, int n_in, void* d_out, int out_size, void* d_ws, size_t ws_size,
                              hipStream_t stream) {
  Params P;
  P.Ax = (const u16*)d_in[0]; P.lre = (const u16*)d_in[1]; P.lim = (const u16*)d_in[2]; P.Bre = (const u16*)d_in[3];
  P.Bim = (const u16*)d_in[4]; P.Cre = (const u16*)d_in[5]; P.Cim = (const u16*)d_in[6]; P.ldt = (const u16*)d_in[7];
  P.Dsk = (const u16*)d_in[8]; P.Bx = (const u16*)d_in[9]; P.wq = (const u16*)d_in[10]; P.wk = (const u16*)d_in[11];
  P.wv = (const u16*)d_in[12]; P.wo = (const u16*)d_in[13];
  P.out = (float*)d_out; P.ws = (char*)d_ws;
  (void)hipFuncSetAttribute((const void*)fused_kernel, hipFuncAttributeMaxDynamicSharedMemorySize, LDS_BYTES);
  (void)hipFuncSetAttribute((const void*)dummy_kernel, hipFuncAttributeMaxDynamicSharedMemorySize, LDS_BYTES);
  dummy_kernel<<<256, 512, LDS_BYTES, stream>>>((char*)d_ws);
  fused_kernel<<<256, 512, LDS_BYTES, stream>>>(P);
}
```

```cpp
#include <hip/hip_runtime.h>

typedef unsigned short u16;
typedef unsigned int u32;
typedef unsigned long long u64;
using bf16x8 = __attribute__((ext_vector_type(8))) short;
using f32x16 = __attribute__((ext_vector_type(16))) float;
using u32x4 = __attribute__((ext_vector_type(4))) unsigned;
using u32x2 = __attribute__((ext_vector_type(2))) unsigned;
typedef short v4i16_t __attribute__((ext_vector_type(4)));
typedef __bf16 bf16x2_t __attribute__((ext_vector_type(2)));
typedef float f32x2 __attribute__((ext_vector_type(2)));
#define LAS __attribute__((address_space(3)))
#define GAS __attribute__((address_space(1)))
#define DI __device__ __forceinline__
#define MFMA(a, b, c) __builtin_amdgcn_mfma_f32_32x32x16_bf16((a), (b), (c), 0, 0, 0)
#define SBAR() __builtin_amdgcn_sched_barrier(0)
#define PIN(x) asm volatile("" : "+v"(x))
#define PIN2(a, b) asm volatile("" : "+v"(a), "+v"(b))
#define PIN4(a, b, c, d) asm volatile("" : "+v"(a), "+v"(b), "+v"(c), "+v"(d))
#define MPIN(x) asm volatile("s_nop 7\n\ts_nop 7" : "+v"(x))
#define MPIN2(a, b) asm volatile("s_nop 7\n\ts_nop 7" : "+v"(a), "+v"(b))
#define MPIN4(a, b, c, d) asm volatile("s_nop 7\n\ts_nop 7" : "+v"(a), "+v"(b), "+v"(c), "+v"(d))
#define VMCNT(n) asm volatile("s_waitcnt vmcnt(" #n ")" ::: "memory")
#define LGKM0() asm volatile("s_waitcnt lgkmcnt(0)" ::: "memory")
#define EX(x) __builtin_amdgcn_exp2f(x)

#define NOLOAD 0
#define THR 4
#define KV_AUX 16
#define H_AUX 16
#define FENCE 0
#define ABL 0
#define G1_PRIO 0
#define AUX3_PRIO 2
#define QEARLY 1
#define OUT_AUX 2
#define POLL_SLEEP 2
#define WARM 1
#define PREF 0
#define XEARLY 2
#define XD 4
#define CEARLY 3
#define ROLLED 0
#define KV_DEPTH 6
#define AUX_PRIO 2
#define AUX2_PRIO 0
#define GATE 0
#define KV_REPL 32

constexpr u32 WS_EPOCH = 0;
constexpr u32 WS_HFLAG = 7u << 20;
constexpr u32 WS_OFLAG = WS_HFLAG + 131072;
constexpr u32 WS_KVFLAG = WS_OFLAG + 131072;
constexpr u32 WS_AGG = 32768;
constexpr u32 WS_KVF = WS_AGG + 131072;
constexpr u32 WS_OF = WS_KVF + 262144;
constexpr u32 WS_HF = WS_OF + 2097152;
constexpr u32 WS_END = WS_KVFLAG + 32768;
constexpr u32 WS_DBG = 8u << 20;

constexpr u32 L_XB = 0;
constexpr u32 L_WQ = 57344;
constexpr u32 L_C = 107520;
constexpr u32 L_MISC = L_C + 55296;
constexpr u32 L_AGG = L_MISC + 128;
constexpr u32 L_KN = L_MISC + 896;
constexpr u32 L_CARRY = L_C + 20480;
constexpr u32 L_PRIV = 26880;
constexpr u32 L_WO_OFF = 13440;
constexpr u32 L_QX = L_C + 54 * 864;
constexpr u32 LDS_BYTES = 163840;
enum { C_WQ = 0, C_Q2, C_QDONE, C_BFULL, C_AGG, C_WO, C_O, C_MAX, C_P, C_Y, C_FB, C_KVP, C_KMAX, C_KV, C_OF, C_HF, C_WQ2, C_HD, C_KVL, C_KVR, C_KVD, C_C3, C_CARRY, C_EPOCH = 31 };

struct Params {
  const u16 *Ax, *lre, *lim, *Bre, *Bim, *Cre, *Cim, *ldt, *Dsk, *Bx, *wq, *wk, *wv, *wo;
  float* out;
  char* ws;
};

DI float bf2f(u16 v) { return __uint_as_float(((u32)v) << 16); }
DI u32 cvtpk(float a, float b) { return __builtin_bit_cast(u32, __builtin_convertvector((f32x2){a, b}, bf16x2_t)); }
DI float rbf(float a) { return __uint_as_float(cvtpk(a, 0.f) << 16); }
DI bf16x8 frag(u32x4 v) { return __builtin_bit_cast(bf16x8, v); }
DI void glds16(const void* sbase, u32 voff, u32 lds_base) {
  asm volatile("s_mov_b32 m0, %2\n\ts_nop 0\n\tglobal_load_lds_dwordx4 %0, %1" :: "v"(voff), "s"(sbase), "s"(lds_base) : "memory");
}
DI void glds4(const void* sbase, u32 voff, u32 lds_base) {
  asm volatile("s_mov_b32 m0, %2\n\ts_nop 0\n\tglobal_load_lds_dword %0, %1" :: "v"(voff), "s"(sbase), "s"(lds_base) : "memory");
}
template <int N> DI void vmwait() { asm volatile("s_waitcnt vmcnt(%0)" :: "i"(N) : "memory"); }
DI void vmwait_rt(int n) {
  switch (n) {
    case 0: vmwait<0>(); break; case 4: vmwait<4>(); break; case 8: vmwait<8>(); break; case 12: vmwait<12>(); break;
    case 16: vmwait<16>(); break; case 20: vmwait<20>(); break; default: vmwait<0>(); break;
  }
}
DI u32x4 ldsr128(u32 a) { return *(const LAS u32x4*)(size_t)a; }
DI void ldsw128(u32 a, u32x4 v) { *(LAS u32x4*)(size_t)a = v; }
DI u32 ldsu(u32 a) { return (u32)__builtin_amdgcn_readfirstlane(*(volatile LAS u32*)(size_t)a); }
DI u32x2 ldstr(u32 a) { return __builtin_bit_cast(u32x2, __builtin_amdgcn_ds_read_tr16_b64_v4i16((LAS v4i16_t*)(size_t)a)); }
DI u32x4 trfrag(u32 a0, u32 a1) { u32x2 lo = ldstr(a0), hi = ldstr(a1); return (u32x4){lo.x, lo.y, hi.x, hi.y}; }
DI u32x4 f4(const f32x16& v, int g) {
  return (u32x4){__float_as_uint(v[4 * g]), __float_as_uint(v[4 * g + 1]), __float_as_uint(v[4 * g + 2]), __float_as_uint(v[4 * g + 3])};
}
DI void add4(f32x16& v, int g, u32x4 a) {
  v[4 * g] += __uint_as_float(a.x); v[4 * g + 1] += __uint_as_float(a.y); v[4 * g + 2] += __uint_as_float(a.z); v[4 * g + 3] += __uint_as_float(a.w);
}
DI void arrive(int c, int lane) {
  asm volatile("s_waitcnt lgkmcnt(0)" ::: "memory");
  if (lane == 0) asm volatile("ds_add_u32 %0, %1" :: "v"(L_MISC + 4 * c), "v"(1u) : "memory");
}
DI void await(int c, u32 target) {
#pragma unroll 1
  for (u32 spin = 0; spin < (1u << 22); ++spin) {
    asm volatile("" ::: "memory");
    if (ldsu(L_MISC + 4 * c) >= target) break;
    __builtin_amdgcn_s_sleep(1);
  }
  asm volatile("" ::: "memory");
}
DI float swap32(float v, bool hi) {
  auto r = __builtin_amdgcn_permlane32_swap(__float_as_uint(v), __float_as_uint(v), false, false);
  return __uint_as_float(hi ? r[0] : r[1]);
}
DI float wave_max_nonneg(float v) {
  u32 x = __float_as_uint(v);
  x = max(x, (u32)__builtin_amdgcn_update_dpp(0, (int)x, 0x111, 0xf, 0xf, false));
  x = max(x, (u32)__builtin_amdgcn_update_dpp(0, (int)x, 0x112, 0xf, 0xf, false));
  x = max(x, (u32)__builtin_amdgcn_update_dpp(0, (int)x, 0x114, 0xf, 0xf, false));
  x = max(x, (u32)__builtin_amdgcn_update_dpp(0, (int)x, 0x118, 0xf, 0xf, false));
  x = max(x, (u32)__builtin_amdgcn_update_dpp(0, (int)x, 0x142, 0xa, 0xf, false));
  x = max(x, (u32)__builtin_amdgcn_update_dpp(0, (int)x, 0x143, 0xc, 0xf, false));
  return __uint_as_float((u32)__builtin_amdgcn_readlane((int)x, 63));
}
DI void gstore_flag(char* ws, u32 off, u32 v) { __hip_atomic_store((GAS u32*)(ws + off), v, __ATOMIC_RELAXED, __HIP_MEMORY_SCOPE_AGENT); }
DI void poll_words(char* ws, u32 off, int n, u32 tag, int lane) {
  GAS u32* p = (GAS u32*)(ws + off) + (lane < n ? lane : 0) * 32;
  u32 a = __hip_atomic_load(p, __ATOMIC_RELAXED, __HIP_MEMORY_SCOPE_AGENT);
#pragma unroll 1
  for (u32 spin = 0; spin < (1u << 16); ++spin) {
    __builtin_amdgcn_s_sleep(POLL_SLEEP);
    u32 c = __hip_atomic_load(p, __ATOMIC_RELAXED, __HIP_MEMORY_SCOPE_AGENT);
    SBAR();
    if (__all(a == tag)) break;
    __builtin_amdgcn_s_sleep(POLL_SLEEP);
    a = __hip_atomic_load(p, __ATOMIC_RELAXED, __HIP_MEMORY_SCOPE_AGENT);
    SBAR();
    if (__all(c == tag)) break;
  }
  asm volatile("" ::: "memory");
}
DI float poll_kv(char* ws, int rep, u32 tag, int lane) {
  GAS u64* p = (GAS u64*)(ws + WS_KVFLAG + rep * 4096) + (lane & 31) * 16;
  u64 a = __hip_atomic_load(p, __ATOMIC_RELAXED, __HIP_MEMORY_SCOPE_AGENT), g = 0;
#pragma unroll 1
  for (u32 spin = 0; spin < (1u << 16); ++spin) {
    __builtin_amdgcn_s_sleep(POLL_SLEEP);
    u64 c = __hip_atomic_load(p, __ATOMIC_RELAXED, __HIP_MEMORY_SCOPE_AGENT);
    SBAR();
    g = a;
    if (__all((u32)a == tag)) break;
    __builtin_amdgcn_s_sleep(POLL_SLEEP);
    a = __hip_atomic_load(p, __ATOMIC_RELAXED, __HIP_MEMORY_SCOPE_AGENT);
    SBAR();
    g = c;
    if (__all((u32)c == tag)) break;
  }
  asm volatile("" ::: "memory");
  return __uint_as_float((u32)(g >> 32));
}
DI void cmul(float ar, float ai, float br, float bi, float& cr, float& ci) {
  float x, y;
  asm("v_mul_f32 %0, %2, %4\n\tv_mul_f32 %1, %2, %5\n\tv_fma_f32 %0, -%3, %5, %0\n\tv_fma_f32 %1, %3, %4, %1"
      : "=&v"(x), "=&v"(y) : "v"(ar), "v"(ai), "v"(br), "v"(bi));
  cr = x; ci = y;
}
DI void cstep(float ar, float ai, float& hr, float& hi, float ur, float ui) {
  float x, y;
  asm("v_fma_f32 %0, -%3, %5, %6\n\tv_fma_f32 %1, %3, %4, %7\n\tv_fma_f32 %0, %2, %4, %0\n\tv_fma_f32 %1, %2, %5, %1"
      : "=&v"(x), "=&v"(y) : "v"(ar), "v"(ai), "v"(hr), "v"(hi), "v"(ur), "v"(ui));
  hr = x; hi = y;
}
DI void set_pad(u32x4& f, float m, int h) {
  const float a = rbf(-m), b = rbf(-m - a), c = rbf(-m - a - b);
  if (h) { f.z = cvtpk(a, b); f.w = cvtpk(c, 0.f); }
}

#define TS(i) do {} while (0)
#define TSD() do {} while (0)
#define TSW() do {} while (0)
#define LPRIO 0
#define PUP() do {} while (0)
#define PDN() SBAR()
#define STR_(x) #x
#define VMTHR_(n) asm volatile("s_waitcnt vmcnt(" STR_(n) ")" ::: "memory")
#define VMTHR() VMTHR_(THR)

template <bool W_IS_A, int WROW, bool MID, class F>
DI f32x16 proj_tile(int row, int h, u32 wa, int lane, F mid) {
  const int sw = (row >> 1) & 7;
  u32 xa[4];
#pragma unroll
  for (int bb = 0; bb < 4; ++bb) xa[bb] = L_XB + row * 128 + (((2 * bb + h) ^ sw) << 4);
  u32x4 xf[28], wf[28];
  f32x16 acc = {};
#pragma unroll
  for (int ks = 0; ks < 28 + 5; ++ks) {
    if (ks >= 5) {
      const int k = ks - 5;
      acc = W_IS_A ? MFMA(frag(wf[k]), frag(xf[k]), acc) : MFMA(frag(xf[k]), frag(wf[k]), acc);
      SBAR();
    }
    if (ks < 28) {
      if (MID && ks == 14) {
        asm volatile("s_nop 7\n\ts_nop 7" ::: "memory");
        mid();
        VMCNT(0);
        if (lane == 0) asm volatile("ds_add_u32 %0, %1" :: "v"(L_MISC + 4 * C_WQ2), "v"(1u) : "memory");
        await(C_WQ2, 4);
      }
      xf[ks] = ldsr128(xa[ks & 3] + 8192 * (ks >> 2));
      wf[ks] = trfrag(wa + ks * 16 * WROW, wa + ks * 16 * WROW + 4 * WROW);
      SBAR();
    }
  }
  MPIN(acc);
  return acc;
}

__global__ void __launch_bounds__(512, 2) fused_kernel(Params P) {
  extern __shared__ __attribute__((aligned(16))) char lds_dyn[];
  u64 tfirst; asm volatile("s_memrealtime %0\n\ts_waitcnt lgkmcnt(0)" : "=s"(tfirst) :: "memory");
  asm volatile("" : "+s"(P.Ax), "+s"(P.lre), "+s"(P.lim), "+s"(P.Bre), "+s"(P.Bim), "+s"(P.Cre), "+s"(P.Cim), "+s"(P.ldt));
  asm volatile("" : "+s"(P.Dsk), "+s"(P.Bx), "+s"(P.wq), "+s"(P.wk), "+s"(P.wv), "+s"(P.wo), "+s"(P.out), "+s"(P.ws));
  const int tid = threadIdx.x, lane = tid & 63, wv = __builtin_amdgcn_readfirstlane(tid >> 6);
  const int r = lane & 31, h = lane >> 5;
  const int b = blockIdx.x;
  const int tb = (b & 7) * 4 + (b >> 6), sl = (b >> 3) & 7;
  char* ws = P.ws;
  const int tl = b >> 6;
  TSD();
  TS(0);
  if (tid == 0) asm volatile("s_mov_b32 m0, %2\n\ts_nop 0\n\tglobal_load_lds_dword %0, %1 sc1" :: "v"(0), "s"(ws), "s"(L_MISC + 4 * C_EPOCH) : "memory");
  const u32 vo = lane * 16;
  {
    const int row = 8 * wv + (lane >> 3);
    const u32 xv = row * 896 + (((lane & 7) ^ ((row >> 1) & 7)) << 4);
    const char* src = (const char*)P.Bx + (size_t)tb * 64 * 896;
#pragma unroll
    for (int j = 0; j < 7; ++j) {
      int c = j + sl; c = c >= 7 ? c - 7 : c;
      glds16(src + c * 128, xv, L_XB + c * 8192 + wv * 1024);
    }
  }
  if (tid < 31) *(LAS u32*)(size_t)(L_MISC + 4 * tid) = 0;
  const auto rs_ws = __builtin_amdgcn_make_buffer_rsrc((void*)ws, 0, WS_END, 0x00020000);
  const auto rs_out = __builtin_amdgcn_make_buffer_rsrc((void*)P.out, 0, 2048 * 896 * 4, 0x00020000);
  const int g1 = (lane >> 4) & 1, q4 = (lane >> 2) & 3, p4 = lane & 3;
  const int l7 = lane / 7;
  const u32 vo7 = l7 * 896 + (lane - 7 * l7) * 16;

  if (wv < 4) {
    const int w = wv;
    const char* wqsrc = (const char*)P.wq + sl * 112 + w * 7 * 7168;
    auto stage_wq = [&](int hf) {
      if (lane < 56) {
#pragma unroll
        for (int j = 0; j < 7; ++j) {
          int i = j + 2 * tl; i = (i >= 7 ? i - 7 : i) + 28 * hf;
          glds16(wqsrc + i * 7168, vo7, L_WQ + (w * 7 + i) * 896);
        }
      }
    };
    if (sl != 7) {
      stage_wq(0); stage_wq(1);
      asm volatile("s_waitcnt vmcnt(14) lgkmcnt(0)\n\ts_barrier" ::: "memory");
    } else {
      stage_wq(0);
      asm volatile("s_waitcnt vmcnt(7) lgkmcnt(0)\n\ts_barrier" ::: "memory");
    }
    const u32 tag = ldsu(L_MISC + 4 * C_EPOCH) + 1u;
    if (sl != 7) VMCNT(7); else VMCNT(0);
    arrive(C_WQ, lane);
    await(C_WQ, 4);
    TS(15);
    {
      f32x16 acc = proj_tile<true, 112, true>(32 * (w & 1) + r, h, L_WQ + (8 * h + q4) * 112 + (w >> 1) * 56 + g1 * 32 + 8 * p4, lane,
                                            [&]() { if (sl == 7) { await(C_KVD, 4); stage_wq(1); } });
      TS(8);
      const float qs = 0.27264060f;
      float n2 = 0.f;
#pragma unroll
      for (int i = 0; i < 16; ++i) { acc[i] *= qs; if (i < 12) n2 = fmaf(acc[i], acc[i], n2); else n2 = h ? n2 : fmaf(acc[i], acc[i], n2); }
      n2 += swap32(n2, h);
      u32x4 f0 = {cvtpk(acc[0], acc[1]), cvtpk(acc[2], acc[3]), cvtpk(acc[4], acc[5]), cvtpk(acc[6], acc[7])};
      u32x4 f1 = {cvtpk(acc[8], acc[9]), cvtpk(acc[10], acc[11]), cvtpk(acc[12], acc[13]), cvtpk(acc[14], acc[15])};
      if (h) { f1.z = 0; f1.w = __float_as_uint(__builtin_amdgcn_sqrtf(n2)); }
      ldsw128(L_QX + w * 2048 + vo, f0);
      ldsw128(L_QX + w * 2048 + 1024 + vo, f1);
      arrive(C_Q2, lane);
    }
    u32x4 qf[4][2];
    if (QEARLY) {
      await(C_Q2, 4);
#pragma unroll
      for (int t = 0; t < 4; ++t) { const u32 a = L_QX + ((w + t) & 3) * 2048 + vo; qf[t][0] = ldsr128(a); qf[t][1] = ldsr128(a + 1024); }
      arrive(C_QDONE, lane);
    }
    TS(2);
    if (w == 0) {
      const float kn = wave_max_nonneg(poll_kv(ws, b & 7, tag, lane));
      if (lane == 0) *(LAS float*)(size_t)L_KN = kn;
      arrive(C_KV, lane);
    } else {
      await(C_KV, 1);
    }
    TS(3);
    const u32 kvbase = WS_KVF + w * 65536;
    const int rot = b >> 3;
#define KVOFF(it) (kvbase + ((((it) + rot) & 15) << 12))
#define LDK(it, s) __builtin_amdgcn_raw_buffer_load_b128(rs_ws, vo + 1024 * (s), KVOFF(it), KV_AUX)
#define LDV(it, s) __builtin_amdgcn_raw_buffer_load_b128(rs_ws, vo + 2048 + 1024 * (s), KVOFF(it), KV_AUX)
#define NEXT(x) asm volatile("s_add_u32 %0, %0, 0x1000\n\ts_and_b32 %0, %0, 0xf000" : "+s"(x) :: "scc")
#define LDK2(s_) __builtin_amdgcn_raw_buffer_load_b128(rs_kv, vo + 1024 * (s_), koff, KV_AUX)
#define LDV2(s_) __builtin_amdgcn_raw_buffer_load_b128(rs_kv, vo + 2048 + 1024 * (s_), voff, KV_AUX)
    const auto rs_kv = __builtin_amdgcn_make_buffer_rsrc((void*)(ws + kvbase), 0, 65536, 0x00020000);
    f32x16 o[4];
    for (int attempt = 0; attempt < 2; ++attempt) {
      u32x4 kf[KV_DEPTH][2], vf[KV_DEPTH][2];
#pragma unroll
      for (int d = 0; d < KV_DEPTH; ++d) {
        kf[d][0] = LDK(d, 0); kf[d][1] = LDK(d, 1);
        if (d < KV_DEPTH - 1) { vf[d][0] = LDV(d, 0); vf[d][1] = LDV(d, 1); }
      }
      u32 koff = ((KV_DEPTH - 1 + rot) & 15) << 12, voff = ((KV_DEPTH - 2 + rot) & 15) << 12;
      SBAR();
      if (attempt == 0) {
        arrive(C_KVL, lane);
        if (!QEARLY) {
        await(C_Q2, 4);
#pragma unroll
        for (int t = 0; t < 4; ++t) { const u32 a = L_QX + ((w + t) & 3) * 2048 + vo; qf[t][0] = ldsr128(a); qf[t][1] = ldsr128(a + 1024); }
        arrive(C_QDONE, lane);
        }
        const float kmax = __builtin_amdgcn_sqrtf(*(LAS float*)(size_t)L_KN);
#pragma unroll
        for (int t = 0; t < 4; ++t) set_pad(qf[t][1], __uint_as_float(qf[t][1].w) * kmax, h);
      }
#pragma unroll
      for (int t = 0; t < 4; ++t) { PIN(qf[t][0]); PIN(qf[t][1]); }
      SBAR();
      const f32x16 zero = {};
      f32x16 sc = MFMA(frag(kf[0][0]), frag(qf[0][0]), zero);
      sc = MFMA(frag(kf[0][1]), frag(qf[0][1]), sc);
      TS(9);
      float e0 = 0.f, e1 = 0.f, e2 = 0.f, e3 = 0.f;
      u32x4 p1 = {0, 0, 0, 0};
#pragma unroll
      for (int it = 0; it < 16; ++it) {
        const int d = it % KV_DEPTH, dprev = (it + KV_DEPTH - 1) % KV_DEPTH;
#pragma unroll
        for (int qi = 0; qi < 4; ++qi) {
          const int dn = qi == 3 ? (it + 1) % KV_DEPTH : d, qn = (qi + 1) & 3;
          const int dp = qi == 0 ? dprev : d, qp = (qi + 3) & 3;
          const bool last = it == 15 && qi == 3;
          f32x16 sn; u32x4 p0;
          float c0, c1, c2, c3;
          if (!last) sn = MFMA(frag(kf[dn][0]), frag(qf[qn][0]), zero);
          SBAR();
          p1.z = cvtpk(e0, e1); p1.w = cvtpk(e2, e3); PIN(p1); SBAR();
          c0 = EX(sc[0]); c1 = EX(sc[1]); c2 = EX(sc[2]); c3 = EX(sc[3]); PIN4(c0, c1, c2, c3); SBAR();
          if (it > 0 || qi > 0) o[qp] = MFMA(frag(vf[dp][1]), frag(p1), o[qp]);
          SBAR();
          p0.x = cvtpk(c0, c1); p0.y = cvtpk(c2, c3); PIN2(p0.x, p0.y); SBAR();
          c0 = EX(sc[4]); c1 = EX(sc[5]);
          if (!NOLOAD && qi < 2 && it - 1 + KV_DEPTH < 16) { PIN2(c0, c1); SBAR(); if (qi == 0) NEXT(voff); vf[dprev][qi] = LDV2(qi); SBAR(); }
          c2 = EX(sc[6]); c3 = EX(sc[7]); PIN4(c0, c1, c2, c3); SBAR();
          if (!last) sn = MFMA(frag(kf[dn][1]), frag(qf[qn][1]), sn);
          SBAR();
          p0.z = cvtpk(c0, c1); p0.w = cvtpk(c2, c3); PIN(p0); SBAR();
          c0 = EX(sc[8]); c1 = EX(sc[9]);
          if (!NOLOAD && qi == 2 && it + KV_DEPTH < 16) { PIN2(c0, c1); SBAR(); NEXT(koff); kf[d][0] = LDK2(0); SBAR(); }
          c2 = EX(sc[10]); c3 = EX(sc[11]); PIN4(c0, c1, c2, c3); SBAR();
          o[qi] = MFMA(frag(vf[d][0]), frag(p0), it == 0 ? zero : o[qi]); SBAR();
          p1.x = cvtpk(c0, c1); p1.y = cvtpk(c2, c3); PIN2(p1.x, p1.y); SBAR();
          e0 = EX(sc[12]); e1 = EX(sc[13]);
          if (!NOLOAD && qi == 2 && it + KV_DEPTH < 16) { PIN2(e0, e1); SBAR(); kf[d][1] = LDK2(1); SBAR(); }
          e2 = EX(sc[14]); e3 = EX(sc[15]); PIN4(e0, e1, e2, e3); SBAR();
          if (!last) sc = sn;
        }
      }
      p1.z = cvtpk(e0, e1); p1.w = cvtpk(e2, e3);
      o[3] = MFMA(frag(vf[15 % KV_DEPTH][1]), frag(p1), o[3]);
      MPIN4(o[0], o[1], o[2], o[3]);
      TS(4);
      u32 lv;
      asm volatile("v_mbcnt_lo_u32_b32 %0, -1, 0\n\tv_mbcnt_hi_u32_b32 %0, -1, %0\n\tv_lshlrev_b32 %0, 4, %0" : "=v"(lv));
      if (attempt == 0) {
        bool bad = false;
#pragma unroll
        for (int t = 0; t < 4; ++t) bad |= !(o[t][12] >= 0x1p-80f && o[t][12] <= 0x1p100f);
        if (__any(bad && lv >= 512)) { if (lv == 0) *(LAS u32*)(size_t)(L_MISC + 4 * C_FB) = 1u; }
        await(C_HD, 4);
      }
#pragma unroll
      for (int t = 1; t < 4; ++t) {
#pragma unroll
        for (int g = 0; g < 4; ++g) ldsw128(L_C + w * 12288 + (t - 1) * 4096 + g * 1024 + lv, f4(o[t], g));
      }
      arrive(C_O, lv);
      await(C_O, 4 * (attempt + 1));
      TS(10);
      const u32 fb = attempt == 0 ? ldsu(L_MISC + 4 * C_FB) : 0u;
      if (!fb) break;
      {
        float mx[4];
#pragma unroll
        for (int t = 0; t < 4; ++t) { mx[t] = -3.0e38f; if (lv >= 512) { qf[t][1].z = 0; qf[t][1].w = 0; } }
#pragma unroll 1
        for (int it = 0; it < 16; ++it) {
          const u32x4 k0 = __builtin_amdgcn_raw_buffer_load_b128(rs_kv, lv, it << 12, KV_AUX), k1 = __builtin_amdgcn_raw_buffer_load_b128(rs_kv, lv + 1024, it << 12, KV_AUX);
#pragma unroll
          for (int t = 0; t < 4; ++t) {
            f32x16 s = MFMA(frag(k0), frag(qf[t][0]), zero);
            s = MFMA(frag(k1), frag(qf[t][1]), s);
#pragma unroll
            for (int i = 0; i < 16; ++i) mx[t] = fmaxf(mx[t], s[i]);
          }
        }
        const u32 ma = L_C + 49152;
#pragma unroll
        for (int t = 0; t < 4; ++t) { mx[t] = fmaxf(mx[t], swap32(mx[t], lv >= 512)); *(LAS float*)(size_t)(ma + (w * 4 + ((w + t) & 3)) * 256 + (lv >> 2)) = mx[t]; }
        arrive(C_MAX, lv);
        await(C_MAX, 4);
#pragma unroll
        for (int t = 0; t < 4; ++t) {
          float m = -3.0e38f;
#pragma unroll
          for (int ww = 0; ww < 4; ++ww) m = fmaxf(m, *(LAS float*)(size_t)(ma + (ww * 4 + ((w + t) & 3)) * 256 + (lv >> 2)));
          set_pad(qf[t][1], m, lv >= 512);
        }
      }
    }
    {
    int lane;
    asm volatile("v_mbcnt_lo_u32_b32 %0, -1, 0\n\tv_mbcnt_hi_u32_b32 %0, -1, %0" : "=v"(lane));
    const int r = lane & 31, h = lane >> 5, g1 = (lane >> 4) & 1, q4 = (lane >> 2) & 3, p4 = lane & 3;
    const u32 vo = lane * 16;
    {
      f32x16 own = o[0];
#pragma unroll
      for (int pp = 1; pp < 4; ++pp) {
        const u32 a = L_C + ((w + pp) & 3) * 12288 + (3 - pp) * 4096 + vo;
#pragma unroll
        for (int g = 0; g < 4; ++g) add4(own, g, ldsr128(a + g * 1024));
      }
      const float other = swap32(own[12], h);
      const float inv = __builtin_amdgcn_rcpf(h ? own[12] : other);
      u32x4 f0 = {cvtpk(own[0] * inv, own[1] * inv), cvtpk(own[2] * inv, own[3] * inv), cvtpk(own[4] * inv, own[5] * inv), cvtpk(own[6] * inv, own[7] * inv)};
      u32x4 f1 = {cvtpk(own[8] * inv, own[9] * inv), cvtpk(own[10] * inv, own[11] * inv), cvtpk(own[12] * inv, own[13] * inv), cvtpk(own[14] * inv, own[15] * inv)};
      if (h) { f1.z = 0; f1.w = 0; }
      const u32 so = WS_OF + ((tb * 2 + (w & 1)) * 16 + 2 * sl + (w >> 1)) * 2048 + vo;
      __builtin_amdgcn_raw_buffer_store_b128(f0, rs_ws, so, 0, 16);
      __builtin_amdgcn_raw_buffer_store_b128(f1, rs_ws, so + 1024, 0, 16);
    }
    {
      const int tm = w & 1, kh = w >> 1;
      TS(11);
      VMCNT(0);
      if (lane == 0) gstore_flag(ws, WS_OFLAG + (tb * 32 + sl * 4 + w) * 128, tag);
      TS(5);
      await(C_WO, 4);
      u32x4 wf[16][2];
      const u32 wa = 2 * kh * L_PRIV + L_WO_OFF + (4 * h + q4) * 112 + g1 * 32 + 8 * p4;
      const u32 wa0 = wa + 64 * kh, wa1 = wa + 64 * (1 - kh);
#pragma unroll
      for (int i = 0; i < 16; ++i) {
        const u32 a = (i >> 3) * L_PRIV + (28 * ((i >> 1) & 3) + 16 * (i & 1)) * 112;
        wf[i][0] = trfrag(wa0 + a, wa0 + a + 896); wf[i][1] = trfrag(wa1 + a, wa1 + a + 896);
      }
#pragma unroll
      for (int i = 0; i < 16; ++i) PIN2(wf[i][0], wf[i][1]);
      if (w == 0) { poll_words(ws, WS_OFLAG + tb * 4096, 32, tag, lane); arrive(C_OF, lane); }
      else await(C_OF, 1);
      TS(6);
      u32x4 of[16];
      const u32 so = WS_OF + ((tb * 2 + tm) * 16 + 8 * kh) * 2048;
#pragma unroll
      for (int i = 0; i < 16; ++i) of[i] = __builtin_amdgcn_raw_buffer_load_b128(rs_ws, vo + (i & 3) * 1024, so + (i >> 2) * 4096, H_AUX);
      SBAR();
      f32x16 a0 = {}, a1 = {};
#pragma unroll
      for (int i = 0; i < 16; ++i) {
        a0 = MFMA(frag(of[i]), frag(wf[i][0]), a0);
        a1 = MFMA(frag(of[i]), frag(wf[i][1]), a1);
      }
      MPIN2(a0, a1);
      TS(12);
      const f32x16 snd = a1;
      f32x16 own = a0;
      const u32 aw = L_C + w * 4096 + vo, ar = L_C + (w ^ 2) * 4096 + vo;
#pragma unroll
      for (int g = 0; g < 4; ++g) ldsw128(aw + g * 1024, f4(snd, g));
      arrive(C_P, lane);
      await(C_P, 4);
#pragma unroll
      for (int g = 0; g < 4; ++g) add4(own, g, ldsr128(ar + g * 1024));
      if (32 * kh + r < 56) {
        const u32 ov = ((tb * 64 + 32 * tm + 4 * h) * 896 + 448 + 56 * sl + 32 * kh + r) * 4;
#pragma unroll
        for (int i = 0; i < 16; ++i) __builtin_amdgcn_raw_buffer_store_b32(__float_as_uint(rbf(own[i])), rs_out, ov, ((i & 3) + 8 * (i >> 2)) * 3584, OUT_AUX);
      }
    }
    }
    TS(7);
    TSW();
    if (b == 0 && tid == 0) __hip_atomic_store((GAS u64*)(ws + 2048), tfirst, __ATOMIC_RELAXED, __HIP_MEMORY_SCOPE_AGENT);
  } else {
    __builtin_amdgcn_s_setprio(AUX_PRIO);
    const int q = wv - 4;
    const u32 PA = q * L_PRIV;
    const int part = q >> 1, half = q & 1;
    if (sl == 7 && lane < 56) {
      const char* src = (const char*)((q >> 1) ? P.wv : P.wk) + (q & 1) * 14 * 896;
#pragma unroll
      for (int j = 0; j < 14; ++j) {
        int i = j + 4 * tl; i = i >= 14 ? i - 14 : i;
        glds16(src + i * 896, vo, L_C + (q * 14 + i) * 896);
      }
    }
    asm volatile("s_waitcnt vmcnt(0) lgkmcnt(0)\n\ts_barrier" ::: "memory");
    const u32 tag = ldsu(L_MISC + 4 * C_EPOCH) + 1u;
    TS(13);
    if (sl == 7) {
      const int kind = q >> 1, ktm = q & 1;
      const u32 wa = L_C + kind * 25088 + (8 * h + q4) * 56 + g1 * 32 + 8 * p4;
      f32x16 acc;
      if (kind == 0) acc = proj_tile<true, 56, false>(32 * ktm + r, h, wa, lane, []() {});
      else acc = proj_tile<false, 56, false>(32 * ktm + r, h, wa, lane, []() {});
      u32x4 f0 = {cvtpk(acc[0], acc[1]), cvtpk(acc[2], acc[3]), cvtpk(acc[4], acc[5]), cvtpk(acc[6], acc[7])};
      u32x4 f1 = {cvtpk(acc[8], acc[9]), cvtpk(acc[10], acc[11]), cvtpk(acc[12], acc[13]), cvtpk(acc[14], acc[15])};
      if (kind == 0) {
        if (h) { f1.z = 0x3F803F80u; f1.w = 0x00003F80u; }
      } else if (r >= 28) {
        const u32 c = r == 28 ? 0x3F803F80u : 0u;
        f0 = (u32x4){c, c, c, c}; f1 = f0;
      }
      const u32 so = WS_KVF + (tb * 2 + ktm) * 4096 + kind * 2048 + vo;
      __builtin_amdgcn_raw_buffer_store_b128(f0, rs_ws, so, 0, 16);
      __builtin_amdgcn_raw_buffer_store_b128(f1, rs_ws, so + 1024, 0, 16);
      SBAR();
      TS(12);
      if (kind == 0) {
        float n2 = 0.f;
#pragma unroll
        for (int i = 0; i < 16; ++i) { if (i < 12) n2 = fmaf(acc[i], acc[i], n2); else n2 = h ? n2 : fmaf(acc[i], acc[i], n2); }
        n2 += swap32(n2, h);
        n2 = wave_max_nonneg(n2);
        if (lane == 0) asm volatile("ds_max_u32 %0, %1" :: "v"(L_MISC + 4 * C_KMAX), "v"(n2) : "memory");
      }
      VMCNT(0);
      u32 old = 0;
      if (lane == 0) asm volatile("s_waitcnt lgkmcnt(0)\n\tds_add_rtn_u32 %0, %1, %2\n\ts_waitcnt lgkmcnt(0)" : "=&v"(old) : "v"(L_MISC + 4 * C_KVP), "v"(1u) : "memory");
      if (__builtin_amdgcn_readfirstlane(old) == 3) {
        const u32 km = ldsu(L_MISC + 4 * C_KMAX);
        if (lane < 8) __hip_atomic_store((GAS u64*)(ws + WS_KVFLAG + lane * 4096 + tb * 128), (u64)tag | ((u64)km << 32), __ATOMIC_RELAXED, __HIP_MEMORY_SCOPE_AGENT);
      }
      TS(14);
      arrive(C_KVD, lane);
      await(C_KVD, 4);
    } else if (PREF) {
      const int j = (b >> 3) - tl;
      const char* p0 = (const char*)(q == 0 ? P.Bre : q == 1 ? P.Bim : q == 2 ? P.Cre : P.Cim) + j * 14336;
      const u32 o2 = lane < 48 ? 8192 + lane * 128 : 0;
      glds4(p0, lane * 128, L_AGG + 512);
      glds4(p0, o2, L_AGG + 512);
      if (q == 0) glds4((const char*)P.Ax + (size_t)(b & 7) * 229376 + j * 8192, lane * 128, L_AGG + 512);
      if (q == 1) glds4((const char*)P.wo + j * 14336, lane * 128, L_AGG + 512);
      if (q == 2) glds4((const char*)P.wo + j * 14336, o2, L_AGG + 512);
    }

    u32 cva;
    {
      const int hi = lane >= 28, cs = lane - 28 * hi;
      cva = hi * 896 + (cs << 4);
    }
    auto stage_C4 = [&](int qq, int g) {
      const char* csrc = (const char*)((qq >> 1) ? P.Cim : P.Cre) + (size_t)sl * 56 * 896 + (qq & 1) * 448;
      int p0 = 4 * g + 8 * tl; p0 = p0 >= 28 ? p0 - 28 : p0;
      const u32 va = cva ^ ((p0 & 4) << 3), vb = va ^ 16;
      if (lane < 56) {
#pragma unroll
        for (int j = 0; j < 4; ++j) { glds16(csrc + (p0 + j) * 1792, j < 2 ? va : vb, qq * L_PRIV + (p0 + j) * 896); VMTHR(); }
      }
    };
    const int tm = (q ^ sl) & 1, tn = q >> 1;
    const bool valid = 32 * tn + r < 56;
    const int dcol = 56 * sl + (valid ? 32 * tn + r : 55), t0 = tb * 64 + 32 * tm + 4 * h;
    if (WARM && q == 3) {
      const int k = lane & 7;
      const u32 o1 = k < 4 ? WS_KVF + k * 65536 : k < 6 ? WS_OF + tb * 65536 + (k - 4) * 65024 : k == 6 ? WS_HFLAG : WS_AGG + (((tb & 1) * 7 + sl) & 15) * 8192;
      const u32 o2 = WS_HF + (tb & ~1) * 114688 + (k & 3) * 65536;
      if (lane < 16) {
        asm volatile("s_mov_b32 m0, %2\n\ts_nop 0\n\tglobal_load_lds_dword %0, %1 sc1" :: "v"(lane < 8 ? o1 : o2), "s"(ws), "s"(L_AGG + 512) : "memory");
        glds4((const char*)P.out + (size_t)tb * 229376, (lane & 3) * 65536, L_AGG + 512);
      }
    }
    u32x4 cf[2][14];
    auto read_C = [&]() {
#pragma unroll
      for (int t = 0; t < 2; ++t) {
        int row = 32 * (t ^ part) + r; row = row > 55 ? 55 : row;
        const int x = (row >> 2) & 3;
        const u32 a0 = PA + row * 448 + ((h ^ x) << 4), a1 = PA + row * 448 + (((2 + h) ^ x) << 4);
#pragma unroll
        for (int j = 0; j < 14; ++j) cf[t][j] = ldsr128(((j & 1) ? a1 : a0) + 64 * (j >> 1));
      }
#pragma unroll
      for (int j = 0; j < 14; ++j) PIN2(cf[0][j], cf[1][j]);
      LGKM0();
    };
    if (sl == 7) {
      arrive(C_HD, lane);
      await(C_KVL, 4);
#pragma unroll 1
      for (int g = 0; g < 7; ++g) stage_C4(q, g);
      VMCNT(0);
      read_C();
    } else {
      const int tb2 = tb >> 1, cg = (tb & 1) * 7 + sl, ch = 32 * cg + r;
      const u32 vo1 = (lane ^ 1) * 16;
      float lr, li, cr, ci;
      if (q < 3 && lane < 54) {
#pragma unroll
        for (int j = 0; j < 18; ++j) {
          int i = j + 9 * (tb2 & 1); i = i >= 18 ? i - 18 : i;
          const int row = q * 18 + i;
          glds16((const char*)(row >= 32 ? P.Bim : P.Bre) + (size_t)(cg * 32 + (row & 31)) * 896, (row & 8) ? vo1 : vo, L_C + row * 864);
        }
      }
      u32 vlre = P.lre[ch], vlim = P.lim[ch], vldt = P.ldt[ch];
      u32x4 blast_re = *(const u32x4*)(P.Bre + (size_t)ch * 448 + 432 + 8 * h);
      u32x4 blast_im = *(const u32x4*)(P.Bim + (size_t)ch * 448 + 432 + 8 * h);
      asm volatile("" : "+v"(vlre), "+v"(vlim), "+v"(vldt), "+v"(blast_re), "+v"(blast_im) :: "memory");
      {
        const float dt = rbf(expf(__uint_as_float(vldt << 16))), a = __uint_as_float(vlre << 16), bb = __uint_as_float(vlim << 16);
        const float er = expf(dt * a);
        float sn, cs;
        sincosf(dt * bb, &sn, &cs);
        lr = er * cs; PIN(lr); li = er * sn;
        const float nr = lr - 1.f, ni = li, den = 2.0f / (a * a + bb * bb);
        cr = (nr * a + ni * bb) * den; PIN(cr); ci = (ni * a - nr * bb) * den;
      }
      float l16r = lr, l16i = li;
#pragma unroll
      for (int i = 0; i < 4; ++i) cmul(l16r, l16i, l16r, l16i, l16r, l16i);
      float l32r, l32i, l128r, l128i;
      cmul(l16r, l16i, l16r, l16i, l32r, l32i);
      cmul(l32r, l32i, l32r, l32i, l128r, l128i);
      cmul(l128r, l128i, l128r, l128i, l128r, l128i);
      PIN4(lr, li, cr, ci); PIN4(l16r, l16i, l32r, l32i); PIN2(l128r, l128i);
      const char* xsrc = (const char*)P.Ax + (size_t)(tb2 * 128 + q * 32) * 896;
      const u32 xv0 = (lane >> 3) * 896 + (((lane & 7) ^ (lane >> 4)) << 4), xv1 = xv0 ^ 64;
      const int rho = 16 * ((r >> 2) & 1) + (r & 3) + 4 * (r >> 3), sw = (rho >> 1) & 7;
      u32 xa[4];
#pragma unroll
      for (int bb = 0; bb < 4; ++bb) xa[bb] = PA + rho * 128 + (((2 * bb + h) ^ sw) << 4);
      const int xrot = cg % 6;
      u32 ba[7];
#pragma unroll
      for (int c = 0; c < 7; ++c) {
        int kc = c + xrot; kc = c == 6 ? 6 : kc >= 6 ? kc - 6 : kc;
        ba[c] = L_C + r * 864 + ((h ^ ((r >> 3) & 1)) << 4) + 128 * kc;
        PIN(ba[c]);
      }
      PIN4(xa[0], xa[1], xa[2], xa[3]);
      TS(1);
      await(C_QDONE, 4);
      if (q == 3 && lane < 54) {
#pragma unroll
        for (int i = 22; i < 32; ++i) glds16((const char*)P.Bim + (size_t)(cg * 32 + i) * 896, (i & 8) ? vo1 : vo, L_C + (32 + i) * 864);
      }
      auto xdma = [&](int c) {
#pragma unroll
        for (int j = 0; j < 4; ++j) {
          int kc = c + xrot; kc = c == 6 ? 6 : kc >= 6 ? kc - 6 : kc;
          glds16(xsrc + kc * 128 + j * 7168, (j & 1) ? xv1 : xv0, PA + (c % 6) * 4096 + j * 1024);
        }
      };
      xdma(0); xdma(1);
      vmwait<8>();
      arrive(C_BFULL, lane);
      await(C_BFULL, 4);
      TS(2);
      f32x16 ur = {}, ui = {};
      PIN2(ur, ui);
      __builtin_amdgcn_s_setprio(G1_PRIO);
      {
        u32x4 xf[28], br[28], bi[28];
#pragma unroll
        for (int ks = 0; ks < 28 + 4; ++ks) {
          if (ks >= 4) {
            const int k = ks - 4;
            ur = MFMA(frag(xf[k]), frag(br[k]), ur);
            ui = MFMA(frag(xf[k]), frag(bi[k]), ui);
            SBAR();
          }
          if (ks < 28) {
            if ((ks & 3) == 0) {
              const int c = ks >> 2;
              const int issued = c < 2 ? 1 : (XD + c - 2 > 6 ? 6 : XD + c - 2);
              vmwait_rt(4 * (issued - c));
            }
            if (ks == 7) {
              asm volatile("s_nop 7\n\ts_nop 7" ::: "memory");
              await(C_KVL, 4);
#pragma unroll
              for (int c = 2; c <= XD; ++c) xdma(c);
            }
            if ((ks & 3) == 3 && ks > 7 && XD + (ks >> 2) - 1 <= 6) xdma(XD + (ks >> 2) - 1);
            xf[ks] = ldsr128(xa[ks & 3] + ((ks >> 2) % 6) * 4096);
            br[ks] = ks < 27 ? ldsr128(ba[ks >> 2] + 32 * (ks & 3)) : blast_re;
            bi[ks] = ks < 27 ? ldsr128(ba[ks >> 2] + 27648 + 32 * (ks & 3)) : blast_im;
            SBAR();
          }
        }
      }
      MPIN2(ur, ui);
      __builtin_amdgcn_s_setprio(AUX_PRIO);
      TS(3);
      float ar = 0.f, ai = 0.f;
#pragma unroll
      for (int i = 0; i < 16; ++i) {
        float tr, ti;
        cmul(cr, ci, ur[i], ui[i], tr, ti);
        ur[i] = tr; ui[i] = ti;
        cstep(lr, li, ar, ai, tr, ti);
      }
      const float or_ = swap32(ar, h), oi_ = swap32(ai, h);
      const float lor = h ? or_ : ar, loi = h ? oi_ : ai;
      float a32r = lor, a32i = loi;
      cstep(l16r, l16i, a32r, a32i, h ? ar : or_, h ? ai : oi_);
      if (q < 3 && h == 0) *(LAS f32x2*)(size_t)(L_AGG + q * 256 + r * 8) = (f32x2){a32r, a32i};
      arrive(C_AGG, lane);
      await(C_AGG, 4);
      f32x2 wagg[3];
#pragma unroll
      for (int j = 0; j < 3; ++j) wagg[j] = *(LAS f32x2*)(size_t)(L_AGG + j * 256 + r * 8);
      const u32 aggb = WS_AGG + cg * 8192;
      if (q == 3 && tb2 < 15) {
        float tr = wagg[0].x, ti = wagg[0].y;
        cstep(l32r, l32i, tr, ti, wagg[1].x, wagg[1].y);
        cstep(l32r, l32i, tr, ti, wagg[2].x, wagg[2].y);
        cstep(l32r, l32i, tr, ti, a32r, a32i);
        __hip_atomic_store((GAS u64*)(ws + aggb + tb2 * 512 + r * 16 + h * 8), (u64)__float_as_uint(h ? ti : tr) | ((u64)tag << 32), __ATOMIC_RELAXED, __HIP_MEMORY_SCOPE_AGENT);
      }
      TS(4);
      float hr = 0.f, hi = 0.f;
      if (q < 3) {
#pragma unroll 1
        for (int g = q; g < 7; g += 3) stage_C4(3, g);
        stage_C4(q, 0); stage_C4(q, 1);
        vmwait<8>();
        arrive(C_C3, lane);
#pragma unroll 1
        for (int g = 2; g < 7; ++g) stage_C4(q, g);
        VMCNT(0);
        read_C();
        TS(10);
        await(C_CARRY, 1);
        const f32x2 cv = *(LAS f32x2*)(size_t)(L_CARRY + r * 8);
        hr = cv.x; hi = cv.y;
      } else {
        await(C_C3, 3);
        read_C();
        if (tb2 > 0) {
          u32x4 g[8];
          u32 gv[8];
#pragma unroll
          for (int j = 0; j < 8; ++j) gv[j] = r * 16 + ((8 * h + j < tb2) ? (8 * h + j) * 512 : 0);
#pragma unroll 1
          for (u32 spin = 0; spin < (1u << 17); ++spin) {
            asm volatile("" ::: "memory");
            u32 bad = 0;
#pragma unroll
            for (int j = 0; j < 8; ++j) g[j] = __builtin_amdgcn_raw_buffer_load_b128(rs_ws, gv[j], aggb, 16);
#pragma unroll
            for (int j = 0; j < 8; ++j) bad |= (g[j].y ^ tag) | (g[j].w ^ tag);
            if (__all(bad == 0)) break;
            __builtin_amdgcn_s_sleep(4);
          }
          asm volatile("" ::: "memory");
#pragma unroll
          for (int j = 0; j < 8; ++j)
            if (j < tb2) cstep(l128r, l128i, hr, hi, __uint_as_float(g[j].x), __uint_as_float(g[j].z));
          const float sr = swap32(hr, h), si = swap32(hi, h);
          if (h) { hr = sr; hi = si; }
          if (tb2 > 8) {
#pragma unroll
            for (int j = 0; j < 7; ++j)
              if (8 + j < tb2) cstep(l128r, l128i, hr, hi, __uint_as_float(g[j].x), __uint_as_float(g[j].z));
            const float tr = swap32(hr, h), ti = swap32(hi, h);
            if (!h) { hr = tr; hi = ti; }
          }
        }
        if (h == 0) *(LAS f32x2*)(size_t)(L_CARRY + r * 8) = (f32x2){hr, hi};
        arrive(C_CARRY, lane);
        TS(10);
      }
#pragma unroll
      for (int j = 0; j < 3; ++j)
        if (j < q) cstep(l32r, l32i, hr, hi, wagg[j].x, wagg[j].y);
      if (h) cstep(l16r, l16i, hr, hi, lor, loi);
      TS(5);
      u32 pr[8], pi[8];
#pragma unroll
      for (int i = 0; i < 16; i += 2) {
        cstep(lr, li, hr, hi, ur[i], ui[i]);
        const float r0 = hr, i0 = hi;
        cstep(lr, li, hr, hi, ur[i + 1], ui[i + 1]);
        pr[i >> 1] = cvtpk(r0, hr); pi[i >> 1] = cvtpk(-i0, -hi);
      }
      const u32 ta = L_C + q * 5120 + r * 80 + 32 * h;
      ldsw128(ta, (u32x4){pr[0], pr[1], pr[2], pr[3]}); ldsw128(ta + 16, (u32x4){pr[4], pr[5], pr[6], pr[7]});
      ldsw128(ta + 2560, (u32x4){pi[0], pi[1], pi[2], pi[3]}); ldsw128(ta + 2560 + 16, (u32x4){pi[4], pi[5], pi[6], pi[7]});
      LGKM0();
      const u32 tr = L_C + q * 5120 + (8 * h + q4) * 80 + g1 * 32 + 8 * p4;
      const int tbh = 2 * tb2 + (q >> 1);
      const u32 so = WS_HF + ((tbh * 2 + (q & 1)) * 56 + 2 * cg) * 1024 + vo;
#pragma unroll
      for (int pt = 0; pt < 2; ++pt)
#pragma unroll
        for (int s = 0; s < 2; ++s) {
          const u32 a = tr + pt * 2560 + s * 1280;
          __builtin_amdgcn_raw_buffer_store_b128(trfrag(a, a + 320), rs_ws, so + pt * 28 * 1024 + s * 1024, 0, 16);
        }
      arrive(C_HD, lane);
      VMCNT(0);
      if (lane == 0) gstore_flag(ws, WS_HFLAG + (tbh * 32 + cg * 2 + (q & 1)) * 128, tag);
      TS(6);
    }
    TS(9);
    if (lane < 56) {
      const char* src = (const char*)P.wo + sl * 112 + q * 14 * 7168;
#pragma unroll
      for (int j = 0; j < 15; ++j) {
        int i = j + 4 * tl; i = i >= 15 ? i - 15 : i;
        glds16(src + ((i == 14 && q == 3) ? 13 : i) * 7168, vo7, PA + L_WO_OFF + 896 * i); VMTHR();
      }
    }
    VMCNT(0);
    arrive(C_WO, lane);
    TS(11);
    if (q == 0) { poll_words(ws, WS_HFLAG + tb * 4096, 28, tag, lane); arrive(C_HF, lane); }
    else await(C_HF, 1);
    TS(7);
    {
      f32x16 y[4];
      const u32 so = WS_HF + ((tb * 2 + tm) * 56 + part * 28 + half * 14) * 1024, so1 = WS_HF + ((tb * 2 + (tm ^ 1)) * 56 + part * 28 + half * 14) * 1024;
      u32x4 hf[14];
#pragma unroll
      for (int j = 0; j < 14; ++j) hf[j] = __builtin_amdgcn_raw_buffer_load_b128(rs_ws, vo + (j & 3) * 1024, so + (j >> 2) * 4096, H_AUX);
      SBAR();
#pragma unroll
      for (int t = 0; t < 4; ++t) y[t] = (f32x16){};
      PIN4(y[0], y[1], y[2], y[3]);
      __builtin_amdgcn_s_setprio(AUX2_PRIO);
#pragma unroll
      for (int j = 0; j < 14; ++j) {
        y[0] = MFMA(frag(hf[j]), frag(cf[0][j]), y[0]);
        y[2] = MFMA(frag(hf[j]), frag(cf[1][j]), y[2]);
        SBAR();
        hf[j] = __builtin_amdgcn_raw_buffer_load_b128(rs_ws, vo + (j & 3) * 1024, so1 + (j >> 2) * 4096, H_AUX);
        SBAR();
      }
#pragma unroll
      for (int j = 0; j < 14; ++j) {
        y[1] = MFMA(frag(hf[j]), frag(cf[0][j]), y[1]);
        y[3] = MFMA(frag(hf[j]), frag(cf[1][j]), y[3]);
      }
      u16 xs[16];
      {
        const auto rs_ax = __builtin_amdgcn_make_buffer_rsrc((void*)P.Ax, 0, 2048 * 896, 0x00020000);
        const u32 xv = (t0 * 448 + dcol) * 2;
#pragma unroll
        for (int i = 0; i < 16; ++i) xs[i] = __builtin_amdgcn_raw_buffer_load_b16(rs_ax, xv + (i & 3) * 896, (i >> 2) * 7168, 0);
      }
      const u16 dsk16 = P.Dsk[dcol];
      MPIN4(y[0], y[1], y[2], y[3]);
      __builtin_amdgcn_s_setprio(AUX3_PRIO);
#pragma unroll
      for (int t = 1; t < 4; ++t) {
#pragma unroll
        for (int g = 0; g < 4; ++g) ldsw128(PA + (t - 1) * 4096 + g * 1024 + vo, f4(y[t], g));
      }
      arrive(C_Y, lane);
      await(C_Y, 4);
      f32x16 own = y[0];
#pragma unroll
      for (int t = 1; t < 4; ++t) {
#pragma unroll
        for (int g = 0; g < 4; ++g) add4(own, g, ldsr128((q ^ t) * L_PRIV + (t - 1) * 4096 + g * 1024 + vo));
      }
      if (valid) {
        const u32 ov = (t0 * 896 + dcol) * 4;
#pragma unroll
        for (int i = 0; i < 16; ++i) {
          float sk = bf2f(xs[i]) * bf2f(dsk16);
          PIN(sk);
          __builtin_amdgcn_raw_buffer_store_b32(__float_as_uint(own[i] + rbf(sk)), rs_out, ov, ((i & 3) + 8 * (i >> 2)) * 3584, OUT_AUX);
        }
      }
    }
    TS(8);
    TSW();
    if (tb == 31 && sl == 0 && q == 0) {
      await(C_KV, 1);
      if (lane == 0) gstore_flag(ws, WS_EPOCH, tag);
    }
  }
}

__global__ void __launch_bounds__(64) dummy_kernel(char* ws) {
  const u64 t0 = __hip_atomic_load((GAS u64*)(ws + 2048), __ATOMIC_RELAXED, __HIP_MEMORY_SCOPE_AGENT);
  const u64 lim = __builtin_amdgcn_s_memrealtime() + 6000;
  while (__builtin_amdgcn_s_memrealtime() - t0 < 4500 && __builtin_amdgcn_s_memrealtime() < lim) __builtin_amdgcn_s_sleep(2);
}
extern "C" void kernel_launch(void* const* d_in, const int* in_sizes, int n_in, void* d_out, int out_size, void* d_ws, size_t ws_size,
                              hipStream_t stream) {
  Params P;
  P.Ax = (const u16*)d_in[0]; P.lre = (const u16*)d_in[1]; P.lim = (const u16*)d_in[2]; P.Bre = (const u16*)d_in[3];
  P.Bim = (const u16*)d_in[4]; P.Cre = (const u16*)d_in[5]; P.Cim = (const u16*)d_in[6]; P.ldt = (const u16*)d_in[7];
  P.Dsk = (const u16*)d_in[8]; P.Bx = (const u16*)d_in[9]; P.wq = (const u16*)d_in[10]; P.wk = (const u16*)d_in[11];
  P.wv = (const u16*)d_in[12]; P.wo = (const u16*)d_in[13];
  P.out = (float*)d_out; P.ws = (char*)d_ws;
  (void)hipFuncSetAttribute((const void*)fused_kernel, hipFuncAttributeMaxDynamicSharedMemorySize, LDS_BYTES);
  fused_kernel<<<256, 512, LDS_BYTES, stream>>>(P);
  dummy_kernel<<<1, 64, 0, stream>>>((char*)d_ws);
}
```

```cpp
#include <hip/hip_runtime.h>

typedef unsigned short u16;
typedef unsigned int u32;
typedef unsigned long long u64;
using bf16x8 = __attribute__((ext_vector_type(8))) short;
using f32x16 = __attribute__((ext_vector_type(16))) float;
using u32x4 = __attribute__((ext_vector_type(4))) unsigned;
using u32x2 = __attribute__((ext_vector_type(2))) unsigned;
typedef short v4i16_t __attribute__((ext_vector_type(4)));
typedef __bf16 bf16x2_t __attribute__((ext_vector_type(2)));
typedef float f32x2 __attribute__((ext_vector_type(2)));
#define LAS __attribute__((address_space(3)))
#define GAS __attribute__((address_space(1)))
#define DI __device__ __forceinline__
#define MFMA(a, b, c) __builtin_amdgcn_mfma_f32_32x32x16_bf16((a), (b), (c), 0, 0, 0)
#define SBAR() __builtin_amdgcn_sched_barrier(0)
#define PIN(x) asm volatile("" : "+v"(x))
#define PIN2(a, b) asm volatile("" : "+v"(a), "+v"(b))
#define PIN4(a, b, c, d) asm volatile("" : "+v"(a), "+v"(b), "+v"(c), "+v"(d))
#define MPIN(x) asm volatile("s_nop 7\n\ts_nop 7" : "+v"(x))
#define MPIN2(a, b) asm volatile("s_nop 7\n\ts_nop 7" : "+v"(a), "+v"(b))
#define MPIN4(a, b, c, d) asm volatile("s_nop 7\n\ts_nop 7" : "+v"(a), "+v"(b), "+v"(c), "+v"(d))
#define VMCNT(n) asm volatile("s_waitcnt vmcnt(" #n ")" ::: "memory")
#define LGKM0() asm volatile("s_waitcnt lgkmcnt(0)" ::: "memory")
#define EX(x) __builtin_amdgcn_exp2f(x)

#define NOLOAD 0
#define THR 4
#define KV_AUX 16
#define H_AUX 16
#define FENCE 0
#define ABL 0
#define G1_PRIO 0
#define AUX3_PRIO 2
#define QEARLY 1
#define OUT_AUX 2
#define POLL_SLEEP 2
#define WARM 1
#define PREF 0
#define XEARLY 2
#define XD 4
#define CEARLY 3
#define ROLLED 0
#define HWIN 8
#define PF7 1
#define XG 2
#define KV_DEPTH 4
#define AUX_PRIO 2
#define AUX2_PRIO 0
#define GATE 0
#define KV_REPL 32

constexpr u32 WS_EPOCH = 0;
constexpr u32 WS_HFLAG = 7u << 20;
constexpr u32 WS_OFLAG = WS_HFLAG + 131072;
constexpr u32 WS_KVFLAG = WS_OFLAG + 131072;
constexpr u32 WS_AGG = 32768;
constexpr u32 WS_KVF = WS_AGG + 131072;
constexpr u32 WS_OF = WS_KVF + 262144;
constexpr u32 WS_HF = WS_OF + 2097152;
constexpr u32 WS_END = WS_KVFLAG + 32768;
constexpr u32 WS_DBG = 8u << 20;

constexpr u32 L_XB = 0;
constexpr u32 L_WQ = 57344;
constexpr u32 L_C = 107520;
constexpr u32 L_MISC = L_C + 55296;
constexpr u32 L_AGG = L_MISC + 128;
constexpr u32 L_KN = L_MISC + 896;
constexpr u32 L_CARRY = L_C + 20480;
constexpr u32 L_PRIV = 26880;
constexpr u32 L_WO_OFF = 12288;
constexpr u32 L_QX = L_WQ;
constexpr u32 LDS_BYTES = 163840;
enum { C_WQ = 0, C_Q2, C_QDONE, C_BFULL, C_AGG, C_WO, C_O, C_MAX, C_P, C_Y, C_FB, C_KVP, C_KMAX, C_KV, C_OF, C_HF, C_WQ2, C_HD, C_CREG, C_KVL, C_KVR, C_KVD, C_C3, C_CARRY, C_EPOCH = 31 };

struct Params {
  const u16 *Ax, *lre, *lim, *Bre, *Bim, *Cre, *Cim, *ldt, *Dsk, *Bx, *wq, *wk, *wv, *wo;
  float* out;
  char* ws;
};

DI float bf2f(u16 v) { return __uint_as_float(((u32)v) << 16); }
DI u32 cvtpk(float a, float b) { return __builtin_bit_cast(u32, __builtin_convertvector((f32x2){a, b}, bf16x2_t)); }
DI float rbf(float a) { return __uint_as_float(cvtpk(a, 0.f) << 16); }
DI bf16x8 frag(u32x4 v) { return __builtin_bit_cast(bf16x8, v); }
DI void glds16(const void* sbase, u32 voff, u32 lds_base) {
  asm volatile("s_mov_b32 m0, %2\n\ts_nop 0\n\tglobal_load_lds_dwordx4 %0, %1" :: "v"(voff), "s"(sbase), "s"(lds_base) : "memory");
}
DI void glds4(const void* sbase, u32 voff, u32 lds_base) {
  asm volatile("s_mov_b32 m0, %2\n\ts_nop 0\n\tglobal_load_lds_dword %0, %1" :: "v"(voff), "s"(sbase), "s"(lds_base) : "memory");
}
template <int N> DI void vmwait() { asm volatile("s_waitcnt vmcnt(%0)" :: "i"(N) : "memory"); }
DI void vmwait_rt(int n) {
  switch (n) {
    case 0: vmwait<0>(); break; case 4: vmwait<4>(); break; case 8: vmwait<8>(); break; case 12: vmwait<12>(); break;
    case 16: vmwait<16>(); break; case 20: vmwait<20>(); break; default: vmwait<0>(); break;
  }
}
DI u32x4 ldsr128(u32 a) { return *(const LAS u32x4*)(size_t)a; }
DI void ldsw128(u32 a, u32x4 v) { *(LAS u32x4*)(size_t)a = v; }
DI u32 ldsu(u32 a) { return (u32)__builtin_amdgcn_readfirstlane(*(volatile LAS u32*)(size_t)a); }
DI u32x2 ldstr(u32 a) { return __builtin_bit_cast(u32x2, __builtin_amdgcn_ds_read_tr16_b64_v4i16((LAS v4i16_t*)(size_t)a)); }
DI u32x4 trfrag(u32 a0, u32 a1) { u32x2 lo = ldstr(a0), hi = ldstr(a1); return (u32x4){lo.x, lo.y, hi.x, hi.y}; }
DI u32x4 f4(const f32x16& v, int g) {
  return (u32x4){__float_as_uint(v[4 * g]), __float_as_uint(v[4 * g + 1]), __float_as_uint(v[4 * g + 2]), __float_as_uint(v[4 * g + 3])};
}
DI void add4(f32x16& v, int g, u32x4 a) {
  v[4 * g] += __uint_as_float(a.x); v[4 * g + 1] += __uint_as_float(a.y); v[4 * g + 2] += __uint_as_float(a.z); v[4 * g + 3] += __uint_as_float(a.w);
}
DI void arrive(int c, int lane) {
  asm volatile("s_waitcnt lgkmcnt(0)" ::: "memory");
  if (lane == 0) asm volatile("ds_add_u32 %0, %1" :: "v"(L_MISC + 4 * c), "v"(1u) : "memory");
}
DI void await(int c, u32 target) {
#pragma unroll 1
  for (u32 spin = 0; spin < (1u << 22); ++spin) {
    asm volatile("" ::: "memory");
    if (ldsu(L_MISC + 4 * c) >= target) break;
    __builtin_amdgcn_s_sleep(1);
  }
  asm volatile("" ::: "memory");
}
DI float swap32(float v, bool hi) {
  auto r = __builtin_amdgcn_permlane32_swap(__float_as_uint(v), __float_as_uint(v), false, false);
  return __uint_as_float(hi ? r[0] : r[1]);
}
DI float wave_max_nonneg(float v) {
  u32 x = __float_as_uint(v);
  x = max(x, (u32)__builtin_amdgcn_update_dpp(0, (int)x, 0x111, 0xf, 0xf, false));
  x = max(x, (u32)__builtin_amdgcn_update_dpp(0, (int)x, 0x112, 0xf, 0xf, false));
  x = max(x, (u32)__builtin_amdgcn_update_dpp(0, (int)x, 0x114, 0xf, 0xf, false));
  x = max(x, (u32)__builtin_amdgcn_update_dpp(0, (int)x, 0x118, 0xf, 0xf, false));
  x = max(x, (u32)__builtin_amdgcn_update_dpp(0, (int)x, 0x142, 0xa, 0xf, false));
  x = max(x, (u32)__builtin_amdgcn_update_dpp(0, (int)x, 0x143, 0xc, 0xf, false));
  return __uint_as_float((u32)__builtin_amdgcn_readlane((int)x, 63));
}
DI void gstore_flag(char* ws, u32 off, u32 v) { __hip_atomic_store((GAS u32*)(ws + off), v, __ATOMIC_RELAXED, __HIP_MEMORY_SCOPE_AGENT); }
DI void poll_words(char* ws, u32 off, int n, u32 tag, int lane) {
  GAS u32* p = (GAS u32*)(ws + off) + (lane < n ? lane : 0) * 32;
  u32 a = __hip_atomic_load(p, __ATOMIC_RELAXED, __HIP_MEMORY_SCOPE_AGENT);
#pragma unroll 1
  for (u32 spin = 0; spin < (1u << 16); ++spin) {
    __builtin_amdgcn_s_sleep(POLL_SLEEP);
    u32 c = __hip_atomic_load(p, __ATOMIC_RELAXED, __HIP_MEMORY_SCOPE_AGENT);
    SBAR();
    if (__all(a == tag)) break;
    __builtin_amdgcn_s_sleep(POLL_SLEEP);
    a = __hip_atomic_load(p, __ATOMIC_RELAXED, __HIP_MEMORY_SCOPE_AGENT);
    SBAR();
    if (__all(c == tag)) break;
  }
  asm volatile("" ::: "memory");
}
DI float poll_kv(char* ws, int rep, u32 tag, int lane) {
  GAS u64* p = (GAS u64*)(ws + WS_KVFLAG + rep * 4096) + (lane & 31) * 16;
  u64 a = __hip_atomic_load(p, __ATOMIC_RELAXED, __HIP_MEMORY_SCOPE_AGENT), g = 0;
#pragma unroll 1
  for (u32 spin = 0; spin < (1u << 16); ++spin) {
    __builtin_amdgcn_s_sleep(POLL_SLEEP);
    u64 c = __hip_atomic_load(p, __ATOMIC_RELAXED, __HIP_MEMORY_SCOPE_AGENT);
    SBAR();
    g = a;
    if (__all((u32)a == tag)) break;
    __builtin_amdgcn_s_sleep(POLL_SLEEP);
    a = __hip_atomic_load(p, __ATOMIC_RELAXED, __HIP_MEMORY_SCOPE_AGENT);
    SBAR();
    g = c;
    if (__all((u32)c == tag)) break;
  }
  asm volatile("" ::: "memory");
  return __uint_as_float((u32)(g >> 32));
}
DI void cmul(float ar, float ai, float br, float bi, float& cr, float& ci) {
  float x, y;
  asm("v_mul_f32 %0, %2, %4\n\tv_mul_f32 %1, %2, %5\n\tv_fma_f32 %0, -%3, %5, %0\n\tv_fma_f32 %1, %3, %4, %1"
      : "=&v"(x), "=&v"(y) : "v"(ar), "v"(ai), "v"(br), "v"(bi));
  cr = x; ci = y;
}
DI void cstep(float ar, float ai, float& hr, float& hi, float ur, float ui) {
  float x, y;
  asm("v_fma_f32 %0, -%3, %5, %6\n\tv_fma_f32 %1, %3, %4, %7\n\tv_fma_f32 %0, %2, %4, %0\n\tv_fma_f32 %1, %2, %5, %1"
      : "=&v"(x), "=&v"(y) : "v"(ar), "v"(ai), "v"(hr), "v"(hi), "v"(ur), "v"(ui));
  hr = x; hi = y;
}
DI void set_pad(u32x4& f, float m, int h) {
  const float a = rbf(-m), b = rbf(-m - a), c = rbf(-m - a - b);
  if (h) { f.z = cvtpk(a, b); f.w = cvtpk(c, 0.f); }
}

#define TS(i) do {} while (0)
#define TSD() do {} while (0)
#define TSW() do {} while (0)
#define LPRIO 0
#define PUP() do {} while (0)
#define PDN() SBAR()
#define STR_(x) #x
#define VMTHR_(n) asm volatile("s_waitcnt vmcnt(" STR_(n) ")" ::: "memory")
#define VMTHR() VMTHR_(THR)

template <bool W_IS_A, int WROW, bool MID, class F>
DI f32x16 proj_tile(int row, int h, u32 wa, int lane, F mid) {
  const int sw = (row >> 1) & 7;
  u32 xa[4];
#pragma unroll
  for (int bb = 0; bb < 4; ++bb) xa[bb] = L_XB + row * 128 + (((2 * bb + h) ^ sw) << 4);
  u32x4 xf[28], wf[28];
  f32x16 acc = {};
#pragma unroll
  for (int ks = 0; ks < 28 + 5; ++ks) {
    if (ks >= 5) {
      const int k = ks - 5;
      acc = W_IS_A ? MFMA(frag(wf[k]), frag(xf[k]), acc) : MFMA(frag(xf[k]), frag(wf[k]), acc);
      SBAR();
    }
    if (ks < 28) {
      if (MID && ks == 14) {
        asm volatile("s_nop 7\n\ts_nop 7" ::: "memory");
        mid();
        asm volatile("s_waitcnt vmcnt(0) lgkmcnt(0)" ::: "memory");
        if (lane == 0) asm volatile("ds_add_u32 %0, %1" :: "v"(L_MISC + 4 * C_WQ2), "v"(1u) : "memory");
        await(C_WQ2, 4);
      }
      xf[ks] = ldsr128(xa[ks & 3] + 8192 * (ks >> 2));
      wf[ks] = trfrag(wa + ks * 16 * WROW, wa + ks * 16 * WROW + 4 * WROW);
      SBAR();
    }
  }
  MPIN(acc);
  return acc;
}

__global__ void __launch_bounds__(512, 2) fused_kernel(Params P) {
  extern __shared__ __attribute__((aligned(16))) char lds_dyn[];
  asm volatile("" : "+s"(P.Ax), "+s"(P.lre), "+s"(P.lim), "+s"(P.Bre), "+s"(P.Bim), "+s"(P.Cre), "+s"(P.Cim), "+s"(P.ldt));
  asm volatile("" : "+s"(P.Dsk), "+s"(P.Bx), "+s"(P.wq), "+s"(P.wk), "+s"(P.wv), "+s"(P.wo), "+s"(P.out), "+s"(P.ws));
  const int tid = threadIdx.x, lane = tid & 63, wv = __builtin_amdgcn_readfirstlane(tid >> 6);
  const int r = lane & 31, h = lane >> 5;
  const int b = blockIdx.x;
  const int tb = (b & 7) * 4 + (b >> 6), sl = (b >> 3) & 7;
  char* ws = P.ws;
  const int tl = b >> 6;
  TSD();
  TS(0);
  if (tid == 0) asm volatile("s_mov_b32 m0, %2\n\ts_nop 0\n\tglobal_load_lds_dword %0, %1 sc1" :: "v"(0), "s"(ws), "s"(L_MISC + 4 * C_EPOCH) : "memory");
  const u32 vo = lane * 16;
  {
    const int row = 8 * wv + (lane >> 3);
    const u32 xv = row * 896 + (((lane & 7) ^ ((row >> 1) & 7)) << 4);
    const char* src = (const char*)P.Bx + (size_t)tb * 64 * 896;
#pragma unroll
    for (int j = 0; j < 7; ++j) {
      int c = j + sl; c = c >= 7 ? c - 7 : c;
      glds16(src + c * 128, xv, L_XB + c * 8192 + wv * 1024);
    }
  }
  if (tid < 31) *(LAS u32*)(size_t)(L_MISC + 4 * tid) = 0;
  const auto rs_ws = __builtin_amdgcn_make_buffer_rsrc((void*)ws, 0, WS_END, 0x00020000);
  const auto rs_out = __builtin_amdgcn_make_buffer_rsrc((void*)P.out, 0, 2048 * 896 * 4, 0x00020000);
  const int g1 = (lane >> 4) & 1, q4 = (lane >> 2) & 3, p4 = lane & 3;
  const int l7 = lane / 7;
  const u32 vo7 = l7 * 896 + (lane - 7 * l7) * 16;

  if (wv < 4) {
    const int w = wv;
    const char* wqsrc = (const char*)P.wq + sl * 112 + w * 7 * 7168;
    auto stage_wq = [&](int hf) {
      if (lane < 56) {
#pragma unroll
        for (int j = 0; j < 7; ++j) {
          int i = j + 2 * tl; i = (i >= 7 ? i - 7 : i) + 28 * hf;
          glds16(wqsrc + i * 7168, vo7, L_WQ + (w * 7 + i) * 896);
        }
      }
    };
    if (sl != 7) {
      stage_wq(0); stage_wq(1);
      asm volatile("s_waitcnt vmcnt(14) lgkmcnt(0)\n\ts_barrier" ::: "memory");
    } else {
      VMCNT(0);
      stage_wq(0); stage_wq(1);
      asm volatile("s_waitcnt lgkmcnt(0)\n\ts_barrier" ::: "memory");
    }
    const u32 tag = ldsu(L_MISC + 4 * C_EPOCH) + 1u;
    VMCNT(7);
    arrive(C_WQ, lane);
    await(C_WQ, 4);
    {
      f32x16 acc = proj_tile<true, 112, true>(32 * (w & 1) + r, h, L_WQ + (8 * h + q4) * 112 + (w >> 1) * 56 + g1 * 32 + 8 * p4, lane,
                                            []() {});
      TS(8);
      const float qs = 0.27264060f;
      float n2 = 0.f;
#pragma unroll
      for (int i = 0; i < 16; ++i) { acc[i] *= qs; if (i < 12) n2 = fmaf(acc[i], acc[i], n2); else n2 = h ? n2 : fmaf(acc[i], acc[i], n2); }
      n2 += swap32(n2, h);
      u32x4 f0 = {cvtpk(acc[0], acc[1]), cvtpk(acc[2], acc[3]), cvtpk(acc[4], acc[5]), cvtpk(acc[6], acc[7])};
      u32x4 f1 = {cvtpk(acc[8], acc[9]), cvtpk(acc[10], acc[11]), cvtpk(acc[12], acc[13]), cvtpk(acc[14], acc[15])};
      if (h) { f1.z = 0; f1.w = __float_as_uint(__builtin_amdgcn_sqrtf(n2)); }
      ldsw128(L_QX + w * 2048 + vo, f0);
      ldsw128(L_QX + w * 2048 + 1024 + vo, f1);
      arrive(C_Q2, lane);
    }
    u32x4 qf[4][2];
    if (QEARLY) {
      await(C_Q2, 4);
#pragma unroll
      for (int t = 0; t < 4; ++t) { const u32 a = L_QX + ((w + t) & 3) * 2048 + vo; qf[t][0] = ldsr128(a); qf[t][1] = ldsr128(a + 1024); }
      arrive(C_QDONE, lane);
    }
    TS(2);
    if (w == 0) {
      const float kn = wave_max_nonneg(poll_kv(ws, b & 7, tag, lane));
      if (lane == 0) *(LAS float*)(size_t)L_KN = kn;
      arrive(C_KV, lane);
    } else {
      await(C_KV, 1);
    }
    TS(3);
    const u32 kvbase = WS_KVF + w * 65536;
    const int rot = b >> 3;
#define KVOFF(it) (kvbase + ((((it) + rot) & 15) << 12))
#define LDK(it, s) __builtin_amdgcn_raw_buffer_load_b128(rs_ws, vo + 1024 * (s), KVOFF(it), KV_AUX)
#define LDV(it, s) __builtin_amdgcn_raw_buffer_load_b128(rs_ws, vo + 2048 + 1024 * (s), KVOFF(it), KV_AUX)
#define NEXT(x) asm volatile("s_add_u32 %0, %0, 0x1000\n\ts_and_b32 %0, %0, 0xf000" : "+s"(x) :: "scc")
#define LDK2(s_) __builtin_amdgcn_raw_buffer_load_b128(rs_kv, vo + 1024 * (s_), koff, KV_AUX)
#define LDV2(s_) __builtin_amdgcn_raw_buffer_load_b128(rs_kv, vo + 2048 + 1024 * (s_), voff, KV_AUX)
    const auto rs_kv = __builtin_amdgcn_make_buffer_rsrc((void*)(ws + kvbase), 0, 65536, 0x00020000);
    f32x16 o[4];
    for (int attempt = 0; attempt < 2; ++attempt) {
      u32x4 kf[KV_DEPTH][2], vf[KV_DEPTH][2];
#pragma unroll
      for (int d = 0; d < KV_DEPTH; ++d) {
        kf[d][0] = LDK(d, 0); kf[d][1] = LDK(d, 1);
        if (d < KV_DEPTH - 1) { vf[d][0] = LDV(d, 0); vf[d][1] = LDV(d, 1); }
      }
      u32 koff = ((KV_DEPTH - 1 + rot) & 15) << 12, voff = ((KV_DEPTH - 2 + rot) & 15) << 12;
      SBAR();
      if (attempt == 0) {
        arrive(C_KVL, lane);
        if (!QEARLY) {
        await(C_Q2, 4);
#pragma unroll
        for (int t = 0; t < 4; ++t) { const u32 a = L_QX + ((w + t) & 3) * 2048 + vo; qf[t][0] = ldsr128(a); qf[t][1] = ldsr128(a + 1024); }
        arrive(C_QDONE, lane);
        }
        const float kmax = __builtin_amdgcn_sqrtf(*(LAS float*)(size_t)L_KN);
#pragma unroll
        for (int t = 0; t < 4; ++t) set_pad(qf[t][1], __uint_as_float(qf[t][1].w) * kmax, h);
      }
#pragma unroll
      for (int t = 0; t < 4; ++t) { PIN(qf[t][0]); PIN(qf[t][1]); }
      SBAR();
      const f32x16 zero = {};
      f32x16 sc = MFMA(frag(kf[0][0]), frag(qf[0][0]), zero);
      sc = MFMA(frag(kf[0][1]), frag(qf[0][1]), sc);
      TS(9);
      float e0 = 0.f, e1 = 0.f, e2 = 0.f, e3 = 0.f;
      u32x4 p1 = {0, 0, 0, 0};
#pragma unroll
      for (int it = 0; it < 16; ++it) {
        const int d = it % KV_DEPTH, dprev = (it + KV_DEPTH - 1) % KV_DEPTH;
#pragma unroll
        for (int qi = 0; qi < 4; ++qi) {
          const int dn = qi == 3 ? (it + 1) % KV_DEPTH : d, qn = (qi + 1) & 3;
          const int dp = qi == 0 ? dprev : d, qp = (qi + 3) & 3;
          const bool last = it == 15 && qi == 3;
          f32x16 sn; u32x4 p0;
          float c0, c1, c2, c3;
          if (!last) sn = MFMA(frag(kf[dn][0]), frag(qf[qn][0]), zero);
          SBAR();
          if (qi == 0) NEXT(voff);
          if (qi == 2) NEXT(koff);
          p1.z = cvtpk(e0, e1); p1.w = cvtpk(e2, e3); PIN(p1); SBAR();
          c0 = EX(sc[0]); c1 = EX(sc[1]); c2 = EX(sc[2]); c3 = EX(sc[3]); PIN4(c0, c1, c2, c3); SBAR();
          if (it > 0 || qi > 0) o[qp] = MFMA(frag(vf[dp][1]), frag(p1), o[qp]);
          SBAR();
          if (qi < 2 && it - 1 + KV_DEPTH < 16) { vf[dprev][qi] = LDV2(qi); SBAR(); }
          p0.x = cvtpk(c0, c1); p0.y = cvtpk(c2, c3); PIN2(p0.x, p0.y); SBAR();
          c0 = EX(sc[4]); c1 = EX(sc[5]);
          c2 = EX(sc[6]); c3 = EX(sc[7]); PIN4(c0, c1, c2, c3); SBAR();
          if (!last) sn = MFMA(frag(kf[dn][1]), frag(qf[qn][1]), sn);
          SBAR();
          if (qi == 2 && it + KV_DEPTH < 16) { kf[d][0] = LDK2(0); SBAR(); }
          p0.z = cvtpk(c0, c1); p0.w = cvtpk(c2, c3); PIN(p0); SBAR();
          c0 = EX(sc[8]); c1 = EX(sc[9]);
          c2 = EX(sc[10]); c3 = EX(sc[11]); PIN4(c0, c1, c2, c3); SBAR();
          o[qi] = MFMA(frag(vf[d][0]), frag(p0), it == 0 ? zero : o[qi]); SBAR();
          if (qi == 2 && it + KV_DEPTH < 16) { kf[d][1] = LDK2(1); SBAR(); }
          p1.x = cvtpk(c0, c1); p1.y = cvtpk(c2, c3); PIN2(p1.x, p1.y); SBAR();
          e0 = EX(sc[12]); e1 = EX(sc[13]);
          e2 = EX(sc[14]); e3 = EX(sc[15]); PIN4(e0, e1, e2, e3); SBAR();
          if (!last) sc = sn;
        }
      }
      p1.z = cvtpk(e0, e1); p1.w = cvtpk(e2, e3);
      o[3] = MFMA(frag(vf[15 % KV_DEPTH][1]), frag(p1), o[3]);
      MPIN4(o[0], o[1], o[2], o[3]);
      TS(4);
      u32 lv;
      asm volatile("v_mbcnt_lo_u32_b32 %0, -1, 0\n\tv_mbcnt_hi_u32_b32 %0, -1, %0\n\tv_lshlrev_b32 %0, 4, %0" : "=v"(lv));
      if (attempt == 0) {
        bool bad = false;
#pragma unroll
        for (int t = 0; t < 4; ++t) bad |= !(o[t][12] >= 0x1p-80f && o[t][12] <= 0x1p100f);
        if (__any(bad && lv >= 512)) { if (lv == 0) *(LAS u32*)(size_t)(L_MISC + 4 * C_FB) = 1u; }
        await(C_HD, 4);
      }
#pragma unroll
      for (int t = 1; t < 4; ++t) {
#pragma unroll
        for (int g = 0; g < 4; ++g) ldsw128(L_C + w * 12288 + (t - 1) * 4096 + g * 1024 + lv, f4(o[t], g));
      }
      arrive(C_O, lv);
      await(C_O, 4 * (attempt + 1));
      TS(10);
      const u32 fb = attempt == 0 ? ldsu(L_MISC + 4 * C_FB) : 0u;
      if (!fb) break;
      {
        float mx[4];
#pragma unroll
        for (int t = 0; t < 4; ++t) { mx[t] = -3.0e38f; if (lv >= 512) { qf[t][1].z = 0; qf[t][1].w = 0; } }
#pragma unroll 1
        for (int it = 0; it < 16; ++it) {
          const u32x4 k0 = __builtin_amdgcn_raw_buffer_load_b128(rs_kv, lv, it << 12, KV_AUX), k1 = __builtin_amdgcn_raw_buffer_load_b128(rs_kv, lv + 1024, it << 12, KV_AUX);
#pragma unroll
          for (int t = 0; t < 4; ++t) {
            f32x16 s = MFMA(frag(k0), frag(qf[t][0]), zero);
            s = MFMA(frag(k1), frag(qf[t][1]), s);
#pragma unroll
            for (int i = 0; i < 16; ++i) mx[t] = fmaxf(mx[t], s[i]);
          }
        }
        const u32 ma = L_C + 49152;
#pragma unroll
        for (int t = 0; t < 4; ++t) { mx[t] = fmaxf(mx[t], swap32(mx[t], lv >= 512)); *(LAS float*)(size_t)(ma + (w * 4 + ((w + t) & 3)) * 256 + (lv >> 2)) = mx[t]; }
        arrive(C_MAX, lv);
        await(C_MAX, 4);
#pragma unroll
        for (int t = 0; t < 4; ++t) {
          float m = -3.0e38f;
#pragma unroll
          for (int ww = 0; ww < 4; ++ww) m = fmaxf(m, *(LAS float*)(size_t)(ma + (ww * 4 + ((w + t) & 3)) * 256 + (lv >> 2)));
          set_pad(qf[t][1], m, lv >= 512);
        }
      }
    }
    {
    int lane;
    asm volatile("v_mbcnt_lo_u32_b32 %0, -1, 0\n\tv_mbcnt_hi_u32_b32 %0, -1, %0" : "=v"(lane));
    const int r = lane & 31, h = lane >> 5, g1 = (lane >> 4) & 1, q4 = (lane >> 2) & 3, p4 = lane & 3;
    const u32 vo = lane * 16;
    {
      f32x16 own = o[0];
#pragma unroll
      for (int pp = 1; pp < 4; ++pp) {
        const u32 a = L_C + ((w + pp) & 3) * 12288 + (3 - pp) * 4096 + vo;
#pragma unroll
        for (int g = 0; g < 4; ++g) add4(own, g, ldsr128(a + g * 1024));
      }
      const float other = swap32(own[12], h);
      const float inv = __builtin_amdgcn_rcpf(h ? own[12] : other);
      u32x4 f0 = {cvtpk(own[0] * inv, own[1] * inv), cvtpk(own[2] * inv, own[3] * inv), cvtpk(own[4] * inv, own[5] * inv), cvtpk(own[6] * inv, own[7] * inv)};
      u32x4 f1 = {cvtpk(own[8] * inv, own[9] * inv), cvtpk(own[10] * inv, own[11] * inv), cvtpk(own[12] * inv, own[13] * inv), cvtpk(own[14] * inv, own[15] * inv)};
      if (h) { f1.z = 0; f1.w = 0; }
      const u32 so = WS_OF + ((tb * 2 + (w & 1)) * 16 + 2 * sl + (w >> 1)) * 2048 + vo;
      __builtin_amdgcn_raw_buffer_store_b128(f0, rs_ws, so, 0, 16);
      __builtin_amdgcn_raw_buffer_store_b128(f1, rs_ws, so + 1024, 0, 16);
    }
    {
      const int tm = w & 1, kh = w >> 1;
      TS(11);
      VMCNT(0);
      if (lane == 0) gstore_flag(ws, WS_OFLAG + (tb * 32 + sl * 4 + w) * 128, tag);
      TS(5);
      if (sl == 7) { TSW(); return; }
      await(C_WO, 4);
      TS(15);
      u32x4 wf[16][2];
      const u32 wa = 2 * kh * L_PRIV + L_WO_OFF + (4 * h + q4) * 128 + g1 * 32 + 8 * p4;
      const u32 wa0 = wa + 64 * kh, wa1 = wa + 64 * (1 - kh);
      const u32 hz = h ? 0 : 1024;
#pragma unroll
      for (int i = 0; i < 16; ++i) {
        const u32 a = (i >> 3) * L_PRIV + (28 * ((i >> 1) & 3) + 16 * (i & 1)) * 128;
        if ((i & 7) == 7) { wf[i][0] = trfrag(wa0 + a, wa0 + hz + a); wf[i][1] = trfrag(wa1 + a, wa1 + hz + a); }
        else { wf[i][0] = trfrag(wa0 + a, wa0 + a + 1024); wf[i][1] = trfrag(wa1 + a, wa1 + a + 1024); }
      }
#pragma unroll
      for (int i = 0; i < 16; ++i) PIN2(wf[i][0], wf[i][1]);
      if (w == 0) { poll_words(ws, WS_OFLAG + tb * 4096, 32, tag, lane); arrive(C_OF, lane); }
      else await(C_OF, 1);
      TS(6);
      u32x4 of[16];
      const u32 so = WS_OF + ((tb * 2 + tm) * 16 + 8 * kh) * 2048;
#pragma unroll
      for (int i = 0; i < 16; ++i) of[i] = __builtin_amdgcn_raw_buffer_load_b128(rs_ws, vo + (i & 3) * 1024, so + (i >> 2) * 4096, H_AUX);
      SBAR();
      f32x16 a0 = {}, a1 = {};
#pragma unroll
      for (int i = 0; i < 16; ++i) {
        a0 = MFMA(frag(of[i]), frag(wf[i][0]), a0);
        a1 = MFMA(frag(of[i]), frag(wf[i][1]), a1);
      }
      MPIN2(a0, a1);
      TS(12);
      const f32x16 snd = a1;
      f32x16 own = a0;
      const u32 aw = L_C + w * 4096 + vo, ar = L_C + (w ^ 2) * 4096 + vo;
#pragma unroll
      for (int g = 0; g < 4; ++g) ldsw128(aw + g * 1024, f4(snd, g));
      arrive(C_P, lane);
      await(C_P, 4);
#pragma unroll
      for (int g = 0; g < 4; ++g) add4(own, g, ldsr128(ar + g * 1024));
      {
        const u32 ov = ((tb * 64 + 32 * tm + 4 * h) * 896 + 448 + 64 * sl + 32 * kh + r) * 4;
#pragma unroll
        for (int i = 0; i < 16; ++i) __builtin_amdgcn_raw_buffer_store_b32(__float_as_uint(rbf(own[i])), rs_out, ov, ((i & 3) + 8 * (i >> 2)) * 3584, OUT_AUX);
      }
    }
    }
    TS(7);
    TSW();
  } else {
    __builtin_amdgcn_s_setprio(AUX_PRIO);
    const int q = wv - 4;
    const u32 PA = q * L_PRIV;
    const int part = q >> 1, half = q & 1;
    if (sl == 7 && lane < 56) {
      const char* src = (const char*)((q >> 1) ? P.wv : P.wk) + (q & 1) * 14 * 896;
#pragma unroll
      for (int j = 0; j < 14; ++j) {
        int i = j + 4 * tl; i = i >= 14 ? i - 14 : i;
        glds16(src + i * 896, vo, L_C + (q * 14 + i) * 896);
      }
    }
    asm volatile("s_waitcnt vmcnt(0) lgkmcnt(0)\n\ts_barrier" ::: "memory");
    const u32 tag = ldsu(L_MISC + 4 * C_EPOCH) + 1u;
    TS(13);
    if (sl == 7) {
      const int kind = q >> 1, ktm = q & 1;
      const u32 wa = L_C + kind * 25088 + (8 * h + q4) * 56 + g1 * 32 + 8 * p4;
      f32x16 acc;
      if (kind == 0) acc = proj_tile<true, 56, false>(32 * ktm + r, h, wa, lane, []() {});
      else acc = proj_tile<false, 56, false>(32 * ktm + r, h, wa, lane, []() {});
      u32x4 f0 = {cvtpk(acc[0], acc[1]), cvtpk(acc[2], acc[3]), cvtpk(acc[4], acc[5]), cvtpk(acc[6], acc[7])};
      u32x4 f1 = {cvtpk(acc[8], acc[9]), cvtpk(acc[10], acc[11]), cvtpk(acc[12], acc[13]), cvtpk(acc[14], acc[15])};
      if (kind == 0) {
        if (h) { f1.z = 0x3F803F80u; f1.w = 0x00003F80u; }
      } else if (r >= 28) {
        const u32 c = r == 28 ? 0x3F803F80u : 0u;
        f0 = (u32x4){c, c, c, c}; f1 = f0;
      }
      const u32 so = WS_KVF + (tb * 2 + ktm) * 4096 + kind * 2048 + vo;
      __builtin_amdgcn_raw_buffer_store_b128(f0, rs_ws, so, 0, 16);
      __builtin_amdgcn_raw_buffer_store_b128(f1, rs_ws, so + 1024, 0, 16);
      SBAR();
      TS(12);
      if (kind == 0) {
        float n2 = 0.f;
#pragma unroll
        for (int i = 0; i < 16; ++i) { if (i < 12) n2 = fmaf(acc[i], acc[i], n2); else n2 = h ? n2 : fmaf(acc[i], acc[i], n2); }
        n2 += swap32(n2, h);
        n2 = wave_max_nonneg(n2);
        if (lane == 0) asm volatile("ds_max_u32 %0, %1" :: "v"(L_MISC + 4 * C_KMAX), "v"(n2) : "memory");
      }
      VMCNT(0);
      u32 old = 0;
      if (lane == 0) asm volatile("s_waitcnt lgkmcnt(0)\n\tds_add_rtn_u32 %0, %1, %2\n\ts_waitcnt lgkmcnt(0)" : "=&v"(old) : "v"(L_MISC + 4 * C_KVP), "v"(1u) : "memory");
      if (__builtin_amdgcn_readfirstlane(old) == 3) {
        const u32 km = ldsu(L_MISC + 4 * C_KMAX);
        if (lane < 8) __hip_atomic_store((GAS u64*)(ws + WS_KVFLAG + lane * 4096 + tb * 128), (u64)tag | ((u64)km << 32), __ATOMIC_RELAXED, __HIP_MEMORY_SCOPE_AGENT);
      }
      TS(14);
      arrive(C_KVD, lane);
      await(C_KVD, 4);
    } else if (PREF) {
      const int j = (b >> 3) - tl;
      const char* p0 = (const char*)(q == 0 ? P.Bre : q == 1 ? P.Bim : q == 2 ? P.Cre : P.Cim) + j * 14336;
      const u32 o2 = lane < 48 ? 8192 + lane * 128 : 0;
      glds4(p0, lane * 128, L_AGG + 512);
      glds4(p0, o2, L_AGG + 512);
      if (q == 0) glds4((const char*)P.Ax + (size_t)(b & 7) * 229376 + j * 8192, lane * 128, L_AGG + 512);
      if (q == 1) glds4((const char*)P.wo + j * 14336, lane * 128, L_AGG + 512);
      if (q == 2) glds4((const char*)P.wo + j * 14336, o2, L_AGG + 512);
    }

    u32 cva;
    {
      const int hi = lane >= 28, cs = lane - 28 * hi;
      cva = hi * 896 + (cs << 4);
    }
    auto stage_C4 = [&](int qq, int g) {
      const char* csrc = (const char*)((qq >> 1) ? P.Cim : P.Cre) + (size_t)sl * 56 * 896 + (qq & 1) * 448;
      int p0 = 4 * g + 8 * tl; p0 = p0 >= 28 ? p0 - 28 : p0;
      const u32 va = cva ^ ((p0 & 4) << 3), vb = va ^ 16;
      if (lane < 56) {
#pragma unroll
        for (int j = 0; j < 4; ++j) { glds16(csrc + (p0 + j) * 1792, j < 2 ? va : vb, qq * L_PRIV + (p0 + j) * 896); VMTHR(); }
      }
    };
    const int tm = (q ^ sl) & 1, tn = q >> 1;
    const bool valid = 32 * tn + r < 56;
    const int dcol = 56 * sl + (valid ? 32 * tn + r : 55), t0 = tb * 64 + 32 * tm + 4 * h;
    if (WARM && q == 3) {
      const int k = lane & 7;
      const u32 o1 = k < 4 ? WS_KVF + k * 65536 : k < 6 ? WS_OF + tb * 65536 + (k - 4) * 65024 : k == 6 ? WS_HFLAG : WS_AGG + (((tb & 1) * 7 + sl) & 15) * 8192;
      const u32 o2 = WS_HF + (tb & ~1) * 114688 + (k & 3) * 65536;
      if (lane < 16) {
        asm volatile("s_mov_b32 m0, %2\n\ts_nop 0\n\tglobal_load_lds_dword %0, %1 sc1" :: "v"(lane < 8 ? o1 : o2), "s"(ws), "s"(L_AGG + 512) : "memory");
        glds4((const char*)P.out + (size_t)tb * 229376, (lane & 3) * 65536, L_AGG + 512);
      }
    }
    u32x4 cf[2][14];
    auto read_C = [&]() {
#pragma unroll
      for (int t = 0; t < 2; ++t) {
        int row = 32 * (t ^ part) + r; row = row > 55 ? 55 : row;
        const int x = (row >> 2) & 3;
        const u32 a0 = PA + row * 448 + ((h ^ x) << 4), a1 = PA + row * 448 + (((2 + h) ^ x) << 4);
#pragma unroll
        for (int j = 0; j < 14; ++j) cf[t][j] = ldsr128(((j & 1) ? a1 : a0) + 64 * (j >> 1));
      }
#pragma unroll
      for (int j = 0; j < 14; ++j) PIN2(cf[0][j], cf[1][j]);
      LGKM0();
    };
    if (sl == 7) {
      arrive(C_HD, lane);
      await(C_KVL, 4);
      if (PF7) {
        const char* cp = (const char*)(part ? P.Cim : P.Cre) + (tl * 2 + half) * 43904;
#pragma unroll 1
        for (int i = 0; i < 43; ++i) { glds16(cp + (i < 42 ? i * 1024 : 42880), vo, PA + (i & 7) * 1024); VMTHR(); }
        const char* wp = (const char*)P.wo + (tl * 4 + q) * 25088;
#pragma unroll 1
        for (int i = 0; i < 25; ++i) { glds16(wp + (i < 24 ? i * 1024 : 24064), vo, PA + (i & 7) * 1024); VMTHR(); }
        VMCNT(0);
      }
      TS(9);
#pragma unroll 1
      for (int g = 0; g < 7; ++g) stage_C4(q, g);
      VMCNT(0);
      read_C();
    } else {
      const int tb2 = tb >> 1, cg = (tb & 1) * 7 + sl, ch = 32 * cg + r;
      const u32 vo1 = (lane ^ 1) * 16;
      float lr, li, cr, ci;
      if (lane < 54) {
        const char* bsrc = (const char*)(part ? P.Bim : P.Bre) + (size_t)(cg * 32 + half * 16) * 896;
        const u32 va = (tb2 & 1) ? vo1 : vo, vb = (tb2 & 1) ? vo : vo1;
#pragma unroll
        for (int j = 0; j < 16; ++j) {
          const int i = j ^ (8 * (tb2 & 1));
          glds16(bsrc + i * 896, j < 8 ? va : vb, L_C + (q * 16 + i) * 864);
        }
      }
      u32 vlre = P.lre[ch], vlim = P.lim[ch], vldt = P.ldt[ch];
      u32x4 blast_re = *(const u32x4*)(P.Bre + (size_t)ch * 448 + 432 + 8 * h);
      u32x4 blast_im = *(const u32x4*)(P.Bim + (size_t)ch * 448 + 432 + 8 * h);
      asm volatile("" : "+v"(vlre), "+v"(vlim), "+v"(vldt), "+v"(blast_re), "+v"(blast_im) :: "memory");
      {
        const float dt = rbf(expf(__uint_as_float(vldt << 16))), a = __uint_as_float(vlre << 16), bb = __uint_as_float(vlim << 16);
        const float er = expf(dt * a);
        float sn, cs;
        sincosf(dt * bb, &sn, &cs);
        lr = er * cs; PIN(lr); li = er * sn;
        const float nr = lr - 1.f, ni = li, den = 2.0f / (a * a + bb * bb);
        cr = (nr * a + ni * bb) * den; PIN(cr); ci = (ni * a - nr * bb) * den;
      }
      float l16r = lr, l16i = li;
#pragma unroll
      for (int i = 0; i < 4; ++i) cmul(l16r, l16i, l16r, l16i, l16r, l16i);
      float l32r, l32i, l128r, l128i;
      cmul(l16r, l16i, l16r, l16i, l32r, l32i);
      cmul(l32r, l32i, l32r, l32i, l128r, l128i);
      cmul(l128r, l128i, l128r, l128i, l128r, l128i);
      PIN4(lr, li, cr, ci); PIN4(l16r, l16i, l32r, l32i); PIN2(l128r, l128i);
      const char* xsrc = (const char*)P.Ax + (size_t)(tb2 * 128 + q * 32) * 896;
      const u32 xv0 = (lane >> 3) * 896 + (((lane & 7) ^ (lane >> 4)) << 4), xv1 = xv0 ^ 64;
      const int rho = 16 * ((r >> 2) & 1) + (r & 3) + 4 * (r >> 3), sw = (rho >> 1) & 7;
      u32 xa[4];
#pragma unroll
      for (int bb = 0; bb < 4; ++bb) xa[bb] = PA + rho * 128 + (((2 * bb + h) ^ sw) << 4);
      const int xrot = cg % 6;
      u32 ba[7];
#pragma unroll
      for (int c = 0; c < 7; ++c) {
        int kc = c + xrot; kc = c == 6 ? 6 : kc >= 6 ? kc - 6 : kc;
        ba[c] = L_C + r * 864 + ((h ^ ((r >> 3) & 1)) << 4) + 128 * kc;
        PIN(ba[c]);
      }
      PIN4(xa[0], xa[1], xa[2], xa[3]);
      arrive(C_BFULL, lane);
      TS(1);
      await(C_QDONE, 4);
      auto xdma = [&](int c) {
#pragma unroll
        for (int j = 0; j < 4; ++j) {
          int kc = c + xrot; kc = c == 6 ? 6 : kc >= 6 ? kc - 6 : kc;
          glds16(xsrc + kc * 128 + j * 7168, (j & 1) ? xv1 : xv0, PA + (c % 6) * 4096 + j * 1024);
        }
      };
      xdma(0);
      if (XG != 1) xdma(1);
      await(C_BFULL, 4);
      TS(2);
      f32x16 ur = {}, ui = {};
      PIN2(ur, ui);
      __builtin_amdgcn_s_setprio(G1_PRIO);
      {
        u32x4 xf[28], br[28], bi[28];
#pragma unroll
        for (int ks = 0; ks < 28 + 4; ++ks) {
          if (ks >= 4) {
            const int k = ks - 4;
            ur = MFMA(frag(xf[k]), frag(br[k]), ur);
            ui = MFMA(frag(xf[k]), frag(bi[k]), ui);
            SBAR();
          }
          if (ks < 28) {
            if ((ks & 3) == 0) {
              const int c = ks >> 2;
              if (c + 1 <= 6 && c + 1 != XG) vmwait<4>(); else vmwait<0>();
              if (c + 1 == XG) { asm volatile("s_nop 7\n\ts_nop 7" ::: "memory"); await(C_KVL, 4); xdma(c + 1); }
              if (c + 2 <= 6 && c + 2 != XG) xdma(c + 2);
            }
            xf[ks] = ldsr128(xa[ks & 3] + ((ks >> 2) % 6) * 4096);
            br[ks] = ks < 27 ? ldsr128(ba[ks >> 2] + 32 * (ks & 3)) : blast_re;
            bi[ks] = ks < 27 ? ldsr128(ba[ks >> 2] + 27648 + 32 * (ks & 3)) : blast_im;
            SBAR();
          }
        }
      }
      MPIN2(ur, ui);
      __builtin_amdgcn_s_setprio(AUX_PRIO);
      TS(3);
      float ar = 0.f, ai = 0.f;
#pragma unroll
      for (int i = 0; i < 16; ++i) {
        float tr, ti;
        cmul(cr, ci, ur[i], ui[i], tr, ti);
        ur[i] = tr; ui[i] = ti;
        cstep(lr, li, ar, ai, tr, ti);
      }
      const float or_ = swap32(ar, h), oi_ = swap32(ai, h);
      const float lor = h ? or_ : ar, loi = h ? oi_ : ai;
      float a32r = lor, a32i = loi;
      cstep(l16r, l16i, a32r, a32i, h ? ar : or_, h ? ai : oi_);
      if (q < 3 && h == 0) *(LAS f32x2*)(size_t)(L_AGG + q * 256 + r * 8) = (f32x2){a32r, a32i};
      arrive(C_AGG, lane);
      await(C_AGG, 4);
      f32x2 wagg[3];
#pragma unroll
      for (int j = 0; j < 3; ++j) wagg[j] = *(LAS f32x2*)(size_t)(L_AGG + j * 256 + r * 8);
      const u32 aggb = WS_AGG + cg * 8192;
      if (q == 3 && tb2 < 15) {
        float tr = wagg[0].x, ti = wagg[0].y;
        cstep(l32r, l32i, tr, ti, wagg[1].x, wagg[1].y);
        cstep(l32r, l32i, tr, ti, wagg[2].x, wagg[2].y);
        cstep(l32r, l32i, tr, ti, a32r, a32i);
        __hip_atomic_store((GAS u64*)(ws + aggb + tb2 * 512 + r * 16 + h * 8), (u64)__float_as_uint(h ? ti : tr) | ((u64)tag << 32), __ATOMIC_RELAXED, __HIP_MEMORY_SCOPE_AGENT);
      }
      TS(4);
      float hr = 0.f, hi = 0.f;
      if (q < 3) {
#pragma unroll 1
        for (int g = q; g < 7; g += 3) stage_C4(3, g);
        stage_C4(q, 0); stage_C4(q, 1);
        vmwait<8>();
        arrive(C_C3, lane);
#pragma unroll 1
        for (int g = 2; g < CEARLY; ++g) stage_C4(q, g);
        TS(10);
        await(C_CARRY, 1);
        const f32x2 cv = *(LAS f32x2*)(size_t)(L_CARRY + r * 8);
        hr = cv.x; hi = cv.y;
      } else {
        if (tb2 > 0) {
          u32x4 g[8];
          u32 gv[8];
#pragma unroll
          for (int j = 0; j < 8; ++j) gv[j] = r * 16 + ((8 * h + j < tb2) ? (8 * h + j) * 512 : 0);
#pragma unroll 1
          for (u32 spin = 0; spin < (1u << 17); ++spin) {
            asm volatile("" ::: "memory");
            u32 bad = 0;
#pragma unroll
            for (int j = 0; j < 8; ++j) g[j] = __builtin_amdgcn_raw_buffer_load_b128(rs_ws, gv[j], aggb, 16);
#pragma unroll
            for (int j = 0; j < 8; ++j) bad |= (g[j].y ^ tag) | (g[j].w ^ tag);
            if (__all(bad == 0)) break;
            __builtin_amdgcn_s_sleep(4);
          }
          asm volatile("" ::: "memory");
#pragma unroll
          for (int j = 0; j < 8; ++j)
            if (j < tb2) cstep(l128r, l128i, hr, hi, __uint_as_float(g[j].x), __uint_as_float(g[j].z));
          const float sr = swap32(hr, h), si = swap32(hi, h);
          if (h) { hr = sr; hi = si; }
          if (tb2 > 8) {
#pragma unroll
            for (int j = 0; j < 7; ++j)
              if (8 + j < tb2) cstep(l128r, l128i, hr, hi, __uint_as_float(g[j].x), __uint_as_float(g[j].z));
            const float tr = swap32(hr, h), ti = swap32(hi, h);
            if (!h) { hr = tr; hi = ti; }
          }
        }
        if (h == 0) *(LAS f32x2*)(size_t)(L_CARRY + r * 8) = (f32x2){hr, hi};
        arrive(C_CARRY, lane);
        TS(10);
      }
#pragma unroll
      for (int j = 0; j < 3; ++j)
        if (j < q) cstep(l32r, l32i, hr, hi, wagg[j].x, wagg[j].y);
      if (h) cstep(l16r, l16i, hr, hi, lor, loi);
      TS(5);
      u32 pr[8], pi[8];
#pragma unroll
      for (int i = 0; i < 16; i += 2) {
        cstep(lr, li, hr, hi, ur[i], ui[i]);
        const float r0 = hr, i0 = hi;
        cstep(lr, li, hr, hi, ur[i + 1], ui[i + 1]);
        pr[i >> 1] = cvtpk(r0, hr); pi[i >> 1] = cvtpk(-i0, -hi);
      }
      const u32 ta = L_C + q * 5120 + r * 80 + 32 * h;
      ldsw128(ta, (u32x4){pr[0], pr[1], pr[2], pr[3]}); ldsw128(ta + 16, (u32x4){pr[4], pr[5], pr[6], pr[7]});
      ldsw128(ta + 2560, (u32x4){pi[0], pi[1], pi[2], pi[3]}); ldsw128(ta + 2560 + 16, (u32x4){pi[4], pi[5], pi[6], pi[7]});
      LGKM0();
      const u32 tr = L_C + q * 5120 + (8 * h + q4) * 80 + g1 * 32 + 8 * p4;
      const int tbh = 2 * tb2 + (q >> 1);
      const u32 so = WS_HF + ((tbh * 2 + (q & 1)) * 56 + 2 * cg) * 1024 + vo;
#pragma unroll
      for (int pt = 0; pt < 2; ++pt)
#pragma unroll
        for (int s = 0; s < 2; ++s) {
          const u32 a = tr + pt * 2560 + s * 1280;
          __builtin_amdgcn_raw_buffer_store_b128(trfrag(a, a + 320), rs_ws, so + pt * 28 * 1024 + s * 1024, 0, 16);
        }
      arrive(C_HD, lane);
      VMCNT(0);
      if (lane == 0) gstore_flag(ws, WS_HFLAG + (tbh * 32 + cg * 2 + (q & 1)) * 128, tag);
      TS(6);
      if (q < 3) {
#pragma unroll 1
        for (int g = CEARLY; g < 7; ++g) stage_C4(q, g);
        VMCNT(0);
      } else {
        await(C_C3, 3);
      }
      read_C();
      TS(9);
      const char* src = (const char*)P.wo + sl * 128 + q * 112 * 896;
      const u32 wv8 = (lane >> 3) * 896 + (lane & 7) * 16;
#pragma unroll
      for (int j = 0; j < 14; ++j) {
        int i = j + 4 * tl; i = i >= 14 ? i - 14 : i;
        glds16(src + i * 7168, wv8, PA + L_WO_OFF + 1024 * i); VMTHR();
      }
      VMCNT(0);
      arrive(C_WO, lane);
      TS(11);
    }
    if (q == 0) { poll_words(ws, WS_HFLAG + tb * 4096, 28, tag, lane); arrive(C_HF, lane); }
    else await(C_HF, 1);
    TS(7);
    {
      f32x16 y[4];
      const u32 so = WS_HF + ((tb * 2 + tm) * 56 + part * 28 + half * 14) * 1024, so1 = WS_HF + ((tb * 2 + (tm ^ 1)) * 56 + part * 28 + half * 14) * 1024;
      u32x4 hf[28];
#define LDH(j) __builtin_amdgcn_raw_buffer_load_b128(rs_ws, vo + (((j) % 14) & 3) * 1024, ((j) < 14 ? so : so1) + (((j) % 14) >> 2) * 4096, 16)
#pragma unroll
      for (int j = 0; j < HWIN; ++j) hf[j] = LDH(j);
      SBAR();
      const f32x16 zero = {};
      __builtin_amdgcn_s_setprio(AUX2_PRIO);
#pragma unroll
      for (int j = 0; j < 28; ++j) {
        const int t = j / 14, k = j % 14;
        y[t] = MFMA(frag(hf[j]), frag(cf[0][k]), k == 0 ? zero : y[t]);
        y[2 + t] = MFMA(frag(hf[j]), frag(cf[1][k]), k == 0 ? zero : y[2 + t]);
        SBAR();
        if (j + HWIN < 28) { hf[j + HWIN] = LDH(j + HWIN); SBAR(); }
      }
      u16 xs[16];
      {
        const auto rs_ax = __builtin_amdgcn_make_buffer_rsrc((void*)P.Ax, 0, 2048 * 896, 0x00020000);
        const u32 xv = (t0 * 448 + dcol) * 2;
#pragma unroll
        for (int i = 0; i < 16; ++i) xs[i] = __builtin_amdgcn_raw_buffer_load_b16(rs_ax, xv + (i & 3) * 896, (i >> 2) * 7168, 0);
      }
      const u16 dsk16 = P.Dsk[dcol];
      MPIN4(y[0], y[1], y[2], y[3]);
      __builtin_amdgcn_s_setprio(AUX3_PRIO);
#pragma unroll
      for (int t = 1; t < 4; ++t) {
#pragma unroll
        for (int g = 0; g < 4; ++g) ldsw128(PA + (t - 1) * 4096 + g * 1024 + vo, f4(y[t], g));
      }
      arrive(C_Y, lane);
      await(C_Y, 4);
      f32x16 own = y[0];
#pragma unroll
      for (int t = 1; t < 4; ++t) {
#pragma unroll
        for (int g = 0; g < 4; ++g) add4(own, g, ldsr128((q ^ t) * L_PRIV + (t - 1) * 4096 + g * 1024 + vo));
      }
      if (valid) {
        const u32 ov = (t0 * 896 + dcol) * 4;
#pragma unroll
        for (int i = 0; i < 16; ++i) {
          float sk = bf2f(xs[i]) * bf2f(dsk16);
          PIN(sk);
          __builtin_amdgcn_raw_buffer_store_b32(__float_as_uint(own[i] + rbf(sk)), rs_out, ov, ((i & 3) + 8 * (i >> 2)) * 3584, OUT_AUX);
        }
      }
    }
    TS(8);
    TSW();
    if (tb == 31 && sl == 0 && q == 0) {
      await(C_KV, 1);
      if (lane == 0) gstore_flag(ws, WS_EPOCH, tag);
    }
  }
}

extern "C" void kernel_launch(void* const* d_in, const int* in_sizes, int n_in, void* d_out, int out_size, void* d_ws, size_t ws_size,
                              hipStream_t stream) {
  Params P;
  P.Ax = (const u16*)d_in[0]; P.lre = (const u16*)d_in[1]; P.lim = (const u16*)d_in[2]; P.Bre = (const u16*)d_in[3];
  P.Bim = (const u16*)d_in[4]; P.Cre = (const u16*)d_in[5]; P.Cim = (const u16*)d_in[6]; P.ldt = (const u16*)d_in[7];
  P.Dsk = (const u16*)d_in[8]; P.Bx = (const u16*)d_in[9]; P.wq = (const u16*)d_in[10]; P.wk = (const u16*)d_in[11];
  P.wv = (const u16*)d_in[12]; P.wo = (const u16*)d_in[13];
  P.out = (float*)d_out; P.ws = (char*)d_ws;
  (void)hipFuncSetAttribute((const void*)fused_kernel, hipFuncAttributeMaxDynamicSharedMemorySize, LDS_BYTES);
  fused_kernel<<<256, 512, LDS_BYTES, stream>>>(P);
}
```

```cpp
#define PROBE 13
#define PAUX 1
#include <hip/hip_runtime.h>

typedef unsigned short u16;
typedef unsigned int u32;
typedef unsigned long long u64;
using bf16x8 = __attribute__((ext_vector_type(8))) short;
using f32x16 = __attribute__((ext_vector_type(16))) float;
using u32x4 = __attribute__((ext_vector_type(4))) unsigned;
using u32x2 = __attribute__((ext_vector_type(2))) unsigned;
typedef short v4i16_t __attribute__((ext_vector_type(4)));
typedef __bf16 bf16x2_t __attribute__((ext_vector_type(2)));
typedef float f32x2 __attribute__((ext_vector_type(2)));
#define LAS __attribute__((address_space(3)))
#define GAS __attribute__((address_space(1)))
#define DI __device__ __forceinline__
#define MFMA(a, b, c) __builtin_amdgcn_mfma_f32_32x32x16_bf16((a), (b), (c), 0, 0, 0)
#define SBAR() __builtin_amdgcn_sched_barrier(0)
#define PIN(x) asm volatile("" : "+v"(x))
#define PIN2(a, b) asm volatile("" : "+v"(a), "+v"(b))
#define PIN4(a, b, c, d) asm volatile("" : "+v"(a), "+v"(b), "+v"(c), "+v"(d))
#define MPIN(x) asm volatile("s_nop 7\n\ts_nop 7" : "+v"(x))
#define MPIN2(a, b) asm volatile("s_nop 7\n\ts_nop 7" : "+v"(a), "+v"(b))
#define MPIN4(a, b, c, d) asm volatile("s_nop 7\n\ts_nop 7" : "+v"(a), "+v"(b), "+v"(c), "+v"(d))
#define VMCNT(n) asm volatile("s_waitcnt vmcnt(" #n ")" ::: "memory")
#define LGKM0() asm volatile("s_waitcnt lgkmcnt(0)" ::: "memory")
#define EX(x) __builtin_amdgcn_exp2f(x)

#define NOLOAD 0
#define THR 4
#define KV_AUX 16
#define H_AUX 16
#define FENCE 0
#define ABL 0
#define G1_PRIO 0
#define AUX3_PRIO 2
#define QEARLY 1
#define OUT_AUX 17
#define POLL_SLEEP 2
#define WARM 1
#define PREF 0
#define XEARLY 2
#define XD 4
#define CEARLY 3
#define ROLLED 0
#define HWIN 8
#define PF7 1
#define XG 2
#define WOLATE 0
#define XW 0
#define NOSSM 0
#define KV_DEPTH 4
#define AUX_PRIO 2
#define AUX2_PRIO 0
#define GATE 0
#define KV_REPL 32

constexpr u32 WS_EPOCH = 0;
constexpr u32 WS_HFLAG = 7u << 20;
constexpr u32 WS_OFLAG = WS_HFLAG + 131072;
constexpr u32 WS_KVFLAG = WS_OFLAG + 131072;
constexpr u32 WS_AGG = 32768;
constexpr u32 WS_KVF = WS_AGG + 131072;
constexpr u32 WS_OF = WS_KVF + 262144;
constexpr u32 WS_HF = WS_OF + 2097152;
constexpr u32 WS_END = WS_KVFLAG + 32768;
constexpr u32 WS_DBG = 8u << 20;

constexpr u32 L_XB = 0;
constexpr u32 L_WQ = 57344;
constexpr u32 L_C = 107520;
constexpr u32 L_MISC = L_C + 55296;
constexpr u32 L_AGG = L_MISC + 128;
constexpr u32 L_KN = L_MISC + 896;
constexpr u32 L_CARRY = L_C + 20480;
constexpr u32 L_PRIV = 26880;
constexpr u32 L_WO_OFF = 12288;
constexpr u32 L_QX = L_WQ;
constexpr u32 LDS_BYTES = 163840;
enum { C_WQ = 0, C_Q2, C_QDONE, C_BFULL, C_AGG, C_WO, C_O, C_MAX, C_P, C_Y, C_FB, C_KVP, C_KMAX, C_KV, C_OF, C_HF, C_WQ2, C_HD, C_CREG, C_KVL, C_KVR, C_KVD, C_C3, C_CARRY, C_EPOCH = 31 };

struct Params {
  const u16 *Ax, *lre, *lim, *Bre, *Bim, *Cre, *Cim, *ldt, *Dsk, *Bx, *wq, *wk, *wv, *wo;
  float* out;
  char* ws;
};

DI float bf2f(u16 v) { return __uint_as_float(((u32)v) << 16); }
DI u32 cvtpk(float a, float b) { return __builtin_bit_cast(u32, __builtin_convertvector((f32x2){a, b}, bf16x2_t)); }
DI float rbf(float a) { return __uint_as_float(cvtpk(a, 0.f) << 16); }
DI bf16x8 frag(u32x4 v) { return __builtin_bit_cast(bf16x8, v); }
DI void glds16(const void* sbase, u32 voff, u32 lds_base) {
  asm volatile("s_mov_b32 m0, %2\n\ts_nop 0\n\tglobal_load_lds_dwordx4 %0, %1" :: "v"(voff), "s"(sbase), "s"(lds_base) : "memory");
}
DI void glds4(const void* sbase, u32 voff, u32 lds_base) {
  asm volatile("s_mov_b32 m0, %2\n\ts_nop 0\n\tglobal_load_lds_dword %0, %1" :: "v"(voff), "s"(sbase), "s"(lds_base) : "memory");
}
template <int N> DI void vmwait() { asm volatile("s_waitcnt vmcnt(%0)" :: "i"(N) : "memory"); }
DI void vmwait_rt(int n) {
  switch (n) {
    case 0: vmwait<0>(); break; case 4: vmwait<4>(); break; case 8: vmwait<8>(); break; case 12: vmwait<12>(); break;
    case 16: vmwait<16>(); break; case 20: vmwait<20>(); break; default: vmwait<0>(); break;
  }
}
DI u32x4 ldsr128(u32 a) { return *(const LAS u32x4*)(size_t)a; }
DI void ldsw128(u32 a, u32x4 v) { *(LAS u32x4*)(size_t)a = v; }
DI u32 ldsu(u32 a) { return (u32)__builtin_amdgcn_readfirstlane(*(volatile LAS u32*)(size_t)a); }
DI u32x2 ldstr(u32 a) { return __builtin_bit_cast(u32x2, __builtin_amdgcn_ds_read_tr16_b64_v4i16((LAS v4i16_t*)(size_t)a)); }
DI u32x4 trfrag(u32 a0, u32 a1) { u32x2 lo = ldstr(a0), hi = ldstr(a1); return (u32x4){lo.x, lo.y, hi.x, hi.y}; }
DI u32x4 f4(const f32x16& v, int g) {
  return (u32x4){__float_as_uint(v[4 * g]), __float_as_uint(v[4 * g + 1]), __float_as_uint(v[4 * g + 2]), __float_as_uint(v[4 * g + 3])};
}
DI void add4(f32x16& v, int g, u32x4 a) {
  v[4 * g] += __uint_as_float(a.x); v[4 * g + 1] += __uint_as_float(a.y); v[4 * g + 2] += __uint_as_float(a.z); v[4 * g + 3] += __uint_as_float(a.w);
}
DI void arrive(int c, int lane) {
  asm volatile("s_waitcnt lgkmcnt(0)" ::: "memory");
  if (lane == 0) asm volatile("ds_add_u32 %0, %1" :: "v"(L_MISC + 4 * c), "v"(1u) : "memory");
}
DI void await(int c, u32 target) {
#pragma unroll 1
  for (u32 spin = 0; spin < (1u << 22); ++spin) {
    asm volatile("" ::: "memory");
    if (ldsu(L_MISC + 4 * c) >= target) break;
    __builtin_amdgcn_s_sleep(1);
  }
  asm volatile("" ::: "memory");
}
DI float swap32(float v, bool hi) {
  auto r = __builtin_amdgcn_permlane32_swap(__float_as_uint(v), __float_as_uint(v), false, false);
  return __uint_as_float(hi ? r[0] : r[1]);
}
DI float wave_max_nonneg(float v) {
  u32 x = __float_as_uint(v);
  x = max(x, (u32)__builtin_amdgcn_update_dpp(0, (int)x, 0x111, 0xf, 0xf, false));
  x = max(x, (u32)__builtin_amdgcn_update_dpp(0, (int)x, 0x112, 0xf, 0xf, false));
  x = max(x, (u32)__builtin_amdgcn_update_dpp(0, (int)x, 0x114, 0xf, 0xf, false));
  x = max(x, (u32)__builtin_amdgcn_update_dpp(0, (int)x, 0x118, 0xf, 0xf, false));
  x = max(x, (u32)__builtin_amdgcn_update_dpp(0, (int)x, 0x142, 0xa, 0xf, false));
  x = max(x, (u32)__builtin_amdgcn_update_dpp(0, (int)x, 0x143, 0xc, 0xf, false));
  return __uint_as_float((u32)__builtin_amdgcn_readlane((int)x, 63));
}
DI void gstore_flag(char* ws, u32 off, u32 v) { __hip_atomic_store((GAS u32*)(ws + off), v, __ATOMIC_RELAXED, __HIP_MEMORY_SCOPE_AGENT); }
DI void poll_words(char* ws, u32 off, int n, u32 tag, int lane) {
  GAS u32* p = (GAS u32*)(ws + off) + (lane < n ? lane : 0) * 32;
  u32 a = __hip_atomic_load(p, __ATOMIC_RELAXED, __HIP_MEMORY_SCOPE_AGENT);
#pragma unroll 1
  for (u32 spin = 0; spin < (1u << 16); ++spin) {
    __builtin_amdgcn_s_sleep(POLL_SLEEP);
    u32 c = __hip_atomic_load(p, __ATOMIC_RELAXED, __HIP_MEMORY_SCOPE_AGENT);
    SBAR();
    if (__all(a == tag)) break;
    __builtin_amdgcn_s_sleep(POLL_SLEEP);
    a = __hip_atomic_load(p, __ATOMIC_RELAXED, __HIP_MEMORY_SCOPE_AGENT);
    SBAR();
    if (__all(c == tag)) break;
  }
  asm volatile("" ::: "memory");
}
DI float poll_kv(char* ws, int rep, u32 tag, int lane) {
  GAS u64* p = (GAS u64*)(ws + WS_KVFLAG + rep * 4096) + (lane & 31) * 16;
  u64 a = __hip_atomic_load(p, __ATOMIC_RELAXED, __HIP_MEMORY_SCOPE_AGENT), g = 0;
#pragma unroll 1
  for (u32 spin = 0; spin < (1u << 16); ++spin) {
    __builtin_amdgcn_s_sleep(POLL_SLEEP);
    u64 c = __hip_atomic_load(p, __ATOMIC_RELAXED, __HIP_MEMORY_SCOPE_AGENT);
    SBAR();
    g = a;
    if (__all((u32)a == tag)) break;
    __builtin_amdgcn_s_sleep(POLL_SLEEP);
    a = __hip_atomic_load(p, __ATOMIC_RELAXED, __HIP_MEMORY_SCOPE_AGENT);
    SBAR();
    g = c;
    if (__all((u32)c == tag)) break;
  }
  asm volatile("" ::: "memory");
  return __uint_as_float((u32)(g >> 32));
}
DI void cmul(float ar, float ai, float br, float bi, float& cr, float& ci) {
  float x, y;
  asm("v_mul_f32 %0, %2, %4\n\tv_mul_f32 %1, %2, %5\n\tv_fma_f32 %0, -%3, %5, %0\n\tv_fma_f32 %1, %3, %4, %1"
      : "=&v"(x), "=&v"(y) : "v"(ar), "v"(ai), "v"(br), "v"(bi));
  cr = x; ci = y;
}
DI void cstep(float ar, float ai, float& hr, float& hi, float ur, float ui) {
  float x, y;
  asm("v_fma_f32 %0, -%3, %5, %6\n\tv_fma_f32 %1, %3, %4, %7\n\tv_fma_f32 %0, %2, %4, %0\n\tv_fma_f32 %1, %2, %5, %1"
      : "=&v"(x), "=&v"(y) : "v"(ar), "v"(ai), "v"(hr), "v"(hi), "v"(ur), "v"(ui));
  hr = x; hi = y;
}
DI void set_pad(u32x4& f, float m, int h) {
  const float a = rbf(-m), b = rbf(-m - a), c = rbf(-m - a - b);
  if (h) { f.z = cvtpk(a, b); f.w = cvtpk(c, 0.f); }
}

#define TS(i) do { if ((i) == PROBE) asm volatile("s_memrealtime %0\n\ts_waitcnt lgkmcnt(0)" : "=s"(tprobe) :: "memory"); } while (0)
#define TSD() u64 tprobe = 0
#define TSW() do { if ((wv >= 4) == PAUX && tprobe != 0) while (__builtin_amdgcn_s_memrealtime() - tprobe < 3000) __builtin_amdgcn_s_sleep(2); } while (0)
#define LPRIO 0
#define PUP() do {} while (0)
#define PDN() SBAR()
#define STR_(x) #x
#define VMTHR_(n) asm volatile("s_waitcnt vmcnt(" STR_(n) ")" ::: "memory")
#define VMTHR() VMTHR_(THR)

template <bool W_IS_A, int WROW, bool MID, class F>
DI f32x16 proj_tile(int row, int h, u32 wa, int lane, F mid) {
  const int sw = (row >> 1) & 7;
  u32 xa[4];
#pragma unroll
  for (int bb = 0; bb < 4; ++bb) xa[bb] = L_XB + row * 128 + (((2 * bb + h) ^ sw) << 4);
  u32x4 xf[28], wf[28];
  f32x16 acc = {};
#pragma unroll
  for (int ks = 0; ks < 28 + 5; ++ks) {
    if (ks >= 5) {
      const int k = ks - 5;
      acc = W_IS_A ? MFMA(frag(wf[k]), frag(xf[k]), acc) : MFMA(frag(xf[k]), frag(wf[k]), acc);
      SBAR();
    }
    if (ks < 28) {
      if (MID && ks == 14) {
        asm volatile("s_nop 7\n\ts_nop 7" ::: "memory");
        mid();
        asm volatile("s_waitcnt vmcnt(0) lgkmcnt(0)" ::: "memory");
        if (lane == 0) asm volatile("ds_add_u32 %0, %1" :: "v"(L_MISC + 4 * C_WQ2), "v"(1u) : "memory");
        await(C_WQ2, 4);
      }
      xf[ks] = ldsr128(xa[ks & 3] + 8192 * (ks >> 2));
      wf[ks] = trfrag(wa + ks * 16 * WROW, wa + ks * 16 * WROW + 4 * WROW);
      SBAR();
    }
  }
  MPIN(acc);
  return acc;
}

__global__ void __launch_bounds__(512, 2) fused_kernel(Params P) {
  extern __shared__ __attribute__((aligned(16))) char lds_dyn[];
  asm volatile("" : "+s"(P.Ax), "+s"(P.lre), "+s"(P.lim), "+s"(P.Bre), "+s"(P.Bim), "+s"(P.Cre), "+s"(P.Cim), "+s"(P.ldt));
  asm volatile("" : "+s"(P.Dsk), "+s"(P.Bx), "+s"(P.wq), "+s"(P.wk), "+s"(P.wv), "+s"(P.wo), "+s"(P.out), "+s"(P.ws));
  const int tid = threadIdx.x, lane = tid & 63, wv = __builtin_amdgcn_readfirstlane(tid >> 6);
  const int r = lane & 31, h = lane >> 5;
  const int b = blockIdx.x;
  const int tb = (b & 7) * 4 + (b >> 6), sl = (b >> 3) & 7;
  char* ws = P.ws;
  const int tl = b >> 6;
  TSD();
  TS(0);
  if (tid == 0) asm volatile("s_mov_b32 m0, %2\n\ts_nop 0\n\tglobal_load_lds_dword %0, %1 sc1" :: "v"(0), "s"(ws), "s"(L_MISC + 4 * C_EPOCH) : "memory");
  const u32 vo = lane * 16;
  {
    const int row = 8 * wv + (lane >> 3);
    const u32 xv = row * 896 + (((lane & 7) ^ ((row >> 1) & 7)) << 4);
    const char* src = (const char*)P.Bx + (size_t)tb * 64 * 896;
#pragma unroll
    for (int j = 0; j < 7; ++j) {
      int c = j + sl; c = c >= 7 ? c - 7 : c;
      glds16(src + c * 128, xv, L_XB + c * 8192 + wv * 1024);
    }
  }
  if (tid < 31) *(LAS u32*)(size_t)(L_MISC + 4 * tid) = 0;
  const auto rs_ws = __builtin_amdgcn_make_buffer_rsrc((void*)ws, 0, WS_END, 0x00020000);
  const auto rs_out = __builtin_amdgcn_make_buffer_rsrc((void*)P.out, 0, 2048 * 896 * 4, 0x00020000);
  const int g1 = (lane >> 4) & 1, q4 = (lane >> 2) & 3, p4 = lane & 3;
  const int l7 = lane / 7;
  const u32 vo7 = l7 * 896 + (lane - 7 * l7) * 16;

  if (wv < 4) {
    const int w = wv;
    const char* wqsrc = (const char*)P.wq + sl * 112 + w * 7 * 7168;
    auto stage_wq = [&](int hf) {
      if (lane < 56) {
#pragma unroll
        for (int j = 0; j < 7; ++j) {
          int i = j + 2 * tl; i = (i >= 7 ? i - 7 : i) + 28 * hf;
          glds16(wqsrc + i * 7168, vo7, L_WQ + (w * 7 + i) * 896);
        }
      }
    };
    if (sl != 7) {
      stage_wq(0); stage_wq(1);
      asm volatile("s_waitcnt vmcnt(14) lgkmcnt(0)\n\ts_barrier" ::: "memory");
    } else {
      VMCNT(0);
      stage_wq(0); stage_wq(1);
      asm volatile("s_waitcnt lgkmcnt(0)\n\ts_barrier" ::: "memory");
    }
    const u32 tag = ldsu(L_MISC + 4 * C_EPOCH) + 1u;
    VMCNT(7);
    arrive(C_WQ, lane);
    await(C_WQ, 4);
    {
      f32x16 acc = proj_tile<true, 112, true>(32 * (w & 1) + r, h, L_WQ + (8 * h + q4) * 112 + (w >> 1) * 56 + g1 * 32 + 8 * p4, lane,
                                            []() {});
      TS(8);
      const float qs = 0.27264060f;
      float n2 = 0.f;
#pragma unroll
      for (int i = 0; i < 16; ++i) { acc[i] *= qs; if (i < 12) n2 = fmaf(acc[i], acc[i], n2); else n2 = h ? n2 : fmaf(acc[i], acc[i], n2); }
      n2 += swap32(n2, h);
      u32x4 f0 = {cvtpk(acc[0], acc[1]), cvtpk(acc[2], acc[3]), cvtpk(acc[4], acc[5]), cvtpk(acc[6], acc[7])};
      u32x4 f1 = {cvtpk(acc[8], acc[9]), cvtpk(acc[10], acc[11]), cvtpk(acc[12], acc[13]), cvtpk(acc[14], acc[15])};
      if (h) { f1.z = 0; f1.w = __float_as_uint(__builtin_amdgcn_sqrtf(n2)); }
      ldsw128(L_QX + w * 2048 + vo, f0);
      ldsw128(L_QX + w * 2048 + 1024 + vo, f1);
      arrive(C_Q2, lane);
    }
    u32x4 qf[4][2];
    if (QEARLY) {
      await(C_Q2, 4);
#pragma unroll
      for (int t = 0; t < 4; ++t) { const u32 a = L_QX + ((w + t) & 3) * 2048 + vo; qf[t][0] = ldsr128(a); qf[t][1] = ldsr128(a + 1024); }
      arrive(C_QDONE, lane);
    }
    TS(2);
    if (w == 0) {
      const float kn = wave_max_nonneg(poll_kv(ws, b & 7, tag, lane));
      if (lane == 0) *(LAS float*)(size_t)L_KN = kn;
      arrive(C_KV, lane);
    } else {
      await(C_KV, 1);
    }
    TS(3);
    const u32 kvbase = WS_KVF + w * 65536;
    const int rot = b >> 3;
#define KVOFF(it) (kvbase + ((((it) + rot) & 15) << 12))
#define LDK(it, s) __builtin_amdgcn_raw_buffer_load_b128(rs_ws, vo + 1024 * (s), KVOFF(it), KV_AUX)
#define LDV(it, s) __builtin_amdgcn_raw_buffer_load_b128(rs_ws, vo + 2048 + 1024 * (s), KVOFF(it), KV_AUX)
#define NEXT(x) asm volatile("s_add_u32 %0, %0, 0x1000\n\ts_and_b32 %0, %0, 0xf000" : "+s"(x) :: "scc")
#define LDK2(s_) __builtin_amdgcn_raw_buffer_load_b128(rs_kv, vo + 1024 * (s_), koff, KV_AUX)
#define LDV2(s_) __builtin_amdgcn_raw_buffer_load_b128(rs_kv, vo + 2048 + 1024 * (s_), voff, KV_AUX)
    const auto rs_kv = __builtin_amdgcn_make_buffer_rsrc((void*)(ws + kvbase), 0, 65536, 0x00020000);
    f32x16 o[4];
    for (int attempt = 0; attempt < 2; ++attempt) {
      u32x4 kf[KV_DEPTH][2], vf[KV_DEPTH][2];
#pragma unroll
      for (int d = 0; d < KV_DEPTH; ++d) {
        kf[d][0] = LDK(d, 0); kf[d][1] = LDK(d, 1);
        if (d < KV_DEPTH - 1) { vf[d][0] = LDV(d, 0); vf[d][1] = LDV(d, 1); }
      }
      u32 koff = ((KV_DEPTH - 1 + rot) & 15) << 12, voff = ((KV_DEPTH - 2 + rot) & 15) << 12;
      SBAR();
      if (attempt == 0) {
        arrive(C_KVL, lane);
        if (!QEARLY) {
        await(C_Q2, 4);
#pragma unroll
        for (int t = 0; t < 4; ++t) { const u32 a = L_QX + ((w + t) & 3) * 2048 + vo; qf[t][0] = ldsr128(a); qf[t][1] = ldsr128(a + 1024); }
        arrive(C_QDONE, lane);
        }
        const float kmax = __builtin_amdgcn_sqrtf(*(LAS float*)(size_t)L_KN);
#pragma unroll
        for (int t = 0; t < 4; ++t) set_pad(qf[t][1], __uint_as_float(qf[t][1].w) * kmax, h);
      }
#pragma unroll
      for (int t = 0; t < 4; ++t) { PIN(qf[t][0]); PIN(qf[t][1]); }
      SBAR();
      const f32x16 zero = {};
      f32x16 sc = MFMA(frag(kf[0][0]), frag(qf[0][0]), zero);
      sc = MFMA(frag(kf[0][1]), frag(qf[0][1]), sc);
      TS(9);
      float e0 = 0.f, e1 = 0.f, e2 = 0.f, e3 = 0.f;
      u32x4 p1 = {0, 0, 0, 0};
#pragma unroll
      for (int it = 0; it < 16; ++it) {
        const int d = it % KV_DEPTH, dprev = (it + KV_DEPTH - 1) % KV_DEPTH;
#pragma unroll
        for (int qi = 0; qi < 4; ++qi) {
          const int dn = qi == 3 ? (it + 1) % KV_DEPTH : d, qn = (qi + 1) & 3;
          const int dp = qi == 0 ? dprev : d, qp = (qi + 3) & 3;
          const bool last = it == 15 && qi == 3;
          f32x16 sn; u32x4 p0;
          float c0, c1, c2, c3;
          if (!last) sn = MFMA(frag(kf[dn][0]), frag(qf[qn][0]), zero);
          SBAR();
          if (qi == 0) NEXT(voff);
          if (qi == 2) NEXT(koff);
          p1.z = cvtpk(e0, e1); p1.w = cvtpk(e2, e3); PIN(p1); SBAR();
          c0 = EX(sc[0]); c1 = EX(sc[1]); c2 = EX(sc[2]); c3 = EX(sc[3]); PIN4(c0, c1, c2, c3); SBAR();
          if (it > 0 || qi > 0) o[qp] = MFMA(frag(vf[dp][1]), frag(p1), o[qp]);
          SBAR();
          if (qi < 2 && it - 1 + KV_DEPTH < 16) { vf[dprev][qi] = LDV2(qi); SBAR(); }
          p0.x = cvtpk(c0, c1); p0.y = cvtpk(c2, c3); PIN2(p0.x, p0.y); SBAR();
          c0 = EX(sc[4]); c1 = EX(sc[5]);
          c2 = EX(sc[6]); c3 = EX(sc[7]); PIN4(c0, c1, c2, c3); SBAR();
          if (!last) sn = MFMA(frag(kf[dn][1]), frag(qf[qn][1]), sn);
          SBAR();
          if (qi == 2 && it + KV_DEPTH < 16) { kf[d][0] = LDK2(0); SBAR(); }
          p0.z = cvtpk(c0, c1); p0.w = cvtpk(c2, c3); PIN(p0); SBAR();
          c0 = EX(sc[8]); c1 = EX(sc[9]);
          c2 = EX(sc[10]); c3 = EX(sc[11]); PIN4(c0, c1, c2, c3); SBAR();
          o[qi] = MFMA(frag(vf[d][0]), frag(p0), it == 0 ? zero : o[qi]); SBAR();
          if (qi == 2 && it + KV_DEPTH < 16) { kf[d][1] = LDK2(1); SBAR(); }
          p1.x = cvtpk(c0, c1); p1.y = cvtpk(c2, c3); PIN2(p1.x, p1.y); SBAR();
          e0 = EX(sc[12]); e1 = EX(sc[13]);
          e2 = EX(sc[14]); e3 = EX(sc[15]); PIN4(e0, e1, e2, e3); SBAR();
          if (!last) sc = sn;
        }
      }
      p1.z = cvtpk(e0, e1); p1.w = cvtpk(e2, e3);
      o[3] = MFMA(frag(vf[15 % KV_DEPTH][1]), frag(p1), o[3]);
      MPIN4(o[0], o[1], o[2], o[3]);
      TS(4);
      u32 lv;
      asm volatile("v_mbcnt_lo_u32_b32 %0, -1, 0\n\tv_mbcnt_hi_u32_b32 %0, -1, %0\n\tv_lshlrev_b32 %0, 4, %0" : "=v"(lv));
      if (attempt == 0) {
        bool bad = false;
#pragma unroll
        for (int t = 0; t < 4; ++t) bad |= !(o[t][12] >= 0x1p-80f && o[t][12] <= 0x1p100f);
        if (__any(bad && lv >= 512)) { if (lv == 0) *(LAS u32*)(size_t)(L_MISC + 4 * C_FB) = 1u; }
        await(C_HD, 4);
      }
#pragma unroll
      for (int t = 1; t < 4; ++t) {
#pragma unroll
        for (int g = 0; g < 4; ++g) ldsw128(L_C + w * 12288 + (t - 1) * 4096 + g * 1024 + lv, f4(o[t], g));
      }
      arrive(C_O, lv);
      await(C_O, 4 * (attempt + 1));
      TS(10);
      const u32 fb = attempt == 0 ? ldsu(L_MISC + 4 * C_FB) : 0u;
      if (!fb) break;
      {
        float mx[4];
#pragma unroll
        for (int t = 0; t < 4; ++t) { mx[t] = -3.0e38f; if (lv >= 512) { qf[t][1].z = 0; qf[t][1].w = 0; } }
#pragma unroll 1
        for (int it = 0; it < 16; ++it) {
          const u32x4 k0 = __builtin_amdgcn_raw_buffer_load_b128(rs_kv, lv, it << 12, KV_AUX), k1 = __builtin_amdgcn_raw_buffer_load_b128(rs_kv, lv + 1024, it << 12, KV_AUX);
#pragma unroll
          for (int t = 0; t < 4; ++t) {
            f32x16 s = MFMA(frag(k0), frag(qf[t][0]), zero);
            s = MFMA(frag(k1), frag(qf[t][1]), s);
#pragma unroll
            for (int i = 0; i < 16; ++i) mx[t] = fmaxf(mx[t], s[i]);
          }
        }
        const u32 ma = L_C + 49152;
#pragma unroll
        for (int t = 0; t < 4; ++t) { mx[t] = fmaxf(mx[t], swap32(mx[t], lv >= 512)); *(LAS float*)(size_t)(ma + (w * 4 + ((w + t) & 3)) * 256 + (lv >> 2)) = mx[t]; }
        arrive(C_MAX, lv);
        await(C_MAX, 4);
#pragma unroll
        for (int t = 0; t < 4; ++t) {
          float m = -3.0e38f;
#pragma unroll
          for (int ww = 0; ww < 4; ++ww) m = fmaxf(m, *(LAS float*)(size_t)(ma + (ww * 4 + ((w + t) & 3)) * 256 + (lv >> 2)));
          set_pad(qf[t][1], m, lv >= 512);
        }
      }
    }
    {
    int lane;
    asm volatile("v_mbcnt_lo_u32_b32 %0, -1, 0\n\tv_mbcnt_hi_u32_b32 %0, -1, %0" : "=v"(lane));
    const int r = lane & 31, h = lane >> 5, g1 = (lane >> 4) & 1, q4 = (lane >> 2) & 3, p4 = lane & 3;
    const u32 vo = lane * 16;
    {
      f32x16 own = o[0];
#pragma unroll
      for (int pp = 1; pp < 4; ++pp) {
        const u32 a = L_C + ((w + pp) & 3) * 12288 + (3 - pp) * 4096 + vo;
#pragma unroll
        for (int g = 0; g < 4; ++g) add4(own, g, ldsr128(a + g * 1024));
      }
      const float other = swap32(own[12], h);
      const float inv = __builtin_amdgcn_rcpf(h ? own[12] : other);
      u32x4 f0 = {cvtpk(own[0] * inv, own[1] * inv), cvtpk(own[2] * inv, own[3] * inv), cvtpk(own[4] * inv, own[5] * inv), cvtpk(own[6] * inv, own[7] * inv)};
      u32x4 f1 = {cvtpk(own[8] * inv, own[9] * inv), cvtpk(own[10] * inv, own[11] * inv), cvtpk(own[12] * inv, own[13] * inv), cvtpk(own[14] * inv, own[15] * inv)};
      if (h) { f1.z = 0; f1.w = 0; }
      const u32 so = WS_OF + ((tb * 2 + (w & 1)) * 16 + 2 * sl + (w >> 1)) * 2048 + vo;
      __builtin_amdgcn_raw_buffer_store_b128(f0, rs_ws, so, 0, 16);
      __builtin_amdgcn_raw_buffer_store_b128(f1, rs_ws, so + 1024, 0, 16);
    }
    {
      const int tm = w & 1, kh = w >> 1;
      TS(11);
      VMCNT(0);
      if (lane == 0) gstore_flag(ws, WS_OFLAG + (tb * 32 + sl * 4 + w) * 128, tag);
      TS(5);
      if (sl == 7) { TSW(); return; }
      await(C_WO, 4);
      TS(15);
      u32x4 wf[16][2];
      const u32 wa = 2 * kh * L_PRIV + L_WO_OFF + (4 * h + q4) * 128 + g1 * 32 + 8 * p4;
      const u32 wa0 = wa + 64 * kh, wa1 = wa + 64 * (1 - kh);
      const u32 hz = h ? 0 : 1024;
#pragma unroll
      for (int i = 0; i < 16; ++i) {
        const u32 a = (i >> 3) * L_PRIV + (28 * ((i >> 1) & 3) + 16 * (i & 1)) * 128;
        if ((i & 7) == 7) { wf[i][0] = trfrag(wa0 + a, wa0 + hz + a); wf[i][1] = trfrag(wa1 + a, wa1 + hz + a); }
        else { wf[i][0] = trfrag(wa0 + a, wa0 + a + 1024); wf[i][1] = trfrag(wa1 + a, wa1 + a + 1024); }
      }
#pragma unroll
      for (int i = 0; i < 16; ++i) PIN2(wf[i][0], wf[i][1]);
      if (w == 0) { poll_words(ws, WS_OFLAG + tb * 4096, 32, tag, lane); arrive(C_OF, lane); }
      else await(C_OF, 1);
      TS(6);
      u32x4 of[16];
      const u32 so = WS_OF + ((tb * 2 + tm) * 16 + 8 * kh) * 2048;
#pragma unroll
      for (int i = 0; i < 16; ++i) of[i] = __builtin_amdgcn_raw_buffer_load_b128(rs_ws, vo + (i & 3) * 1024, so + (i >> 2) * 4096, H_AUX);
      SBAR();
      f32x16 a0 = {}, a1 = {};
#pragma unroll
      for (int i = 0; i < 16; ++i) {
        a0 = MFMA(frag(of[i]), frag(wf[i][0]), a0);
        a1 = MFMA(frag(of[i]), frag(wf[i][1]), a1);
      }
      MPIN2(a0, a1);
      TS(12);
      const f32x16 snd = a1;
      f32x16 own = a0;
      const u32 aw = L_C + w * 4096 + vo, ar = L_C + (w ^ 2) * 4096 + vo;
#pragma unroll
      for (int g = 0; g < 4; ++g) ldsw128(aw + g * 1024, f4(snd, g));
      arrive(C_P, lane);
      await(C_P, 4);
#pragma unroll
      for (int g = 0; g < 4; ++g) add4(own, g, ldsr128(ar + g * 1024));
      {
        const u32 ov = ((tb * 64 + 32 * tm + 4 * h) * 896 + 448 + 64 * sl + 32 * kh + r) * 4;
#pragma unroll
        for (int i = 0; i < 16; ++i) __builtin_amdgcn_raw_buffer_store_b32(__float_as_uint(rbf(own[i])), rs_out, ov, ((i & 3) + 8 * (i >> 2)) * 3584, OUT_AUX);
      }
    }
    }
    TS(7);
    TSW();
  } else {
    if (sl == 7) __builtin_amdgcn_s_setprio(AUX_PRIO);
    const int q = wv - 4;
    const u32 PA = q * L_PRIV;
    const int part = q >> 1, half = q & 1;
    if (sl == 7 && lane < 56) {
      const char* src = (const char*)((q >> 1) ? P.wv : P.wk) + (q & 1) * 14 * 896;
#pragma unroll
      for (int j = 0; j < 14; ++j) {
        int i = j + 4 * tl; i = i >= 14 ? i - 14 : i;
        glds16(src + i * 896, vo, L_C + (q * 14 + i) * 896);
      }
    }
    asm volatile("s_waitcnt vmcnt(0) lgkmcnt(0)\n\ts_barrier" ::: "memory");
    const u32 tag = ldsu(L_MISC + 4 * C_EPOCH) + 1u;
    TS(13);
    if (sl == 7) {
      const int kind = q >> 1, ktm = q & 1;
      const u32 wa = L_C + kind * 25088 + (8 * h + q4) * 56 + g1 * 32 + 8 * p4;
      f32x16 acc;
      if (kind == 0) acc = proj_tile<true, 56, false>(32 * ktm + r, h, wa, lane, []() {});
      else acc = proj_tile<false, 56, false>(32 * ktm + r, h, wa, lane, []() {});
      u32x4 f0 = {cvtpk(acc[0], acc[1]), cvtpk(acc[2], acc[3]), cvtpk(acc[4], acc[5]), cvtpk(acc[6], acc[7])};
      u32x4 f1 = {cvtpk(acc[8], acc[9]), cvtpk(acc[10], acc[11]), cvtpk(acc[12], acc[13]), cvtpk(acc[14], acc[15])};
      if (kind == 0) {
        if (h) { f1.z = 0x3F803F80u; f1.w = 0x00003F80u; }
      } else if (r >= 28) {
        const u32 c = r == 28 ? 0x3F803F80u : 0u;
        f0 = (u32x4){c, c, c, c}; f1 = f0;
      }
      const u32 so = WS_KVF + (tb * 2 + ktm) * 4096 + kind * 2048 + vo;
      __builtin_amdgcn_raw_buffer_store_b128(f0, rs_ws, so, 0, 16);
      __builtin_amdgcn_raw_buffer_store_b128(f1, rs_ws, so + 1024, 0, 16);
      SBAR();
      TS(12);
      if (kind == 0) {
        float n2 = 0.f;
#pragma unroll
        for (int i = 0; i < 16; ++i) { if (i < 12) n2 = fmaf(acc[i], acc[i], n2); else n2 = h ? n2 : fmaf(acc[i], acc[i], n2); }
        n2 += swap32(n2, h);
        n2 = wave_max_nonneg(n2);
        if (lane == 0) asm volatile("ds_max_u32 %0, %1" :: "v"(L_MISC + 4 * C_KMAX), "v"(n2) : "memory");
      }
      VMCNT(0);
      u32 old = 0;
      if (lane == 0) asm volatile("s_waitcnt lgkmcnt(0)\n\tds_add_rtn_u32 %0, %1, %2\n\ts_waitcnt lgkmcnt(0)" : "=&v"(old) : "v"(L_MISC + 4 * C_KVP), "v"(1u) : "memory");
      if (__builtin_amdgcn_readfirstlane(old) == 3) {
        const u32 km = ldsu(L_MISC + 4 * C_KMAX);
        if (lane < 8) __hip_atomic_store((GAS u64*)(ws + WS_KVFLAG + lane * 4096 + tb * 128), (u64)tag | ((u64)km << 32), __ATOMIC_RELAXED, __HIP_MEMORY_SCOPE_AGENT);
      }
      TS(14);
      arrive(C_KVD, lane);
      await(C_KVD, 4);
    } else if (PREF) {
      const int j = (b >> 3) - tl;
      const char* p0 = (const char*)(q == 0 ? P.Bre : q == 1 ? P.Bim : q == 2 ? P.Cre : P.Cim) + j * 14336;
      const u32 o2 = lane < 48 ? 8192 + lane * 128 : 0;
      glds4(p0, lane * 128, L_AGG + 512);
      glds4(p0, o2, L_AGG + 512);
      if (q == 0) glds4((const char*)P.Ax + (size_t)(b & 7) * 229376 + j * 8192, lane * 128, L_AGG + 512);
      if (q == 1) glds4((const char*)P.wo + j * 14336, lane * 128, L_AGG + 512);
      if (q == 2) glds4((const char*)P.wo + j * 14336, o2, L_AGG + 512);
    }

    u32 cva;
    {
      const int hi = lane >= 28, cs = lane - 28 * hi;
      cva = hi * 896 + (cs << 4);
    }
    auto stage_C4 = [&](int qq, int g) {
      const char* csrc = (const char*)((qq >> 1) ? P.Cim : P.Cre) + (size_t)sl * 56 * 896 + (qq & 1) * 448;
      int p0 = 4 * g + 8 * tl; p0 = p0 >= 28 ? p0 - 28 : p0;
      const u32 va = cva ^ ((p0 & 4) << 3), vb = va ^ 16;
      if (lane < 56) {
#pragma unroll
        for (int j = 0; j < 4; ++j) { glds16(csrc + (p0 + j) * 1792, j < 2 ? va : vb, qq * L_PRIV + (p0 + j) * 896); VMTHR(); }
      }
    };
    const int tm = (q ^ sl) & 1, tn = q >> 1;
    const bool valid = 32 * tn + r < 56;
    const int dcol = 56 * sl + (valid ? 32 * tn + r : 55), t0 = tb * 64 + 32 * tm + 4 * h;
    if (WARM && q == 3) {
      const int k = lane & 7;
      const u32 o1 = k < 4 ? WS_KVF + k * 65536 : k < 6 ? WS_OF + tb * 65536 + (k - 4) * 65024 : k == 6 ? WS_HFLAG : WS_AGG + (((tb & 1) * 7 + sl) & 15) * 8192;
      const u32 o2 = WS_HF + (tb & ~1) * 114688 + (k & 3) * 65536;
      if (lane < 16) {
        asm volatile("s_mov_b32 m0, %2\n\ts_nop 0\n\tglobal_load_lds_dword %0, %1 sc1" :: "v"(lane < 8 ? o1 : o2), "s"(ws), "s"(L_AGG + 512) : "memory");
        glds4((const char*)P.out + (size_t)tb * 229376, (lane & 3) * 65536, L_AGG + 512);
      }
    }
    if (NOSSM == 1) {
      arrive(C_HD, lane);
      await(C_KVL, 4);
      if (sl != 7) {
        const char* src = (const char*)P.wo + sl * 128 + q * 112 * 896;
        const u32 wv8 = (lane >> 3) * 896 + (lane & 7) * 16;
#pragma unroll
        for (int j = 0; j < 14; ++j) { glds16(src + j * 7168, wv8, PA + L_WO_OFF + 1024 * j); VMTHR(); }
      }
      VMCNT(0);
      arrive(C_WO, lane);
      TSW();
      if (tb == 31 && sl == 0 && q == 0) { for (int i = 0; i < 300; ++i) __builtin_amdgcn_s_sleep(1); if (lane == 0) gstore_flag(ws, WS_EPOCH, tag); }
      return;
    }
    u32x4 cf[2][14];
    auto read_C = [&]() {
#pragma unroll
      for (int t = 0; t < 2; ++t) {
        int row = 32 * (t ^ part) + r; row = row > 55 ? 55 : row;
        const int x = (row >> 2) & 3;
        const u32 a0 = PA + row * 448 + ((h ^ x) << 4), a1 = PA + row * 448 + (((2 + h) ^ x) << 4);
#pragma unroll
        for (int j = 0; j < 14; ++j) cf[t][j] = ldsr128(((j & 1) ? a1 : a0) + 64 * (j >> 1));
      }
#pragma unroll
      for (int j = 0; j < 14; ++j) PIN2(cf[0][j], cf[1][j]);
      LGKM0();
    };
    if (sl == 7) {
      arrive(C_HD, lane);
      await(C_KVL, 4);
      if (PF7) {
        const char* cp = (const char*)(part ? P.Cim : P.Cre) + (tl * 2 + half) * 43904;
#pragma unroll 1
        for (int i = 0; i < 43; ++i) { glds16(cp + (i < 42 ? i * 1024 : 42880), vo, PA + (i & 7) * 1024); VMTHR(); }
        const char* wp = (const char*)P.wo + (tl * 4 + q) * 25088;
#pragma unroll 1
        for (int i = 0; i < 25; ++i) { glds16(wp + (i < 24 ? i * 1024 : 24064), vo, PA + (i & 7) * 1024); VMTHR(); }
        VMCNT(0);
      }
      TS(9);
#pragma unroll 1
      for (int g = 0; g < 7; ++g) stage_C4(q, g);
      VMCNT(0);
      read_C();
    } else {
      const int tb2 = tb >> 1, cg = (tb & 1) * 7 + sl, ch = 32 * cg + r;
      const u32 vo1 = (lane ^ 1) * 16;
      float lr, li, cr, ci;
      if (lane < 54) {
        const char* bsrc = (const char*)(part ? P.Bim : P.Bre) + (size_t)(cg * 32 + half * 16) * 896;
        const u32 va = (tb2 & 1) ? vo1 : vo, vb = (tb2 & 1) ? vo : vo1;
#pragma unroll
        for (int j = 0; j < 16; ++j) {
          const int i = j ^ (8 * (tb2 & 1));
          glds16(bsrc + i * 896, j < 8 ? va : vb, L_C + (q * 16 + i) * 864);
        }
      }
      u32 vlre = P.lre[ch], vlim = P.lim[ch], vldt = P.ldt[ch];
      u32x4 blast_re = *(const u32x4*)(P.Bre + (size_t)ch * 448 + 432 + 8 * h);
      u32x4 blast_im = *(const u32x4*)(P.Bim + (size_t)ch * 448 + 432 + 8 * h);
      asm volatile("" : "+v"(vlre), "+v"(vlim), "+v"(vldt), "+v"(blast_re), "+v"(blast_im) :: "memory");
      {
        const float dt = rbf(expf(__uint_as_float(vldt << 16))), a = __uint_as_float(vlre << 16), bb = __uint_as_float(vlim << 16);
        const float er = expf(dt * a);
        float sn, cs;
        sincosf(dt * bb, &sn, &cs);
        lr = er * cs; PIN(lr); li = er * sn;
        const float nr = lr - 1.f, ni = li, den = 2.0f / (a * a + bb * bb);
        cr = (nr * a + ni * bb) * den; PIN(cr); ci = (ni * a - nr * bb) * den;
      }
      float l16r = lr, l16i = li;
#pragma unroll
      for (int i = 0; i < 4; ++i) cmul(l16r, l16i, l16r, l16i, l16r, l16i);
      float l32r, l32i, l128r, l128i;
      cmul(l16r, l16i, l16r, l16i, l32r, l32i);
      cmul(l32r, l32i, l32r, l32i, l128r, l128i);
      cmul(l128r, l128i, l128r, l128i, l128r, l128i);
      PIN4(lr, li, cr, ci); PIN4(l16r, l16i, l32r, l32i); PIN2(l128r, l128i);
      const char* xsrc = (const char*)P.Ax + (size_t)(tb2 * 128 + q * 32) * 896;
      const u32 xv0 = (lane >> 3) * 896 + (((lane & 7) ^ (lane >> 4)) << 4), xv1 = xv0 ^ 64;
      const int rho = 16 * ((r >> 2) & 1) + (r & 3) + 4 * (r >> 3), sw = (rho >> 1) & 7;
      u32 xa[4];
#pragma unroll
      for (int bb = 0; bb < 4; ++bb) xa[bb] = PA + rho * 128 + (((2 * bb + h) ^ sw) << 4);
      const int xrot = cg % 6;
      u32 ba[7];
#pragma unroll
      for (int c = 0; c < 7; ++c) {
        int kc = c + xrot; kc = c == 6 ? 6 : kc >= 6 ? kc - 6 : kc;
        ba[c] = L_C + r * 864 + ((h ^ ((r >> 3) & 1)) << 4) + 128 * kc;
        PIN(ba[c]);
      }
      PIN4(xa[0], xa[1], xa[2], xa[3]);
      arrive(C_BFULL, lane);
      TS(1);
      await(C_QDONE, 4);
      auto xdma = [&](int c) {
#pragma unroll
        for (int j = 0; j < 4; ++j) {
          int kc = c + xrot; kc = c == 6 ? 6 : kc >= 6 ? kc - 6 : kc;
          glds16(xsrc + kc * 128 + j * 7168, (j & 1) ? xv1 : xv0, PA + (c % 6) * 4096 + j * 1024);
        }
      };
      xdma(0);
      if (XG != 1) xdma(1);
      await(C_BFULL, 4);
      TS(2);
      f32x16 ur = {}, ui = {};
      PIN2(ur, ui);
      __builtin_amdgcn_s_setprio(G1_PRIO);
      {
        u32x4 xf[28], br[28], bi[28];
#pragma unroll
        for (int ks = 0; ks < 28 + 4; ++ks) {
          if (ks >= 4) {
            const int k = ks - 4;
            ur = MFMA(frag(xf[k]), frag(br[k]), ur);
            ui = MFMA(frag(xf[k]), frag(bi[k]), ui);
            SBAR();
          }
          if (ks < 28) {
            if ((ks & 3) == 0) {
              const int c = ks >> 2;
              if (c + 1 <= 6 && c + 1 != XG) vmwait<4>(); else vmwait<0>();
              if (c + 1 == XG) { asm volatile("s_nop 7\n\ts_nop 7" ::: "memory"); await(C_KVL, 4); xdma(c + 1); }
              if (c + 2 <= 6 && c + 2 != XG) xdma(c + 2);
            }
            xf[ks] = ldsr128(xa[ks & 3] + ((ks >> 2) % 6) * 4096);
            br[ks] = ks < 27 ? ldsr128(ba[ks >> 2] + 32 * (ks & 3)) : blast_re;
            bi[ks] = ks < 27 ? ldsr128(ba[ks >> 2] + 27648 + 32 * (ks & 3)) : blast_im;
            SBAR();
          }
        }
      }
      MPIN2(ur, ui);
      __builtin_amdgcn_s_setprio(AUX_PRIO);
      TS(3);
      float ar = 0.f, ai = 0.f;
#pragma unroll
      for (int i = 0; i < 16; ++i) {
        float tr, ti;
        cmul(cr, ci, ur[i], ui[i], tr, ti);
        ur[i] = tr; ui[i] = ti;
        cstep(lr, li, ar, ai, tr, ti);
      }
      const float or_ = swap32(ar, h), oi_ = swap32(ai, h);
      const float lor = h ? or_ : ar, loi = h ? oi_ : ai;
      float a32r = lor, a32i = loi;
      cstep(l16r, l16i, a32r, a32i, h ? ar : or_, h ? ai : oi_);
      if (q < 3 && h == 0) *(LAS f32x2*)(size_t)(L_AGG + q * 256 + r * 8) = (f32x2){a32r, a32i};
      arrive(C_AGG, lane);
      await(C_AGG, 4);
      f32x2 wagg[3];
#pragma unroll
      for (int j = 0; j < 3; ++j) wagg[j] = *(LAS f32x2*)(size_t)(L_AGG + j * 256 + r * 8);
      const u32 aggb = WS_AGG + cg * 8192;
      if (q == 3 && tb2 < 15) {
        float tr = wagg[0].x, ti = wagg[0].y;
        cstep(l32r, l32i, tr, ti, wagg[1].x, wagg[1].y);
        cstep(l32r, l32i, tr, ti, wagg[2].x, wagg[2].y);
        cstep(l32r, l32i, tr, ti, a32r, a32i);
        __hip_atomic_store((GAS u64*)(ws + aggb + tb2 * 512 + r * 16 + h * 8), (u64)__float_as_uint(h ? ti : tr) | ((u64)tag << 32), __ATOMIC_RELAXED, __HIP_MEMORY_SCOPE_AGENT);
      }
      TS(4);
      float hr = 0.f, hi = 0.f;
      if (q < 3) {
#pragma unroll 1
        for (int g = q; g < 7; g += 3) stage_C4(3, g);
        stage_C4(q, 0); stage_C4(q, 1);
        vmwait<8>();
        arrive(C_C3, lane);
#pragma unroll 1
        for (int g = 2; g < CEARLY; ++g) stage_C4(q, g);
        TS(10);
        await(C_CARRY, 1);
        const f32x2 cv = *(LAS f32x2*)(size_t)(L_CARRY + r * 8);
        hr = cv.x; hi = cv.y;
      } else {
        if (tb2 > 0) {
          u32x4 g[8];
          u32 gv[8];
#pragma unroll
          for (int j = 0; j < 8; ++j) gv[j] = r * 16 + ((8 * h + j < tb2) ? (8 * h + j) * 512 : 0);
#pragma unroll 1
          for (u32 spin = 0; spin < (1u << 17); ++spin) {
            asm volatile("" ::: "memory");
            u32 bad = 0;
#pragma unroll
            for (int j = 0; j < 8; ++j) g[j] = __builtin_amdgcn_raw_buffer_load_b128(rs_ws, gv[j], aggb, 16);
#pragma unroll
            for (int j = 0; j < 8; ++j) bad |= (g[j].y ^ tag) | (g[j].w ^ tag);
            if (__all(bad == 0)) break;
            __builtin_amdgcn_s_sleep(4);
          }
          asm volatile("" ::: "memory");
#pragma unroll
          for (int j = 0; j < 8; ++j)
            if (j < tb2) cstep(l128r, l128i, hr, hi, __uint_as_float(g[j].x), __uint_as_float(g[j].z));
          const float sr = swap32(hr, h), si = swap32(hi, h);
          if (h) { hr = sr; hi = si; }
          if (tb2 > 8) {
#pragma unroll
            for (int j = 0; j < 7; ++j)
              if (8 + j < tb2) cstep(l128r, l128i, hr, hi, __uint_as_float(g[j].x), __uint_as_float(g[j].z));
            const float tr = swap32(hr, h), ti = swap32(hi, h);
            if (!h) { hr = tr; hi = ti; }
          }
        }
        if (h == 0) *(LAS f32x2*)(size_t)(L_CARRY + r * 8) = (f32x2){hr, hi};
        arrive(C_CARRY, lane);
        TS(10);
      }
#pragma unroll
      for (int j = 0; j < 3; ++j)
        if (j < q) cstep(l32r, l32i, hr, hi, wagg[j].x, wagg[j].y);
      if (h) cstep(l16r, l16i, hr, hi, lor, loi);
      TS(5);
      u32 pr[8], pi[8];
#pragma unroll
      for (int i = 0; i < 16; i += 2) {
        cstep(lr, li, hr, hi, ur[i], ui[i]);
        const float r0 = hr, i0 = hi;
        cstep(lr, li, hr, hi, ur[i + 1], ui[i + 1]);
        pr[i >> 1] = cvtpk(r0, hr); pi[i >> 1] = cvtpk(-i0, -hi);
      }
      const u32 ta = L_C + q * 5120 + r * 80 + 32 * h;
      ldsw128(ta, (u32x4){pr[0], pr[1], pr[2], pr[3]}); ldsw128(ta + 16, (u32x4){pr[4], pr[5], pr[6], pr[7]});
      ldsw128(ta + 2560, (u32x4){pi[0], pi[1], pi[2], pi[3]}); ldsw128(ta + 2560 + 16, (u32x4){pi[4], pi[5], pi[6], pi[7]});
      LGKM0();
      const u32 tr = L_C + q * 5120 + (8 * h + q4) * 80 + g1 * 32 + 8 * p4;
      const int tbh = 2 * tb2 + (q >> 1);
      const u32 so = WS_HF + ((tbh * 2 + (q & 1)) * 56 + 2 * cg) * 1024 + vo;
#pragma unroll
      for (int pt = 0; pt < 2; ++pt)
#pragma unroll
        for (int s = 0; s < 2; ++s) {
          const u32 a = tr + pt * 2560 + s * 1280;
          __builtin_amdgcn_raw_buffer_store_b128(trfrag(a, a + 320), rs_ws, so + pt * 28 * 1024 + s * 1024, 0, 16);
        }
      arrive(C_HD, lane);
      VMCNT(0);
      if (lane == 0) gstore_flag(ws, WS_HFLAG + (tbh * 32 + cg * 2 + (q & 1)) * 128, tag);
      TS(6);
      if (q < 3) {
#pragma unroll 1
        for (int g = CEARLY; g < 7; ++g) stage_C4(q, g);
        VMCNT(0);
      } else {
        await(C_C3, 3);
      }
      read_C();
      TS(9);
      const char* src = (const char*)P.wo + sl * 128 + q * 112 * 896;
      const u32 wv8 = (lane >> 3) * 896 + (lane & 7) * 16;
#pragma unroll
      for (int j = 0; j < 14; ++j) {
        int i = j + 4 * tl; i = i >= 14 ? i - 14 : i;
        glds16(src + i * 7168, wv8, PA + L_WO_OFF + 1024 * i); VMTHR();
      }
      VMCNT(0);
      arrive(C_WO, lane);
      TS(11);
    }
    if (NOSSM == 2) { TSW(); if (tb == 31 && sl == 0 && q == 0) { if (lane == 0) gstore_flag(ws, WS_EPOCH, tag); } return; }
    if (q == 0) { poll_words(ws, WS_HFLAG + tb * 4096, 28, tag, lane); arrive(C_HF, lane); }
    else await(C_HF, 1);
    TS(7);
    {
      f32x16 y[4];
      const u32 so = WS_HF + ((tb * 2 + tm) * 56 + part * 28 + half * 14) * 1024, so1 = WS_HF + ((tb * 2 + (tm ^ 1)) * 56 + part * 28 + half * 14) * 1024;
      u32x4 hf[28];
#define LDH(j) __builtin_amdgcn_raw_buffer_load_b128(rs_ws, vo + (((j) % 14) & 3) * 1024, ((j) < 14 ? so : so1) + (((j) % 14) >> 2) * 4096, 16)
#pragma unroll
      for (int j = 0; j < HWIN; ++j) hf[j] = LDH(j);
      SBAR();
      const f32x16 zero = {};
      __builtin_amdgcn_s_setprio(AUX2_PRIO);
#pragma unroll
      for (int j = 0; j < 28; ++j) {
        const int t = j / 14, k = j % 14;
        y[t] = MFMA(frag(hf[j]), frag(cf[0][k]), k == 0 ? zero : y[t]);
        y[2 + t] = MFMA(frag(hf[j]), frag(cf[1][k]), k == 0 ? zero : y[2 + t]);
        SBAR();
        if (j + HWIN < 28) { hf[j + HWIN] = LDH(j + HWIN); SBAR(); }
      }
      u16 xs[16];
      {
        const auto rs_ax = __builtin_amdgcn_make_buffer_rsrc((void*)P.Ax, 0, 2048 * 896, 0x00020000);
        const u32 xv = (t0 * 448 + dcol) * 2;
#pragma unroll
        for (int i = 0; i < 16; ++i) xs[i] = __builtin_amdgcn_raw_buffer_load_b16(rs_ax, xv + (i & 3) * 896, (i >> 2) * 7168, 0);
      }
      const u16 dsk16 = P.Dsk[dcol];
      MPIN4(y[0], y[1], y[2], y[3]);
      __builtin_amdgcn_s_setprio(AUX3_PRIO);
#pragma unroll
      for (int t = 1; t < 4; ++t) {
#pragma unroll
        for (int g = 0; g < 4; ++g) ldsw128(PA + (t - 1) * 4096 + g * 1024 + vo, f4(y[t], g));
      }
      arrive(C_Y, lane);
      await(C_Y, 4);
      f32x16 own = y[0];
#pragma unroll
      for (int t = 1; t < 4; ++t) {
#pragma unroll
        for (int g = 0; g < 4; ++g) add4(own, g, ldsr128((q ^ t) * L_PRIV + (t - 1) * 4096 + g * 1024 + vo));
      }
      if (valid) {
        const u32 ov = (t0 * 896 + dcol) * 4;
#pragma unroll
        for (int i = 0; i < 16; ++i) {
          float sk = bf2f(xs[i]) * bf2f(dsk16);
          PIN(sk);
          __builtin_amdgcn_raw_buffer_store_b32(__float_as_uint(own[i] + rbf(sk)), rs_out, ov, ((i & 3) + 8 * (i >> 2)) * 3584, OUT_AUX);
        }
      }
    }
    TS(8);
    TSW();
    if (tb == 31 && sl == 0 && q == 0) {
      await(C_KV, 1);
      if (lane == 0) gstore_flag(ws, WS_EPOCH, tag);
    }
  }
}

extern "C" void kernel_launch(void* const* d_in, const int* in_sizes, int n_in, void* d_out, int out_size, void* d_ws, size_t ws_size,
                              hipStream_t stream) {
  Params P;
  P.Ax = (const u16*)d_in[0]; P.lre = (const u16*)d_in[1]; P.lim = (const u16*)d_in[2]; P.Bre = (const u16*)d_in[3];
  P.Bim = (const u16*)d_in[4]; P.Cre = (const u16*)d_in[5]; P.Cim = (const u16*)d_in[6]; P.ldt = (const u16*)d_in[7];
  P.Dsk = (const u16*)d_in[8]; P.Bx = (const u16*)d_in[9]; P.wq = (const u16*)d_in[10]; P.wk = (const u16*)d_in[11];
  P.wv = (const u16*)d_in[12]; P.wo = (const u16*)d_in[13];
  P.out = (float*)d_out; P.ws = (char*)d_ws;
  (void)hipFuncSetAttribute((const void*)fused_kernel, hipFuncAttributeMaxDynamicSharedMemorySize, LDS_BYTES);
  fused_kernel<<<256, 512, LDS_BYTES, stream>>>(P);
}
```

```cpp
#include <hip/hip_runtime.h>

typedef unsigned short u16;
typedef unsigned int u32;
typedef unsigned long long u64;
using bf16x8 = __attribute__((ext_vector_type(8))) short;
using f32x16 = __attribute__((ext_vector_type(16))) float;
using u32x4 = __attribute__((ext_vector_type(4))) unsigned;
using u32x2 = __attribute__((ext_vector_type(2))) unsigned;
typedef short v4i16_t __attribute__((ext_vector_type(4)));
typedef __bf16 bf16x2_t __attribute__((ext_vector_type(2)));
typedef float f32x2 __attribute__((ext_vector_type(2)));
#define LAS __attribute__((address_space(3)))
#define GAS __attribute__((address_space(1)))
#define DI __device__ __forceinline__
#define MFMA(a, b, c) __builtin_amdgcn_mfma_f32_32x32x16_bf16((a), (b), (c), 0, 0, 0)
#define SBAR() __builtin_amdgcn_sched_barrier(0)
#define PIN(x) asm volatile("" : "+v"(x))
#define PIN2(a, b) asm volatile("" : "+v"(a), "+v"(b))
#define PIN4(a, b, c, d) asm volatile("" : "+v"(a), "+v"(b), "+v"(c), "+v"(d))
#define MPIN(x) asm volatile("s_nop 7\n\ts_nop 7" : "+v"(x))
#define MPIN2(a, b) asm volatile("s_nop 7\n\ts_nop 7" : "+v"(a), "+v"(b))
#define MPIN4(a, b, c, d) asm volatile("s_nop 7\n\ts_nop 7" : "+v"(a), "+v"(b), "+v"(c), "+v"(d))
#define VMCNT(n) asm volatile("s_waitcnt vmcnt(" #n ")" ::: "memory")
#define LGKM0() asm volatile("s_waitcnt lgkmcnt(0)" ::: "memory")
#define EX(x) __builtin_amdgcn_exp2f(x)

constexpr int SC1 = 16;
constexpr int NT = 2;
constexpr int KV_DEPTH = 4;
constexpr int HWIN = 8;

constexpr u32 WS_EPOCH = 0;
constexpr u32 WS_HFLAG = 7u << 20;
constexpr u32 WS_OFLAG = WS_HFLAG + 131072;
constexpr u32 WS_KVFLAG = WS_OFLAG + 131072;
constexpr u32 WS_AGG = 32768;
constexpr u32 WS_KVF = WS_AGG + 131072;
constexpr u32 WS_OF = WS_KVF + 262144;
constexpr u32 WS_HF = WS_OF + 2097152;
constexpr u32 WS_END = WS_KVFLAG + 32768;

constexpr u32 L_XB = 0;
constexpr u32 L_WQ = 57344;
constexpr u32 L_C = 107520;
constexpr u32 L_MISC = L_C + 55296;
constexpr u32 L_AGG = L_MISC + 128;
constexpr u32 L_KN = L_MISC + 896;
constexpr u32 L_CARRY = L_C + 20480;
constexpr u32 L_PRIV = 26880;
constexpr u32 L_WO_OFF = 12288;
constexpr u32 L_QX = L_WQ;
constexpr u32 LDS_BYTES = 163840;
enum { C_WQ = 0, C_Q2, C_QDONE, C_BFULL, C_AGG, C_WO, C_O, C_MAX, C_P, C_Y, C_FB, C_KVP, C_KMAX, C_KV, C_OF, C_HF, C_WQ2, C_HD, C_KVL, C_KVD, C_C3, C_CARRY, C_EPOCH = 31 };

struct Params {
  const u16 *Ax, *lre, *lim, *Bre, *Bim, *Cre, *Cim, *ldt, *Dsk, *Bx, *wq, *wk, *wv, *wo;
  float* out;
  char* ws;
};

DI float bf2f(u16 v) { return __uint_as_float(((u32)v) << 16); }
DI u32 cvtpk(float a, float b) { return __builtin_bit_cast(u32, __builtin_convertvector((f32x2){a, b}, bf16x2_t)); }
DI float rbf(float a) { return __uint_as_float(cvtpk(a, 0.f) << 16); }
DI bf16x8 frag(u32x4 v) { return __builtin_bit_cast(bf16x8, v); }
DI void glds16(const void* sbase, u32 voff, u32 lds_base) {
  asm volatile("s_mov_b32 m0, %2\n\ts_nop 0\n\tglobal_load_lds_dwordx4 %0, %1" :: "v"(voff), "s"(sbase), "s"(lds_base) : "memory");
}
DI u32x4 ldsr128(u32 a) { return *(const LAS u32x4*)(size_t)a; }
DI void ldsw128(u32 a, u32x4 v) { *(LAS u32x4*)(size_t)a = v; }
DI u32 ldsu(u32 a) { return (u32)__builtin_amdgcn_readfirstlane(*(volatile LAS u32*)(size_t)a); }
DI u32x2 ldstr(u32 a) { return __builtin_bit_cast(u32x2, __builtin_amdgcn_ds_read_tr16_b64_v4i16((LAS v4i16_t*)(size_t)a)); }
DI u32x4 trfrag(u32 a0, u32 a1) { u32x2 lo = ldstr(a0), hi = ldstr(a1); return (u32x4){lo.x, lo.y, hi.x, hi.y}; }
DI u32x4 f4(const f32x16& v, int g) {
  return (u32x4){__float_as_uint(v[4 * g]), __float_as_uint(v[4 * g + 1]), __float_as_uint(v[4 * g + 2]), __float_as_uint(v[4 * g + 3])};
}
DI void add4(f32x16& v, int g, u32x4 a) {
  v[4 * g] += __uint_as_float(a.x); v[4 * g + 1] += __uint_as_float(a.y); v[4 * g + 2] += __uint_as_float(a.z); v[4 * g + 3] += __uint_as_float(a.w);
}
DI void arrive(int c, int lane) {
  asm volatile("s_waitcnt lgkmcnt(0)" ::: "memory");
  if (lane == 0) asm volatile("ds_add_u32 %0, %1" :: "v"(L_MISC + 4 * c), "v"(1u) : "memory");
}
template <int SL = 1> DI void await(int c, u32 target) {
#pragma unroll 1
  for (u32 spin = 0; spin < (1u << 22); ++spin) {
    asm volatile("" ::: "memory");
    if (ldsu(L_MISC + 4 * c) >= target) break;
    if (SL) __builtin_amdgcn_s_sleep(SL);
  }
  asm volatile("" ::: "memory");
}
DI float swap32(float v, bool hi) {
  auto r = __builtin_amdgcn_permlane32_swap(__float_as_uint(v), __float_as_uint(v), false, false);
  return __uint_as_float(hi ? r[0] : r[1]);
}
DI float wave_max_nonneg(float v) {
  u32 x = __float_as_uint(v);
  x = max(x, (u32)__builtin_amdgcn_update_dpp(0, (int)x, 0x111, 0xf, 0xf, false));
  x = max(x, (u32)__builtin_amdgcn_update_dpp(0, (int)x, 0x112, 0xf, 0xf, false));
  x = max(x, (u32)__builtin_amdgcn_update_dpp(0, (int)x, 0x114, 0xf, 0xf, false));
  x = max(x, (u32)__builtin_amdgcn_update_dpp(0, (int)x, 0x118, 0xf, 0xf, false));
  x = max(x, (u32)__builtin_amdgcn_update_dpp(0, (int)x, 0x142, 0xa, 0xf, false));
  x = max(x, (u32)__builtin_amdgcn_update_dpp(0, (int)x, 0x143, 0xc, 0xf, false));
  return __uint_as_float((u32)__builtin_amdgcn_readlane((int)x, 63));
}
DI void gstore_flag(char* ws, u32 off, u32 v) { __hip_atomic_store((GAS u32*)(ws + off), v, __ATOMIC_RELAXED, __HIP_MEMORY_SCOPE_AGENT); }
DI void poll_words(char* ws, u32 off, int n, u32 tag, int lane) {
  GAS u32* p = (GAS u32*)(ws + off) + (lane < n ? lane : 0) * 32;
  u32 a = __hip_atomic_load(p, __ATOMIC_RELAXED, __HIP_MEMORY_SCOPE_AGENT);
#pragma unroll 1
  for (u32 spin = 0; spin < (1u << 16); ++spin) {
    __builtin_amdgcn_s_sleep(2);
    u32 c = __hip_atomic_load(p, __ATOMIC_RELAXED, __HIP_MEMORY_SCOPE_AGENT);
    SBAR();
    if (__all(a == tag)) break;
    __builtin_amdgcn_s_sleep(2);
    a = __hip_atomic_load(p, __ATOMIC_RELAXED, __HIP_MEMORY_SCOPE_AGENT);
    SBAR();
    if (__all(c == tag)) break;
  }
  asm volatile("" ::: "memory");
}
DI float poll_kv(char* ws, int rep, u32 tag, int lane) {
  GAS u64* p = (GAS u64*)(ws + WS_KVFLAG + rep * 4096) + (lane & 31) * 16;
  u64 a = __hip_atomic_load(p, __ATOMIC_RELAXED, __HIP_MEMORY_SCOPE_AGENT), g = 0;
#pragma unroll 1
  for (u32 spin = 0; spin < (1u << 16); ++spin) {
    __builtin_amdgcn_s_sleep(2);
    u64 c = __hip_atomic_load(p, __ATOMIC_RELAXED, __HIP_MEMORY_SCOPE_AGENT);
    SBAR();
    g = a;
    if (__all((u32)a == tag)) break;
    __builtin_amdgcn_s_sleep(2);
    a = __hip_atomic_load(p, __ATOMIC_RELAXED, __HIP_MEMORY_SCOPE_AGENT);
    SBAR();
    g = c;
    if (__all((u32)c == tag)) break;
  }
  asm volatile("" ::: "memory");
  return __uint_as_float((u32)(g >> 32));
}
DI void cmul(float ar, float ai, float br, float bi, float& cr, float& ci) {
  float x, y;
  asm("v_mul_f32 %0, %2, %4\n\tv_mul_f32 %1, %2, %5\n\tv_fma_f32 %0, -%3, %5, %0\n\tv_fma_f32 %1, %3, %4, %1"
      : "=&v"(x), "=&v"(y) : "v"(ar), "v"(ai), "v"(br), "v"(bi));
  cr = x; ci = y;
}
DI void cstep(float ar, float ai, float& hr, float& hi, float ur, float ui) {
  float x, y;
  asm("v_fma_f32 %0, -%3, %5, %6\n\tv_fma_f32 %1, %3, %4, %7\n\tv_fma_f32 %0, %2, %4, %0\n\tv_fma_f32 %1, %2, %5, %1"
      : "=&v"(x), "=&v"(y) : "v"(ar), "v"(ai), "v"(hr), "v"(hi), "v"(ur), "v"(ui));
  hr = x; hi = y;
}
DI void set_pad(u32x4& f, float m, int h) {
  const float a = rbf(-m), b = rbf(-m - a), c = rbf(-m - a - b);
  if (h) { f.z = cvtpk(a, b); f.w = cvtpk(c, 0.f); }
}

#define VMTHR() VMCNT(4)

template <bool W_IS_A, int WROW, bool MID>
DI f32x16 proj_tile(int row, int h, u32 wa, int lane) {
  const int sw = (row >> 1) & 7;
  u32 xa[4];
#pragma unroll
  for (int bb = 0; bb < 4; ++bb) xa[bb] = L_XB + row * 128 + (((2 * bb + h) ^ sw) << 4);
  u32x4 xf[28], wf[28];
  f32x16 acc = {};
#pragma unroll
  for (int ks = 0; ks < 28 + 5; ++ks) {
    if (ks >= 5) {
      const int k = ks - 5;
      acc = W_IS_A ? MFMA(frag(wf[k]), frag(xf[k]), acc) : MFMA(frag(xf[k]), frag(wf[k]), acc);
      SBAR();
    }
    if (ks < 28) {
      if (MID && ks == 14) {
        asm volatile("s_nop 7\n\ts_nop 7\n\ts_waitcnt vmcnt(0) lgkmcnt(0)" ::: "memory");
        if (lane == 0) asm volatile("ds_add_u32 %0, %1" :: "v"(L_MISC + 4 * C_WQ2), "v"(1u) : "memory");
        await<0>(C_WQ2, 4);
      }
      xf[ks] = ldsr128(xa[ks & 3] + 8192 * (ks >> 2));
      wf[ks] = trfrag(wa + ks * 16 * WROW, wa + ks * 16 * WROW + 4 * WROW);
      SBAR();
    }
  }
  MPIN(acc);
  return acc;
}

__global__ void __launch_bounds__(512, 2) fused_kernel(Params P) {
  extern __shared__ __attribute__((aligned(16))) char lds_dyn[];
  asm volatile("" : "+s"(P.Ax), "+s"(P.lre), "+s"(P.lim), "+s"(P.Bre), "+s"(P.Bim), "+s"(P.Cre), "+s"(P.Cim), "+s"(P.ldt));
  asm volatile("" : "+s"(P.Dsk), "+s"(P.Bx), "+s"(P.wq), "+s"(P.wk), "+s"(P.wv), "+s"(P.wo), "+s"(P.out), "+s"(P.ws));
  const int tid = threadIdx.x, lane = tid & 63, wv = __builtin_amdgcn_readfirstlane(tid >> 6);
  const int r = lane & 31, h = lane >> 5;
  const int b = blockIdx.x;
  const int tb = (b & 7) * 4 + (b >> 6), sl = (b >> 3) & 7;
  char* ws = P.ws;
  const int tl = b >> 6;
  if (tid == 0) asm volatile("s_mov_b32 m0, %2\n\ts_nop 0\n\tglobal_load_lds_dword %0, %1 sc1" :: "v"(0), "s"(ws), "s"(L_MISC + 4 * C_EPOCH) : "memory");
  const u32 vo = lane * 16;
  {
    const int row = 8 * wv + (lane >> 3);
    const u32 xv = row * 896 + (((lane & 7) ^ ((row >> 1) & 7)) << 4);
    const char* src = (const char*)P.Bx + (size_t)tb * 64 * 896;
#pragma unroll
    for (int j = 0; j < 7; ++j) {
      int c = j + sl; c = c >= 7 ? c - 7 : c;
      glds16(src + c * 128, xv, L_XB + c * 8192 + wv * 1024);
    }
  }
  if (tid < 31) *(LAS u32*)(size_t)(L_MISC + 4 * tid) = 0;
  const auto rs_ws = __builtin_amdgcn_make_buffer_rsrc((void*)ws, 0, WS_END, 0x00020000);
  const auto rs_out = __builtin_amdgcn_make_buffer_rsrc((void*)P.out, 0, 2048 * 896 * 4, 0x00020000);
  const int g1 = (lane >> 4) & 1, q4 = (lane >> 2) & 3, p4 = lane & 3;
  const int l7 = lane / 7;
  const u32 vo7 = l7 * 896 + (lane - 7 * l7) * 16;

  if (wv < 4) {
    const int w = wv;
    const char* wqsrc = (const char*)P.wq + sl * 112 + w * 7 * 7168;
    auto stage_wq = [&](int hf) {
      if (lane < 56) {
#pragma unroll
        for (int j = 0; j < 7; ++j) {
          int i = j + 2 * tl; i = (i >= 7 ? i - 7 : i) + 28 * hf;
          glds16(wqsrc + i * 7168, vo7, L_WQ + (w * 7 + i) * 896);
        }
      }
    };
    if (sl != 7) {
      stage_wq(0); stage_wq(1);
      asm volatile("s_waitcnt vmcnt(14) lgkmcnt(0)\n\ts_barrier" ::: "memory");
    } else {
      VMCNT(0);
      stage_wq(0); stage_wq(1);
      asm volatile("s_waitcnt lgkmcnt(0)\n\ts_barrier" ::: "memory");
    }
    const u32 tag = ldsu(L_MISC + 4 * C_EPOCH) + 1u;
    VMCNT(7);
    arrive(C_WQ, lane);
    await<0>(C_WQ, 4);
    {
      f32x16 acc = proj_tile<true, 112, true>(32 * (w & 1) + r, h, L_WQ + (8 * h + q4) * 112 + (w >> 1) * 56 + g1 * 32 + 8 * p4, lane);
      const float qs = 0.27264060f;
      float n2 = 0.f;
#pragma unroll
      for (int i = 0; i < 16; ++i) { acc[i] *= qs; if (i < 12) n2 = fmaf(acc[i], acc[i], n2); else n2 = h ? n2 : fmaf(acc[i], acc[i], n2); }
      n2 += swap32(n2, h);
      u32x4 f0 = {cvtpk(acc[0], acc[1]), cvtpk(acc[2], acc[3]), cvtpk(acc[4], acc[5]), cvtpk(acc[6], acc[7])};
      u32x4 f1 = {cvtpk(acc[8], acc[9]), cvtpk(acc[10], acc[11]), cvtpk(acc[12], acc[13]), cvtpk(acc[14], acc[15])};
      if (h) { f1.z = 0; f1.w = __float_as_uint(__builtin_amdgcn_sqrtf(n2)); }
      ldsw128(L_QX + w * 2048 + vo, f0);
      ldsw128(L_QX + w * 2048 + 1024 + vo, f1);
      arrive(C_Q2, lane);
    }
    u32x4 qf[4][2];
    await<0>(C_Q2, 4);
#pragma unroll
    for (int t = 0; t < 4; ++t) { const u32 a = L_QX + ((w + t) & 3) * 2048 + vo; qf[t][0] = ldsr128(a); qf[t][1] = ldsr128(a + 1024); }
    arrive(C_QDONE, lane);
    if (w == 0) {
      const float kn = wave_max_nonneg(poll_kv(ws, b & 7, tag, lane));
      if (lane == 0) *(LAS float*)(size_t)L_KN = kn;
      arrive(C_KV, lane);
    } else {
      await<0>(C_KV, 1);
    }
    const u32 kvbase = WS_KVF + w * 65536;
    const int rot = b >> 3;
#define KVOFF(it) (kvbase + ((((it) + rot) & 15) << 12))
#define LDK(it, s) __builtin_amdgcn_raw_buffer_load_b128(rs_ws, vo + 1024 * (s), KVOFF(it), SC1)
#define LDV(it, s) __builtin_amdgcn_raw_buffer_load_b128(rs_ws, vo + 2048 + 1024 * (s), KVOFF(it), SC1)
#define NEXT(x) asm volatile("s_add_u32 %0, %0, 0x1000\n\ts_and_b32 %0, %0, 0xf000" : "+s"(x) :: "scc")
#define LDK2(s_) __builtin_amdgcn_raw_buffer_load_b128(rs_kv, vo + 1024 * (s_), koff, SC1)
#define LDV2(s_) __builtin_amdgcn_raw_buffer_load_b128(rs_kv, vo + 2048 + 1024 * (s_), voff, SC1)
    const auto rs_kv = __builtin_amdgcn_make_buffer_rsrc((void*)(ws + kvbase), 0, 65536, 0x00020000);
    f32x16 o[4];
    for (int attempt = 0; attempt < 2; ++attempt) {
      u32x4 kf[KV_DEPTH][2], vf[KV_DEPTH][2];
#pragma unroll
      for (int d = 0; d < KV_DEPTH; ++d) {
        kf[d][0] = LDK(d, 0); kf[d][1] = LDK(d, 1);
        if (d < KV_DEPTH - 1) { vf[d][0] = LDV(d, 0); vf[d][1] = LDV(d, 1); }
      }
      u32 koff = ((KV_DEPTH - 1 + rot) & 15) << 12, voff = ((KV_DEPTH - 2 + rot) & 15) << 12;
      SBAR();
      if (attempt == 0) {
        arrive(C_KVL, lane);
        const float kmax = __builtin_amdgcn_sqrtf(*(LAS float*)(size_t)L_KN);
#pragma unroll
        for (int t = 0; t < 4; ++t) set_pad(qf[t][1], __uint_as_float(qf[t][1].w) * kmax, h);
      }
#pragma unroll
      for (int t = 0; t < 4; ++t) { PIN(qf[t][0]); PIN(qf[t][1]); }
      SBAR();
      const f32x16 zero = {};
      f32x16 sc = MFMA(frag(kf[0][0]), frag(qf[0][0]), zero);
      sc = MFMA(frag(kf[0][1]), frag(qf[0][1]), sc);
      float e0 = 0.f, e1 = 0.f, e2 = 0.f, e3 = 0.f;
      u32x4 p1 = {0, 0, 0, 0};
#pragma unroll
      for (int it = 0; it < 16; ++it) {
        const int d = it % KV_DEPTH, dprev = (it + KV_DEPTH - 1) % KV_DEPTH;
#pragma unroll
        for (int qi = 0; qi < 4; ++qi) {
          const int dn = qi == 3 ? (it + 1) % KV_DEPTH : d, qn = (qi + 1) & 3;
          const int dp = qi == 0 ? dprev : d, qp = (qi + 3) & 3;
          const bool last = it == 15 && qi == 3;
          f32x16 sn; u32x4 p0;
          float c0, c1, c2, c3;
          if (!last) sn = MFMA(frag(kf[dn][0]), frag(qf[qn][0]), zero);
          SBAR();
          if (qi == 0) NEXT(voff);
          if (qi == 2) NEXT(koff);
          p1.z = cvtpk(e0, e1); p1.w = cvtpk(e2, e3); PIN(p1); SBAR();
          c0 = EX(sc[0]); c1 = EX(sc[1]); c2 = EX(sc[2]); c3 = EX(sc[3]); PIN4(c0, c1, c2, c3); SBAR();
          if (it > 0 || qi > 0) o[qp] = MFMA(frag(vf[dp][1]), frag(p1), o[qp]);
          SBAR();
          if (qi < 2 && it - 1 + KV_DEPTH < 16) { vf[dprev][qi] = LDV2(qi); SBAR(); }
          p0.x = cvtpk(c0, c1); p0.y = cvtpk(c2, c3); PIN2(p0.x, p0.y); SBAR();
          c0 = EX(sc[4]); c1 = EX(sc[5]);
          c2 = EX(sc[6]); c3 = EX(sc[7]); PIN4(c0, c1, c2, c3); SBAR();
          if (!last) sn = MFMA(frag(kf[dn][1]), frag(qf[qn][1]), sn);
          SBAR();
          if (qi == 2 && it + KV_DEPTH < 16) { kf[d][0] = LDK2(0); SBAR(); }
          p0.z = cvtpk(c0, c1); p0.w = cvtpk(c2, c3); PIN(p0); SBAR();
          c0 = EX(sc[8]); c1 = EX(sc[9]);
          c2 = EX(sc[10]); c3 = EX(sc[11]); PIN4(c0, c1, c2, c3); SBAR();
          o[qi] = MFMA(frag(vf[d][0]), frag(p0), it == 0 ? zero : o[qi]); SBAR();
          if (qi == 2 && it + KV_DEPTH < 16) { kf[d][1] = LDK2(1); SBAR(); }
          p1.x = cvtpk(c0, c1); p1.y = cvtpk(c2, c3); PIN2(p1.x, p1.y); SBAR();
          e0 = EX(sc[12]); e1 = EX(sc[13]);
          e2 = EX(sc[14]); e3 = EX(sc[15]); PIN4(e0, e1, e2, e3); SBAR();
          if (!last) sc = sn;
        }
      }
      p1.z = cvtpk(e0, e1); p1.w = cvtpk(e2, e3);
      o[3] = MFMA(frag(vf[15 % KV_DEPTH][1]), frag(p1), o[3]);
      MPIN4(o[0], o[1], o[2], o[3]);
      u32 lv;
      asm volatile("v_mbcnt_lo_u32_b32 %0, -1, 0\n\tv_mbcnt_hi_u32_b32 %0, -1, %0\n\tv_lshlrev_b32 %0, 4, %0" : "=v"(lv));
      if (attempt == 0) {
        bool bad = false;
#pragma unroll
        for (int t = 0; t < 4; ++t) bad |= !(o[t][12] >= 0x1p-80f && o[t][12] <= 0x1p100f);
        if (__any(bad && lv >= 512)) { if (lv == 0) *(LAS u32*)(size_t)(L_MISC + 4 * C_FB) = 1u; }
        await(C_HD, 4);
      }
#pragma unroll
      for (int t = 1; t < 4; ++t) {
#pragma unroll
        for (int g = 0; g < 4; ++g) ldsw128(L_C + w * 12288 + (t - 1) * 4096 + g * 1024 + lv, f4(o[t], g));
      }
      arrive(C_O, lv);
      await<0>(C_O, 4 * (attempt + 1));
      const u32 fb = attempt == 0 ? ldsu(L_MISC + 4 * C_FB) : 0u;
      if (!fb) break;
      {
        float mx[4];
#pragma unroll
        for (int t = 0; t < 4; ++t) { mx[t] = -3.0e38f; if (lv >= 512) { qf[t][1].z = 0; qf[t][1].w = 0; } }
#pragma unroll 1
        for (int it = 0; it < 16; ++it) {
          const u32x4 k0 = __builtin_amdgcn_raw_buffer_load_b128(rs_kv, lv, it << 12, SC1), k1 = __builtin_amdgcn_raw_buffer_load_b128(rs_kv, lv + 1024, it << 12, SC1);
#pragma unroll
          for (int t = 0; t < 4; ++t) {
            f32x16 s = MFMA(frag(k0), frag(qf[t][0]), zero);
            s = MFMA(frag(k1), frag(qf[t][1]), s);
#pragma unroll
            for (int i = 0; i < 16; ++i) mx[t] = fmaxf(mx[t], s[i]);
          }
        }
        const u32 ma = L_C + 49152;
#pragma unroll
        for (int t = 0; t < 4; ++t) { mx[t] = fmaxf(mx[t], swap32(mx[t], lv >= 512)); *(LAS float*)(size_t)(ma + (w * 4 + ((w + t) & 3)) * 256 + (lv >> 2)) = mx[t]; }
        arrive(C_MAX, lv);
        await(C_MAX, 4);
#pragma unroll
        for (int t = 0; t < 4; ++t) {
          float m = -3.0e38f;
#pragma unroll
          for (int ww = 0; ww < 4; ++ww) m = fmaxf(m, *(LAS float*)(size_t)(ma + (ww * 4 + ((w + t) & 3)) * 256 + (lv >> 2)));
          set_pad(qf[t][1], m, lv >= 512);
        }
      }
    }
    {
    int lane;
    asm volatile("v_mbcnt_lo_u32_b32 %0, -1, 0\n\tv_mbcnt_hi_u32_b32 %0, -1, %0" : "=v"(lane));
    const int r = lane & 31, h = lane >> 5, g1 = (lane >> 4) & 1, q4 = (lane >> 2) & 3, p4 = lane & 3;
    const u32 vo = lane * 16;
    {
      f32x16 own = o[0];
#pragma unroll
      for (int pp = 1; pp < 4; ++pp) {
        const u32 a = L_C + ((w + pp) & 3) * 12288 + (3 - pp) * 4096 + vo;
#pragma unroll
        for (int g = 0; g < 4; ++g) add4(own, g, ldsr128(a + g * 1024));
      }
      const float other = swap32(own[12], h);
      const float inv = __builtin_amdgcn_rcpf(h ? own[12] : other);
      u32x4 f0 = {cvtpk(own[0] * inv, own[1] * inv), cvtpk(own[2] * inv, own[3] * inv), cvtpk(own[4] * inv, own[5] * inv), cvtpk(own[6] * inv, own[7] * inv)};
      u32x4 f1 = {cvtpk(own[8] * inv, own[9] * inv), cvtpk(own[10] * inv, own[11] * inv), cvtpk(own[12] * inv, own[13] * inv), cvtpk(own[14] * inv, own[15] * inv)};
      if (h) { f1.z = 0; f1.w = 0; }
      const u32 so = WS_OF + ((tb * 2 + (w & 1)) * 16 + 2 * sl + (w >> 1)) * 2048 + vo;
      __builtin_amdgcn_raw_buffer_store_b128(f0, rs_ws, so, 0, SC1);
      __builtin_amdgcn_raw_buffer_store_b128(f1, rs_ws, so + 1024, 0, SC1);
    }
    {
      const int tm = w & 1, kh = w >> 1;
      VMCNT(0);
      if (lane == 0) gstore_flag(ws, WS_OFLAG + (tb * 32 + sl * 4 + w) * 128, tag);
      if (sl == 7) return;
      await(C_WO, 4);
      u32x4 wf[16][2];
      const u32 wa = 2 * kh * L_PRIV + L_WO_OFF + (4 * h + q4) * 128 + g1 * 32 + 8 * p4;
      const u32 wa0 = wa + 64 * kh, wa1 = wa + 64 * (1 - kh);
      const u32 hz = h ? 0 : 1024;
#pragma unroll
      for (int i = 0; i < 16; ++i) {
        const u32 a = (i >> 3) * L_PRIV + (28 * ((i >> 1) & 3) + 16 * (i & 1)) * 128;
        if ((i & 7) == 7) { wf[i][0] = trfrag(wa0 + a, wa0 + hz + a); wf[i][1] = trfrag(wa1 + a, wa1 + hz + a); }
        else { wf[i][0] = trfrag(wa0 + a, wa0 + a + 1024); wf[i][1] = trfrag(wa1 + a, wa1 + a + 1024); }
      }
#pragma unroll
      for (int i = 0; i < 16; ++i) PIN2(wf[i][0], wf[i][1]);
      if (w == 0) { poll_words(ws, WS_OFLAG + tb * 4096, 32, tag, lane); arrive(C_OF, lane); }
      else await<0>(C_OF, 1);
      u32x4 of[16];
      const u32 so = WS_OF + ((tb * 2 + tm) * 16 + 8 * kh) * 2048;
#pragma unroll
      for (int i = 0; i < 16; ++i) of[i] = __builtin_amdgcn_raw_buffer_load_b128(rs_ws, vo + (i & 3) * 1024, so + (i >> 2) * 4096, SC1);
      SBAR();
      f32x16 a0 = {}, a1 = {};
#pragma unroll
      for (int i = 0; i < 16; ++i) {
        a0 = MFMA(frag(of[i]), frag(wf[i][0]), a0);
        a1 = MFMA(frag(of[i]), frag(wf[i][1]), a1);
      }
      MPIN2(a0, a1);
      const f32x16 snd = a1;
      f32x16 own = a0;
      const u32 aw = L_C + w * 4096 + vo, ar = L_C + (w ^ 2) * 4096 + vo;
#pragma unroll
      for (int g = 0; g < 4; ++g) ldsw128(aw + g * 1024, f4(snd, g));
      arrive(C_P, lane);
      await<0>(C_P, 4);
#pragma unroll
      for (int g = 0; g < 4; ++g) add4(own, g, ldsr128(ar + g * 1024));
      {
        const u32 ov = ((tb * 64 + 32 * tm + 4 * h) * 896 + 448 + 64 * sl + 32 * kh + r) * 4;
#pragma unroll
        for (int i = 0; i < 16; ++i) __builtin_amdgcn_raw_buffer_store_b32(__float_as_uint(rbf(own[i])), rs_out, ov, ((i & 3) + 8 * (i >> 2)) * 3584, NT);
      }
    }
    }
  } else {
    if (sl == 7) __builtin_amdgcn_s_setprio(2);
    const int q = wv - 4;
    const u32 PA = q * L_PRIV;
    const int part = q >> 1, half = q & 1;
    if (sl == 7 && lane < 56) {
      const char* src = (const char*)((q >> 1) ? P.wv : P.wk) + (q & 1) * 14 * 896;
#pragma unroll
      for (int j = 0; j < 14; ++j) {
        int i = j + 4 * tl; i = i >= 14 ? i - 14 : i;
        glds16(src + i * 896, vo, L_C + (q * 14 + i) * 896);
      }
    }
    asm volatile("s_waitcnt vmcnt(0) lgkmcnt(0)\n\ts_barrier" ::: "memory");
    const u32 tag = ldsu(L_MISC + 4 * C_EPOCH) + 1u;
    if (sl == 7) {
      const int kind = q >> 1, ktm = q & 1;
      const u32 wa = L_C + kind * 25088 + (8 * h + q4) * 56 + g1 * 32 + 8 * p4;
      f32x16 acc;
      if (kind == 0) acc = proj_tile<true, 56, false>(32 * ktm + r, h, wa, lane);
      else acc = proj_tile<false, 56, false>(32 * ktm + r, h, wa, lane);
      u32x4 f0 = {cvtpk(acc[0], acc[1]), cvtpk(acc[2], acc[3]), cvtpk(acc[4], acc[5]), cvtpk(acc[6], acc[7])};
      u32x4 f1 = {cvtpk(acc[8], acc[9]), cvtpk(acc[10], acc[11]), cvtpk(acc[12], acc[13]), cvtpk(acc[14], acc[15])};
      if (kind == 0) {
        if (h) { f1.z = 0x3F803F80u; f1.w = 0x00003F80u; }
      } else if (r >= 28) {
        const u32 c = r == 28 ? 0x3F803F80u : 0u;
        f0 = (u32x4){c, c, c, c}; f1 = f0;
      }
      const u32 so = WS_KVF + (tb * 2 + ktm) * 4096 + kind * 2048 + vo;
      __builtin_amdgcn_raw_buffer_store_b128(f0, rs_ws, so, 0, SC1);
      __builtin_amdgcn_raw_buffer_store_b128(f1, rs_ws, so + 1024, 0, SC1);
      SBAR();
      if (kind == 0) {
        float n2 = 0.f;
#pragma unroll
        for (int i = 0; i < 16; ++i) { if (i < 12) n2 = fmaf(acc[i], acc[i], n2); else n2 = h ? n2 : fmaf(acc[i], acc[i], n2); }
        n2 += swap32(n2, h);
        n2 = wave_max_nonneg(n2);
        if (lane == 0) asm volatile("ds_max_u32 %0, %1" :: "v"(L_MISC + 4 * C_KMAX), "v"(n2) : "memory");
      }
      VMCNT(0);
      u32 old = 0;
      if (lane == 0) asm volatile("s_waitcnt lgkmcnt(0)\n\tds_add_rtn_u32 %0, %1, %2\n\ts_waitcnt lgkmcnt(0)" : "=&v"(old) : "v"(L_MISC + 4 * C_KVP), "v"(1u) : "memory");
      if (__builtin_amdgcn_readfirstlane(old) == 3) {
        const u32 km = ldsu(L_MISC + 4 * C_KMAX);
        if (lane < 8) __hip_atomic_store((GAS u64*)(ws + WS_KVFLAG + lane * 4096 + tb * 128), (u64)tag | ((u64)km << 32), __ATOMIC_RELAXED, __HIP_MEMORY_SCOPE_AGENT);
      }
      arrive(C_KVD, lane);
      await(C_KVD, 4);
    }

    u32 cva;
    {
      const int hi = lane >= 28, cs = lane - 28 * hi;
      cva = hi * 896 + (cs << 4);
    }
    auto stage_C4 = [&](int qq, int g) {
      const char* csrc = (const char*)((qq >> 1) ? P.Cim : P.Cre) + (size_t)sl * 56 * 896 + (qq & 1) * 448;
      int p0 = 4 * g + 8 * tl; p0 = p0 >= 28 ? p0 - 28 : p0;
      const u32 va = cva ^ ((p0 & 4) << 3), vb = va ^ 16;
      if (lane < 56) {
#pragma unroll
        for (int j = 0; j < 4; ++j) { glds16(csrc + (p0 + j) * 1792, j < 2 ? va : vb, qq * L_PRIV + (p0 + j) * 896); VMTHR(); }
      }
    };
    const int tm = (q ^ sl) & 1, tn = q >> 1;
    const bool valid = 32 * tn + r < 56;
    const int dcol = 56 * sl + (valid ? 32 * tn + r : 55), t0 = tb * 64 + 32 * tm + 4 * h;
    u32x4 cf[2][14];
    auto read_C = [&]() {
#pragma unroll
      for (int t = 0; t < 2; ++t) {
        int row = 32 * (t ^ part) + r; row = row > 55 ? 55 : row;
        const int x = (row >> 2) & 3;
        const u32 a0 = PA + row * 448 + ((h ^ x) << 4), a1 = PA + row * 448 + (((2 + h) ^ x) << 4);
#pragma unroll
        for (int j = 0; j < 14; ++j) cf[t][j] = ldsr128(((j & 1) ? a1 : a0) + 64 * (j >> 1));
      }
#pragma unroll
      for (int j = 0; j < 14; ++j) PIN2(cf[0][j], cf[1][j]);
      LGKM0();
    };
    if (sl == 7) {
      arrive(C_HD, lane);
      await(C_KVL, 4);
      {
        const char* cp = (const char*)(part ? P.Cim : P.Cre) + (tl * 2 + half) * 43904;
#pragma unroll 1
        for (int i = 0; i < 43; ++i) { glds16(cp + (i < 42 ? i * 1024 : 42880), vo, PA + (i & 7) * 1024); VMTHR(); }
        const char* wp = (const char*)P.wo + (tl * 4 + q) * 25088;
#pragma unroll 1
        for (int i = 0; i < 25; ++i) { glds16(wp + (i < 24 ? i * 1024 : 24064), vo, PA + (i & 7) * 1024); VMTHR(); }
        VMCNT(0);
      }
#pragma unroll 1
      for (int g = 0; g < 7; ++g) stage_C4(q, g);
      VMCNT(0);
      read_C();
    } else {
      const int tb2 = tb >> 1, cg = (tb & 1) * 7 + sl, ch = 32 * cg + r;
      const u32 vo1 = (lane ^ 1) * 16;
      float lr, li, cr, ci;
      if (lane < 54) {
        const char* bsrc = (const char*)(part ? P.Bim : P.Bre) + (size_t)(cg * 32 + half * 16) * 896;
        const u32 va = (tb2 & 1) ? vo1 : vo, vb = (tb2 & 1) ? vo : vo1;
#pragma unroll
        for (int j = 0; j < 16; ++j) {
          const int i = j ^ (8 * (tb2 & 1));
          glds16(bsrc + i * 896, j < 8 ? va : vb, L_C + (q * 16 + i) * 864);
        }
      }
      u32 vlre = ((const GAS u16*)P.lre)[ch], vlim = ((const GAS u16*)P.lim)[ch], vldt = ((const GAS u16*)P.ldt)[ch];
      u32x4 blast_re = *(const GAS u32x4*)(P.Bre + (size_t)ch * 448 + 432 + 8 * h);
      u32x4 blast_im = *(const GAS u32x4*)(P.Bim + (size_t)ch * 448 + 432 + 8 * h);
      asm volatile("" : "+v"(vlre), "+v"(vlim), "+v"(vldt), "+v"(blast_re), "+v"(blast_im) :: "memory");
      {
        const float dt = rbf(expf(__uint_as_float(vldt << 16))), a = __uint_as_float(vlre << 16), bb = __uint_as_float(vlim << 16);
        const float er = expf(dt * a);
        float sn, cs;
        sincosf(dt * bb, &sn, &cs);
        lr = er * cs; PIN(lr); li = er * sn;
        const float nr = lr - 1.f, ni = li, den = 2.0f / (a * a + bb * bb);
        cr = (nr * a + ni * bb) * den; PIN(cr); ci = (ni * a - nr * bb) * den;
      }
      float l16r = lr, l16i = li;
#pragma unroll
      for (int i = 0; i < 4; ++i) cmul(l16r, l16i, l16r, l16i, l16r, l16i);
      float l32r, l32i, l128r, l128i;
      cmul(l16r, l16i, l16r, l16i, l32r, l32i);
      cmul(l32r, l32i, l32r, l32i, l128r, l128i);
      cmul(l128r, l128i, l128r, l128i, l128r, l128i);
      float lhr = 1.f, lhi = 0.f;
#pragma unroll 1
      for (int j = 8; j < tb2; ++j) cmul(lhr, lhi, l128r, l128i, lhr, lhi);
      PIN4(lr, li, cr, ci); PIN4(l16r, l16i, l32r, l32i); PIN4(l128r, l128i, lhr, lhi);
      const char* xsrc = (const char*)P.Ax + (size_t)(tb2 * 128 + q * 32) * 896;
      const u32 xv0 = (lane >> 3) * 896 + (((lane & 7) ^ (lane >> 4)) << 4), xv1 = xv0 ^ 64;
      const int rho = 16 * ((r >> 2) & 1) + (r & 3) + 4 * (r >> 3), sw = (rho >> 1) & 7;
      u32 xa[4];
#pragma unroll
      for (int bb = 0; bb < 4; ++bb) xa[bb] = PA + rho * 128 + (((2 * bb + h) ^ sw) << 4);
      const int xrot = cg % 6;
      u32 ba[7];
#pragma unroll
      for (int c = 0; c < 7; ++c) {
        int kc = c + xrot; kc = c == 6 ? 6 : kc >= 6 ? kc - 6 : kc;
        ba[c] = L_C + r * 864 + ((h ^ ((r >> 3) & 1)) << 4) + 128 * kc;
        PIN(ba[c]);
      }
      PIN4(xa[0], xa[1], xa[2], xa[3]);
      VMCNT(0);
      arrive(C_BFULL, lane);
      await(C_QDONE, 4);
      auto xdma = [&](int c) {
#pragma unroll
        for (int j = 0; j < 4; ++j) {
          int kc = c + xrot; kc = c == 6 ? 6 : kc >= 6 ? kc - 6 : kc;
          glds16(xsrc + kc * 128 + j * 7168, (j & 1) ? xv1 : xv0, PA + (c % 6) * 4096 + j * 1024);
        }
      };
      xdma(0); xdma(1);
      await(C_BFULL, 4);
      f32x16 ur = {}, ui = {};
      PIN2(ur, ui);
      __builtin_amdgcn_s_setprio(0);
      {
        u32x4 xf[28], br[28], bi[28];
#pragma unroll
        for (int ks = 0; ks < 28 + 4; ++ks) {
          if (ks >= 4) {
            const int k = ks - 4;
            ur = MFMA(frag(xf[k]), frag(br[k]), ur);
            ui = MFMA(frag(xf[k]), frag(bi[k]), ui);
            SBAR();
          }
          if (ks < 28) {
            if ((ks & 3) == 0) {
              const int c = ks >> 2;
              if (c == 1 || c == 6) VMCNT(0); else VMCNT(4);
              if (c == 1) { asm volatile("s_nop 7\n\ts_nop 7" ::: "memory"); await(C_KVL, 4); xdma(2); }
              if (c >= 1 && c + 2 <= 6) xdma(c + 2);
            }
            xf[ks] = ldsr128(xa[ks & 3] + ((ks >> 2) % 6) * 4096);
            br[ks] = ks < 27 ? ldsr128(ba[ks >> 2] + 32 * (ks & 3)) : blast_re;
            bi[ks] = ks < 27 ? ldsr128(ba[ks >> 2] + 27648 + 32 * (ks & 3)) : blast_im;
            SBAR();
          }
        }
      }
      MPIN2(ur, ui);
      __builtin_amdgcn_s_setprio(2);
      float ar = 0.f, ai = 0.f;
#pragma unroll
      for (int i = 0; i < 16; ++i) {
        float tr, ti;
        cmul(cr, ci, ur[i], ui[i], tr, ti);
        ur[i] = tr; ui[i] = ti;
        cstep(lr, li, ar, ai, tr, ti);
      }
      const float or_ = swap32(ar, h), oi_ = swap32(ai, h);
      const float lor = h ? or_ : ar, loi = h ? oi_ : ai;
      float a32r = lor, a32i = loi;
      cstep(l16r, l16i, a32r, a32i, h ? ar : or_, h ? ai : oi_);
      if (q < 3 && h == 0) *(LAS f32x2*)(size_t)(L_AGG + q * 256 + r * 8) = (f32x2){a32r, a32i};
      arrive(C_AGG, lane);
      await(C_AGG, 4);
      f32x2 wagg[3];
#pragma unroll
      for (int j = 0; j < 3; ++j) wagg[j] = *(LAS f32x2*)(size_t)(L_AGG + j * 256 + r * 8);
      const u32 aggb = WS_AGG + cg * 8192;
      if (q == 3 && tb2 < 15) {
        float tr = wagg[0].x, ti = wagg[0].y;
        cstep(l32r, l32i, tr, ti, wagg[1].x, wagg[1].y);
        cstep(l32r, l32i, tr, ti, wagg[2].x, wagg[2].y);
        cstep(l32r, l32i, tr, ti, a32r, a32i);
        __hip_atomic_store((GAS u64*)(ws + aggb + tb2 * 512 + r * 16 + h * 8), (u64)__float_as_uint(h ? ti : tr) | ((u64)tag << 32), __ATOMIC_RELAXED, __HIP_MEMORY_SCOPE_AGENT);
      }
      float hr = 0.f, hi = 0.f;
      int cnext = 2;
      if (q < 3) {
#pragma unroll 1
        for (int g = q; g < 7; g += 3) stage_C4(3, g);
        stage_C4(q, 0); stage_C4(q, 1);
        VMCNT(8);
        arrive(C_C3, lane);
#pragma unroll 1
        for (; cnext < 7 && ldsu(L_MISC + 4 * C_CARRY) == 0; ++cnext) stage_C4(q, cnext);
        await(C_CARRY, 1);
        const f32x2 cv = *(LAS f32x2*)(size_t)(L_CARRY + r * 8);
        hr = cv.x; hi = cv.y;
      } else {
        if (tb2 > 0) {
          u32x4 g[8];
          u32 gv[8];
#pragma unroll
          for (int j = 0; j < 8; ++j) gv[j] = r * 16 + ((8 * h + j < tb2) ? (8 * h + j) * 512 : 0);
#pragma unroll 1
          for (u32 spin = 0; spin < (1u << 17); ++spin) {
            asm volatile("" ::: "memory");
            u32 bad = 0;
#pragma unroll
            for (int j = 0; j < 8; ++j) g[j] = __builtin_amdgcn_raw_buffer_load_b128(rs_ws, gv[j], aggb, SC1);
#pragma unroll
            for (int j = 0; j < 8; ++j) bad |= (g[j].y ^ tag) | (g[j].w ^ tag);
            if (__all(bad == 0)) break;
            __builtin_amdgcn_s_sleep(4);
          }
          asm volatile("" ::: "memory");
          const int nb = tb2 - 8 * h;
#pragma unroll
          for (int j = 0; j < 8; ++j) {
            if (j < tb2) {
              float nr = hr, ni = hi;
              cstep(l128r, l128i, nr, ni, __uint_as_float(g[j].x), __uint_as_float(g[j].z));
              if (j < nb) { hr = nr; hi = ni; }
            }
          }
          float lo_r = swap32(hr, h), lo_i = swap32(hi, h), up_r = lo_r, up_i = lo_i;
          if (h) { up_r = hr; up_i = hi; } else { lo_r = hr; lo_i = hi; }
          cstep(lhr, lhi, lo_r, lo_i, up_r, up_i);
          hr = lo_r; hi = lo_i;
        }
        if (h == 0) *(LAS f32x2*)(size_t)(L_CARRY + r * 8) = (f32x2){hr, hi};
        arrive(C_CARRY, lane);
      }
#pragma unroll
      for (int j = 0; j < 3; ++j)
        if (j < q) cstep(l32r, l32i, hr, hi, wagg[j].x, wagg[j].y);
      if (h) cstep(l16r, l16i, hr, hi, lor, loi);
      u32 pr[8], pi[8];
#pragma unroll
      for (int i = 0; i < 16; i += 2) {
        cstep(lr, li, hr, hi, ur[i], ui[i]);
        const float r0 = hr, i0 = hi;
        cstep(lr, li, hr, hi, ur[i + 1], ui[i + 1]);
        pr[i >> 1] = cvtpk(r0, hr); pi[i >> 1] = cvtpk(-i0, -hi);
      }
      const u32 ta = L_C + q * 5120 + r * 80 + 32 * h;
      ldsw128(ta, (u32x4){pr[0], pr[1], pr[2], pr[3]}); ldsw128(ta + 16, (u32x4){pr[4], pr[5], pr[6], pr[7]});
      ldsw128(ta + 2560, (u32x4){pi[0], pi[1], pi[2], pi[3]}); ldsw128(ta + 2560 + 16, (u32x4){pi[4], pi[5], pi[6], pi[7]});
      LGKM0();
      const u32 tr = L_C + q * 5120 + (8 * h + q4) * 80 + g1 * 32 + 8 * p4;
      const int tbh = 2 * tb2 + (q >> 1);
      const u32 so = WS_HF + ((tbh * 2 + (q & 1)) * 56 + 2 * cg) * 1024 + vo;
#pragma unroll
      for (int pt = 0; pt < 2; ++pt)
#pragma unroll
        for (int s = 0; s < 2; ++s) {
          const u32 a = tr + pt * 2560 + s * 1280;
          __builtin_amdgcn_raw_buffer_store_b128(trfrag(a, a + 320), rs_ws, so + pt * 28 * 1024 + s * 1024, 0, SC1);
        }
      arrive(C_HD, lane);
      VMCNT(0);
      if (lane == 0) gstore_flag(ws, WS_HFLAG + (tbh * 32 + cg * 2 + (q & 1)) * 128, tag);
      if (q < 3) {
#pragma unroll 1
        for (; cnext < 7; ++cnext) stage_C4(q, cnext);
        VMCNT(0);
      } else {
        await(C_C3, 3);
      }
      read_C();
      const char* src = (const char*)P.wo + sl * 128 + q * 112 * 896;
      const u32 wv8 = (lane >> 3) * 896 + (lane & 7) * 16;
#pragma unroll
      for (int j = 0; j < 14; ++j) {
        int i = j + 4 * tl; i = i >= 14 ? i - 14 : i;
        glds16(src + i * 7168, wv8, PA + L_WO_OFF + 1024 * i); VMTHR();
      }
      VMCNT(0);
      arrive(C_WO, lane);
    }
    if (q == 0) { poll_words(ws, WS_HFLAG + tb * 4096, 28, tag, lane); arrive(C_HF, lane); }
    else await(C_HF, 1);
    {
      f32x16 y[4];
      const u32 so = WS_HF + ((tb * 2 + tm) * 56 + part * 28 + half * 14) * 1024, so1 = WS_HF + ((tb * 2 + (tm ^ 1)) * 56 + part * 28 + half * 14) * 1024;
      u32x4 hf[28];
#define LDH(j) __builtin_amdgcn_raw_buffer_load_b128(rs_ws, vo + (((j) % 14) & 3) * 1024, ((j) < 14 ? so : so1) + (((j) % 14) >> 2) * 4096, SC1)
#pragma unroll
      for (int j = 0; j < HWIN; ++j) hf[j] = LDH(j);
      SBAR();
      const f32x16 zero = {};
      __builtin_amdgcn_s_setprio(0);
#pragma unroll
      for (int j = 0; j < 28; ++j) {
        const int t = j / 14, k = j % 14;
        y[t] = MFMA(frag(hf[j]), frag(cf[0][k]), k == 0 ? zero : y[t]);
        y[2 + t] = MFMA(frag(hf[j]), frag(cf[1][k]), k == 0 ? zero : y[2 + t]);
        SBAR();
        if (j + HWIN < 28) { hf[j + HWIN] = LDH(j + HWIN); SBAR(); }
      }
      u16 xs[16];
      {
        const auto rs_ax = __builtin_amdgcn_make_buffer_rsrc((void*)P.Ax, 0, 2048 * 896, 0x00020000);
        const u32 xv = (t0 * 448 + dcol) * 2;
#pragma unroll
        for (int i = 0; i < 16; ++i) xs[i] = __builtin_amdgcn_raw_buffer_load_b16(rs_ax, xv + (i & 3) * 896, (i >> 2) * 7168, 0);
      }
      const u16 dsk16 = ((const GAS u16*)P.Dsk)[dcol];
      MPIN4(y[0], y[1], y[2], y[3]);
      __builtin_amdgcn_s_setprio(2);
#pragma unroll
      for (int t = 1; t < 4; ++t) {
#pragma unroll
        for (int g = 0; g < 4; ++g) ldsw128(PA + (t - 1) * 4096 + g * 1024 + vo, f4(y[t], g));
      }
      arrive(C_Y, lane);
      await(C_Y, 4);
      f32x16 own = y[0];
#pragma unroll
      for (int t = 1; t < 4; ++t) {
#pragma unroll
        for (int g = 0; g < 4; ++g) add4(own, g, ldsr128((q ^ t) * L_PRIV + (t - 1) * 4096 + g * 1024 + vo));
      }
      if (valid) {
        const u32 ov = (t0 * 896 + dcol) * 4;
#pragma unroll
        for (int i = 0; i < 16; ++i) {
          float sk = bf2f(xs[i]) * bf2f(dsk16);
          PIN(sk);
          __builtin_amdgcn_raw_buffer_store_b32(__float_as_uint(own[i] + rbf(sk)), rs_out, ov, ((i & 3) + 8 * (i >> 2)) * 3584, NT);
        }
      }
    }
    if (tb == 31 && sl == 0 && q == 0) {
      await<0>(C_KV, 1);
      if (lane == 0) gstore_flag(ws, WS_EPOCH, tag);
    }
  }
}

extern "C" void kernel_launch(void* const* d_in, const int* in_sizes, int n_in, void* d_out, int out_size, void* d_ws, size_t ws_size,
                              hipStream_t stream) {
  Params P;
  P.Ax = (const u16*)d_in[0]; P.lre = (const u16*)d_in[1]; P.lim = (const u16*)d_in[2]; P.Bre = (const u16*)d_in[3];
  P.Bim = (const u16*)d_in[4]; P.Cre = (const u16*)d_in[5]; P.Cim = (const u16*)d_in[6]; P.ldt = (const u16*)d_in[7];
  P.Dsk = (const u16*)d_in[8]; P.Bx = (const u16*)d_in[9]; P.wq = (const u16*)d_in[10]; P.wk = (const u16*)d_in[11];
  P.wv = (const u16*)d_in[12]; P.wo = (const u16*)d_in[13];
  P.out = (float*)d_out; P.ws = (char*)d_ws;
  (void)hipFuncSetAttribute((const void*)fused_kernel, hipFuncAttributeMaxDynamicSharedMemorySize, LDS_BYTES);
  fused_kernel<<<256, 512, LDS_BYTES, stream>>>(P);
}
```

```cpp
#include <hip/hip_runtime.h>

typedef unsigned short u16;
typedef unsigned int u32;
typedef unsigned long long u64;
using bf16x8 = __attribute__((ext_vector_type(8))) short;
using f32x16 = __attribute__((ext_vector_type(16))) float;
using u32x4 = __attribute__((ext_vector_type(4))) unsigned;
using u32x2 = __attribute__((ext_vector_type(2))) unsigned;
typedef short v4i16_t __attribute__((ext_vector_type(4)));
typedef __bf16 bf16x2_t __attribute__((ext_vector_type(2)));
typedef float f32x2 __attribute__((ext_vector_type(2)));
#define LAS __attribute__((address_space(3)))
#define GAS __attribute__((address_space(1)))
#define DI __device__ __forceinline__
#define MFMA(a, b, c) __builtin_amdgcn_mfma_f32_32x32x16_bf16((a), (b), (c), 0, 0, 0)
#define SBAR() __builtin_amdgcn_sched_barrier(0)
#define PIN(x) asm volatile("" : "+v"(x))
#define PIN2(a, b) asm volatile("" : "+v"(a), "+v"(b))
#define PIN4(a, b, c, d) asm volatile("" : "+v"(a), "+v"(b), "+v"(c), "+v"(d))
#define MPIN(x) asm volatile("s_nop 7\n\ts_nop 7" : "+v"(x))
#define MPIN2(a, b) asm volatile("s_nop 7\n\ts_nop 7" : "+v"(a), "+v"(b))
#define MPIN4(a, b, c, d) asm volatile("s_nop 7\n\ts_nop 7" : "+v"(a), "+v"(b), "+v"(c), "+v"(d))
#define VMCNT(n) asm volatile("s_waitcnt vmcnt(" #n ")" ::: "memory")
#define LGKM0() asm volatile("s_waitcnt lgkmcnt(0)" ::: "memory")
#define EX(x) __builtin_amdgcn_exp2f(x)

constexpr int SC1 = 16;
constexpr int NT = 2;
constexpr int KV_DEPTH = 4;
constexpr int HWIN = 8;

constexpr u32 WS_EPOCH = 0;
constexpr u32 WS_HFLAG = 7u << 20;
constexpr u32 WS_OFLAG = WS_HFLAG + 131072;
constexpr u32 WS_KVFLAG = WS_OFLAG + 131072;
constexpr u32 WS_AGG = 32768;
constexpr u32 WS_KVF = WS_AGG + 131072;
constexpr u32 WS_OF = WS_KVF + 262144;
constexpr u32 WS_HF = WS_OF + 2097152;
constexpr u32 WS_END = WS_KVFLAG + 32768;

constexpr u32 L_XB = 0;
constexpr u32 L_WQ = 57344;
constexpr u32 L_C = 107520;
constexpr u32 L_MISC = L_C + 55296;
constexpr u32 L_AGG = L_MISC + 128;
constexpr u32 L_KN = L_MISC + 896;
constexpr u32 L_CARRY = L_C + 20480;
constexpr u32 L_PRIV = 26880;
constexpr u32 L_WO_OFF = 12288;
constexpr u32 L_QX = L_WQ;
constexpr u32 LDS_BYTES = 163840;
enum { C_WQ = 0, C_Q2, C_QDONE, C_BFULL, C_AGG, C_WO, C_O, C_MAX, C_P, C_Y, C_FB, C_KVP, C_KMAX, C_KV, C_OF, C_HF, C_WQ2, C_HD, C_KVL, C_KVD, C_C3, C_CARRY, C_CONST, C_EPOCH = 31 };

struct Params {
  const u16 *Ax, *lre, *lim, *Bre, *Bim, *Cre, *Cim, *ldt, *Dsk, *Bx, *wq, *wk, *wv, *wo;
  float* out;
  char* ws;
};

DI float bf2f(u16 v) { return __uint_as_float(((u32)v) << 16); }
DI u32 cvtpk(float a, float b) { return __builtin_bit_cast(u32, __builtin_convertvector((f32x2){a, b}, bf16x2_t)); }
DI float rbf(float a) { return __uint_as_float(cvtpk(a, 0.f) << 16); }
DI bf16x8 frag(u32x4 v) { return __builtin_bit_cast(bf16x8, v); }
DI void glds16(const void* sbase, u32 voff, u32 lds_base) {
  asm volatile("s_mov_b32 m0, %2\n\ts_nop 0\n\tglobal_load_lds_dwordx4 %0, %1 sc1" :: "v"(voff), "s"(sbase), "s"(lds_base) : "memory");
}
DI u32x4 ldsr128(u32 a) { return *(const LAS u32x4*)(size_t)a; }
DI void ldsw128(u32 a, u32x4 v) { *(LAS u32x4*)(size_t)a = v; }
DI u32 ldsu(u32 a) { return (u32)__builtin_amdgcn_readfirstlane(*(volatile LAS u32*)(size_t)a); }
DI u32x2 ldstr(u32 a) { return __builtin_bit_cast(u32x2, __builtin_amdgcn_ds_read_tr16_b64_v4i16((LAS v4i16_t*)(size_t)a)); }
DI u32x4 trfrag(u32 a0, u32 a1) { u32x2 lo = ldstr(a0), hi = ldstr(a1); return (u32x4){lo.x, lo.y, hi.x, hi.y}; }
DI u32x4 f4(const f32x16& v, int g) {
  return (u32x4){__float_as_uint(v[4 * g]), __float_as_uint(v[4 * g + 1]), __float_as_uint(v[4 * g + 2]), __float_as_uint(v[4 * g + 3])};
}
DI void add4(f32x16& v, int g, u32x4 a) {
  v[4 * g] += __uint_as_float(a.x); v[4 * g + 1] += __uint_as_float(a.y); v[4 * g + 2] += __uint_as_float(a.z); v[4 * g + 3] += __uint_as_float(a.w);
}
DI void arrive(int c, int lane) {
  asm volatile("s_waitcnt lgkmcnt(0)" ::: "memory");
  if (lane == 0) asm volatile("ds_add_u32 %0, %1" :: "v"(L_MISC + 4 * c), "v"(1u) : "memory");
}
template <int SL = 1> DI void await(int c, u32 target) {
#pragma unroll 1
  for (u32 spin = 0; spin < (1u << 22); ++spin) {
    asm volatile("" ::: "memory");
    if (ldsu(L_MISC + 4 * c) >= target) break;
    if (SL) __builtin_amdgcn_s_sleep(SL);
  }
  asm volatile("" ::: "memory");
}
DI float swap32(float v, bool hi) {
  auto r = __builtin_amdgcn_permlane32_swap(__float_as_uint(v), __float_as_uint(v), false, false);
  return __uint_as_float(hi ? r[0] : r[1]);
}
DI float wave_max_nonneg(float v) {
  u32 x = __float_as_uint(v);
  x = max(x, (u32)__builtin_amdgcn_update_dpp(0, (int)x, 0x111, 0xf, 0xf, false));
  x = max(x, (u32)__builtin_amdgcn_update_dpp(0, (int)x, 0x112, 0xf, 0xf, false));
  x = max(x, (u32)__builtin_amdgcn_update_dpp(0, (int)x, 0x114, 0xf, 0xf, false));
  x = max(x, (u32)__builtin_amdgcn_update_dpp(0, (int)x, 0x118, 0xf, 0xf, false));
  x = max(x, (u32)__builtin_amdgcn_update_dpp(0, (int)x, 0x142, 0xa, 0xf, false));
  x = max(x, (u32)__builtin_amdgcn_update_dpp(0, (int)x, 0x143, 0xc, 0xf, false));
  return __uint_as_float((u32)__builtin_amdgcn_readlane((int)x, 63));
}
DI void gstore_flag(char* ws, u32 off, u32 v) { __hip_atomic_store((GAS u32*)(ws + off), v, __ATOMIC_RELAXED, __HIP_MEMORY_SCOPE_AGENT); }
DI void poll_words(char* ws, u32 off, int n, u32 tag, int lane) {
  GAS u32* p = (GAS u32*)(ws + off) + (lane < n ? lane : 0) * 32;
  u32 a = __hip_atomic_load(p, __ATOMIC_RELAXED, __HIP_MEMORY_SCOPE_AGENT);
#pragma unroll 1
  for (u32 spin = 0; spin < (1u << 16); ++spin) {
    __builtin_amdgcn_s_sleep(2);
    u32 c = __hip_atomic_load(p, __ATOMIC_RELAXED, __HIP_MEMORY_SCOPE_AGENT);
    SBAR();
    if (__all(a == tag)) break;
    __builtin_amdgcn_s_sleep(2);
    a = __hip_atomic_load(p, __ATOMIC_RELAXED, __HIP_MEMORY_SCOPE_AGENT);
    SBAR();
    if (__all(c == tag)) break;
  }
  asm volatile("" ::: "memory");
}
DI float poll_kv(char* ws, int rep, u32 tag, int lane) {
  GAS u64* p = (GAS u64*)(ws + WS_KVFLAG + rep * 4096) + (lane & 31) * 16;
  u64 a = __hip_atomic_load(p, __ATOMIC_RELAXED, __HIP_MEMORY_SCOPE_AGENT), g = 0;
#pragma unroll 1
  for (u32 spin = 0; spin < (1u << 16); ++spin) {
    __builtin_amdgcn_s_sleep(2);
    u64 c = __hip_atomic_load(p, __ATOMIC_RELAXED, __HIP_MEMORY_SCOPE_AGENT);
    SBAR();
    g = a;
    if (__all((u32)a == tag)) break;
    __builtin_amdgcn_s_sleep(2);
    a = __hip_atomic_load(p, __ATOMIC_RELAXED, __HIP_MEMORY_SCOPE_AGENT);
    SBAR();
    g = c;
    if (__all((u32)c == tag)) break;
  }
  asm volatile("" ::: "memory");
  return __uint_as_float((u32)(g >> 32));
}
DI void cmul(float ar, float ai, float br, float bi, float& cr, float& ci) {
  float x, y;
  asm("v_mul_f32 %0, %2, %4\n\tv_mul_f32 %1, %2, %5\n\tv_fma_f32 %0, -%3, %5, %0\n\tv_fma_f32 %1, %3, %4, %1"
      : "=&v"(x), "=&v"(y) : "v"(ar), "v"(ai), "v"(br), "v"(bi));
  cr = x; ci = y;
}
DI void cstep(float ar, float ai, float& hr, float& hi, float ur, float ui) {
  float x, y;
  asm("v_fma_f32 %0, -%3, %5, %6\n\tv_fma_f32 %1, %3, %4, %7\n\tv_fma_f32 %0, %2, %4, %0\n\tv_fma_f32 %1, %2, %5, %1"
      : "=&v"(x), "=&v"(y) : "v"(ar), "v"(ai), "v"(hr), "v"(hi), "v"(ur), "v"(ui));
  hr = x; hi = y;
}
DI void set_pad(u32x4& f, float m, int h) {
  const float a = rbf(-m), b = rbf(-m - a), c = rbf(-m - a - b);
  if (h) { f.z = cvtpk(a, b); f.w = cvtpk(c, 0.f); }
}

#define VMTHR() VMCNT(4)

template <bool W_IS_A, int WROW, bool MID>
DI f32x16 proj_tile(int row, int h, u32 wa, int lane) {
  const int sw = (row >> 1) & 7;
  u32 xa[4];
#pragma unroll
  for (int bb = 0; bb < 4; ++bb) xa[bb] = L_XB + row * 128 + (((2 * bb + h) ^ sw) << 4);
  u32x4 xf[28], wf[28];
  f32x16 acc = {};
#pragma unroll
  for (int ks = 0; ks < 28 + 5; ++ks) {
    if (ks >= 5) {
      const int k = ks - 5;
      acc = W_IS_A ? MFMA(frag(wf[k]), frag(xf[k]), acc) : MFMA(frag(xf[k]), frag(wf[k]), acc);
      SBAR();
    }
    if (ks < 28) {
      if (MID && ks == 14) {
        asm volatile("s_nop 7\n\ts_nop 7\n\ts_waitcnt vmcnt(0) lgkmcnt(0)" ::: "memory");
        if (lane == 0) asm volatile("ds_add_u32 %0, %1" :: "v"(L_MISC + 4 * C_WQ2), "v"(1u) : "memory");
        await<0>(C_WQ2, 4);
      }
      xf[ks] = ldsr128(xa[ks & 3] + 8192 * (ks >> 2));
      wf[ks] = trfrag(wa + ks * 16 * WROW, wa + ks * 16 * WROW + 4 * WROW);
      SBAR();
    }
  }
  MPIN(acc);
  return acc;
}

__global__ void __launch_bounds__(512, 2) fused_kernel(Params P) {
  extern __shared__ __attribute__((aligned(16))) char lds_dyn[];
  asm volatile("" : "+s"(P.Ax), "+s"(P.lre), "+s"(P.lim), "+s"(P.Bre), "+s"(P.Bim), "+s"(P.Cre), "+s"(P.Cim), "+s"(P.ldt));
  asm volatile("" : "+s"(P.Dsk), "+s"(P.Bx), "+s"(P.wq), "+s"(P.wk), "+s"(P.wv), "+s"(P.wo), "+s"(P.out), "+s"(P.ws));
  const int tid = threadIdx.x, lane = tid & 63, wv = __builtin_amdgcn_readfirstlane(tid >> 6);
  const int r = lane & 31, h = lane >> 5;
  const int b = blockIdx.x;
  const int tb = (b & 7) * 4 + (b >> 6), sl = (b >> 3) & 7;
  char* ws = P.ws;
  const int tl = b >> 6;
  if (tid == 0) asm volatile("s_mov_b32 m0, %2\n\ts_nop 0\n\tglobal_load_lds_dword %0, %1 sc1" :: "v"(0), "s"(ws), "s"(L_MISC + 4 * C_EPOCH) : "memory");
  const u32 vo = lane * 16;
  {
    const int row = 8 * wv + (lane >> 3);
    const u32 xv = row * 896 + (((lane & 7) ^ ((row >> 1) & 7)) << 4);
    const char* src = (const char*)P.Bx + (size_t)tb * 64 * 896;
#pragma unroll
    for (int j = 0; j < 7; ++j) {
      int c = j + sl; c = c >= 7 ? c - 7 : c;
      glds16(src + c * 128, xv, L_XB + c * 8192 + wv * 1024);
    }
  }
  if (tid < 31) *(LAS u32*)(size_t)(L_MISC + 4 * tid) = 0;
  const auto rs_ws = __builtin_amdgcn_make_buffer_rsrc((void*)ws, 0, WS_END, 0x00020000);
  const auto rs_out = __builtin_amdgcn_make_buffer_rsrc((void*)P.out, 0, 2048 * 896 * 4, 0x00020000);
  const int g1 = (lane >> 4) & 1, q4 = (lane >> 2) & 3, p4 = lane & 3;
  const int l7 = lane / 7;
  const u32 vo7 = l7 * 896 + (lane - 7 * l7) * 16;

  if (wv < 4) {
    const int w = wv;
    const char* wqsrc = (const char*)P.wq + sl * 112 + w * 7 * 7168;
    auto stage_wq = [&](int hf) {
      if (lane < 56) {
#pragma unroll
        for (int j = 0; j < 7; ++j) {
          int i = j + 2 * tl; i = (i >= 7 ? i - 7 : i) + 28 * hf;
          glds16(wqsrc + i * 7168, vo7, L_WQ + (w * 7 + i) * 896);
        }
      }
    };
    if (sl != 7) {
      stage_wq(0); stage_wq(1);
      asm volatile("s_waitcnt vmcnt(14) lgkmcnt(0)\n\ts_barrier" ::: "memory");
    } else {
      VMCNT(0);
      stage_wq(0); stage_wq(1);
      asm volatile("s_waitcnt lgkmcnt(0)\n\ts_barrier" ::: "memory");
    }
    const u32 tag = ldsu(L_MISC + 4 * C_EPOCH) + 1u;
    VMCNT(7);
    arrive(C_WQ, lane);
    await<0>(C_WQ, 4);
    {
      f32x16 acc = proj_tile<true, 112, true>(32 * (w & 1) + r, h, L_WQ + (8 * h + q4) * 112 + (w >> 1) * 56 + g1 * 32 + 8 * p4, lane);
      const float qs = 0.27264060f;
      float n2 = 0.f;
#pragma unroll
      for (int i = 0; i < 16; ++i) { acc[i] *= qs; if (i < 12) n2 = fmaf(acc[i], acc[i], n2); else n2 = h ? n2 : fmaf(acc[i], acc[i], n2); }
      n2 += swap32(n2, h);
      u32x4 f0 = {cvtpk(acc[0], acc[1]), cvtpk(acc[2], acc[3]), cvtpk(acc[4], acc[5]), cvtpk(acc[6], acc[7])};
      u32x4 f1 = {cvtpk(acc[8], acc[9]), cvtpk(acc[10], acc[11]), cvtpk(acc[12], acc[13]), cvtpk(acc[14], acc[15])};
      if (h) { f1.z = 0; f1.w = __float_as_uint(__builtin_amdgcn_sqrtf(n2)); }
      ldsw128(L_QX + w * 2048 + vo, f0);
      ldsw128(L_QX + w * 2048 + 1024 + vo, f1);
      arrive(C_Q2, lane);
    }
    u32x4 qf[4][2];
    await<0>(C_Q2, 4);
#pragma unroll
    for (int t = 0; t < 4; ++t) { const u32 a = L_QX + ((w + t) & 3) * 2048 + vo; qf[t][0] = ldsr128(a); qf[t][1] = ldsr128(a + 1024); }
    arrive(C_QDONE, lane);
    if (w == 0) {
      const float kn = wave_max_nonneg(poll_kv(ws, b & 7, tag, lane));
      if (lane == 0) *(LAS float*)(size_t)L_KN = kn;
      arrive(C_KV, lane);
    } else {
      await<0>(C_KV, 1);
    }
    const u32 kvbase = WS_KVF + w * 65536;
    const int rot = b >> 3;
#define KVOFF(it) (kvbase + ((((it) + rot) & 15) << 12))
#define LDK(it, s) __builtin_amdgcn_raw_buffer_load_b128(rs_ws, vo + 1024 * (s), KVOFF(it), SC1)
#define LDV(it, s) __builtin_amdgcn_raw_buffer_load_b128(rs_ws, vo + 2048 + 1024 * (s), KVOFF(it), SC1)
#define NEXT(x) asm volatile("s_add_u32 %0, %0, 0x1000\n\ts_and_b32 %0, %0, 0xf000" : "+s"(x) :: "scc")
#define LDK2(s_) __builtin_amdgcn_raw_buffer_load_b128(rs_kv, vo + 1024 * (s_), koff, SC1)
#define LDV2(s_) __builtin_amdgcn_raw_buffer_load_b128(rs_kv, vo + 2048 + 1024 * (s_), voff, SC1)
    const auto rs_kv = __builtin_amdgcn_make_buffer_rsrc((void*)(ws + kvbase), 0, 65536, 0x00020000);
    f32x16 o[4];
    for (int attempt = 0; attempt < 2; ++attempt) {
      u32x4 kf[KV_DEPTH][2], vf[KV_DEPTH][2];
#pragma unroll
      for (int d = 0; d < KV_DEPTH; ++d) {
        kf[d][0] = LDK(d, 0); kf[d][1] = LDK(d, 1);
        if (d < KV_DEPTH - 1) { vf[d][0] = LDV(d, 0); vf[d][1] = LDV(d, 1); }
      }
      u32 koff = ((KV_DEPTH - 1 + rot) & 15) << 12, voff = ((KV_DEPTH - 2 + rot) & 15) << 12;
      SBAR();
      if (attempt == 0) {
        arrive(C_KVL, lane);
        const float kmax = __builtin_amdgcn_sqrtf(*(LAS float*)(size_t)L_KN);
#pragma unroll
        for (int t = 0; t < 4; ++t) set_pad(qf[t][1], __uint_as_float(qf[t][1].w) * kmax, h);
      }
#pragma unroll
      for (int t = 0; t < 4; ++t) { PIN(qf[t][0]); PIN(qf[t][1]); }
      SBAR();
      const f32x16 zero = {};
      f32x16 sc = MFMA(frag(kf[0][0]), frag(qf[0][0]), zero);
      sc = MFMA(frag(kf[0][1]), frag(qf[0][1]), sc);
      float e0 = 0.f, e1 = 0.f, e2 = 0.f, e3 = 0.f;
      u32x4 p1 = {0, 0, 0, 0};
#pragma unroll
      for (int it = 0; it < 16; ++it) {
        const int d = it % KV_DEPTH, dprev = (it + KV_DEPTH - 1) % KV_DEPTH;
#pragma unroll
        for (int qi = 0; qi < 4; ++qi) {
          const int dn = qi == 3 ? (it + 1) % KV_DEPTH : d, qn = (qi + 1) & 3;
          const int dp = qi == 0 ? dprev : d, qp = (qi + 3) & 3;
          const bool last = it == 15 && qi == 3;
          f32x16 sn; u32x4 p0;
          float c0, c1, c2, c3;
          if (!last) sn = MFMA(frag(kf[dn][0]), frag(qf[qn][0]), zero);
          SBAR();
          if (qi == 0) NEXT(voff);
          if (qi == 2) NEXT(koff);
          p1.z = cvtpk(e0, e1); p1.w = cvtpk(e2, e3); PIN(p1); SBAR();
          c0 = EX(sc[0]); c1 = EX(sc[1]); c2 = EX(sc[2]); c3 = EX(sc[3]); PIN4(c0, c1, c2, c3); SBAR();
          if (it > 0 || qi > 0) o[qp] = MFMA(frag(vf[dp][1]), frag(p1), o[qp]);
          SBAR();
          if (qi < 2 && it - 1 + KV_DEPTH < 16) { vf[dprev][qi] = LDV2(qi); SBAR(); }
          p0.x = cvtpk(c0, c1); p0.y = cvtpk(c2, c3); PIN2(p0.x, p0.y); SBAR();
          c0 = EX(sc[4]); c1 = EX(sc[5]);
          c2 = EX(sc[6]); c3 = EX(sc[7]); PIN4(c0, c1, c2, c3); SBAR();
          if (!last) sn = MFMA(frag(kf[dn][1]), frag(qf[qn][1]), sn);
          SBAR();
          if (qi == 2 && it + KV_DEPTH < 16) { kf[d][0] = LDK2(0); SBAR(); }
          p0.z = cvtpk(c0, c1); p0.w = cvtpk(c2, c3); PIN(p0); SBAR();
          c0 = EX(sc[8]); c1 = EX(sc[9]);
          c2 = EX(sc[10]); c3 = EX(sc[11]); PIN4(c0, c1, c2, c3); SBAR();
          o[qi] = MFMA(frag(vf[d][0]), frag(p0), it == 0 ? zero : o[qi]); SBAR();
          if (qi == 2 && it + KV_DEPTH < 16) { kf[d][1] = LDK2(1); SBAR(); }
          p1.x = cvtpk(c0, c1); p1.y = cvtpk(c2, c3); PIN2(p1.x, p1.y); SBAR();
          e0 = EX(sc[12]); e1 = EX(sc[13]);
          e2 = EX(sc[14]); e3 = EX(sc[15]); PIN4(e0, e1, e2, e3); SBAR();
          if (!last) sc = sn;
        }
      }
      p1.z = cvtpk(e0, e1); p1.w = cvtpk(e2, e3);
      o[3] = MFMA(frag(vf[15 % KV_DEPTH][1]), frag(p1), o[3]);
      MPIN4(o[0], o[1], o[2], o[3]);
      u32 lv;
      asm volatile("v_mbcnt_lo_u32_b32 %0, -1, 0\n\tv_mbcnt_hi_u32_b32 %0, -1, %0\n\tv_lshlrev_b32 %0, 4, %0" : "=v"(lv));
      if (attempt == 0) {
        bool bad = false;
#pragma unroll
        for (int t = 0; t < 4; ++t) bad |= !(o[t][12] >= 0x1p-80f && o[t][12] <= 0x1p100f);
        if (__any(bad && lv >= 512)) { if (lv == 0) *(LAS u32*)(size_t)(L_MISC + 4 * C_FB) = 1u; }
        await(C_HD, 4);
      }
#pragma unroll
      for (int t = 1; t < 4; ++t) {
#pragma unroll
        for (int g = 0; g < 4; ++g) ldsw128(L_C + w * 12288 + (t - 1) * 4096 + g * 1024 + lv, f4(o[t], g));
      }
      arrive(C_O, lv);
      await<0>(C_O, 4 * (attempt + 1));
      const u32 fb = attempt == 0 ? ldsu(L_MISC + 4 * C_FB) : 0u;
      if (!fb) break;
      {
        float mx[4];
#pragma unroll
        for (int t = 0; t < 4; ++t) { mx[t] = -3.0e38f; if (lv >= 512) { qf[t][1].z = 0; qf[t][1].w = 0; } }
#pragma unroll 1
        for (int it = 0; it < 16; ++it) {
          const u32x4 k0 = __builtin_amdgcn_raw_buffer_load_b128(rs_kv, lv, it << 12, SC1), k1 = __builtin_amdgcn_raw_buffer_load_b128(rs_kv, lv + 1024, it << 12, SC1);
#pragma unroll
          for (int t = 0; t < 4; ++t) {
            f32x16 s = MFMA(frag(k0), frag(qf[t][0]), zero);
            s = MFMA(frag(k1), frag(qf[t][1]), s);
#pragma unroll
            for (int i = 0; i < 16; ++i) mx[t] = fmaxf(mx[t], s[i]);
          }
        }
        const u32 ma = L_C + 49152;
#pragma unroll
        for (int t = 0; t < 4; ++t) { mx[t] = fmaxf(mx[t], swap32(mx[t], lv >= 512)); *(LAS float*)(size_t)(ma + (w * 4 + ((w + t) & 3)) * 256 + (lv >> 2)) = mx[t]; }
        arrive(C_MAX, lv);
        await(C_MAX, 4);
#pragma unroll
        for (int t = 0; t < 4; ++t) {
          float m = -3.0e38f;
#pragma unroll
          for (int ww = 0; ww < 4; ++ww) m = fmaxf(m, *(LAS float*)(size_t)(ma + (ww * 4 + ((w + t) & 3)) * 256 + (lv >> 2)));
          set_pad(qf[t][1], m, lv >= 512);
        }
      }
    }
    {
    int lane;
    asm volatile("v_mbcnt_lo_u32_b32 %0, -1, 0\n\tv_mbcnt_hi_u32_b32 %0, -1, %0" : "=v"(lane));
    const int r = lane & 31, h = lane >> 5, g1 = (lane >> 4) & 1, q4 = (lane >> 2) & 3, p4 = lane & 3;
    const u32 vo = lane * 16;
    {
      f32x16 own = o[0];
#pragma unroll
      for (int pp = 1; pp < 4; ++pp) {
        const u32 a = L_C + ((w + pp) & 3) * 12288 + (3 - pp) * 4096 + vo;
#pragma unroll
        for (int g = 0; g < 4; ++g) add4(own, g, ldsr128(a + g * 1024));
      }
      const float other = swap32(own[12], h);
      const float inv = __builtin_amdgcn_rcpf(h ? own[12] : other);
      u32x4 f0 = {cvtpk(own[0] * inv, own[1] * inv), cvtpk(own[2] * inv, own[3] * inv), cvtpk(own[4] * inv, own[5] * inv), cvtpk(own[6] * inv, own[7] * inv)};
      u32x4 f1 = {cvtpk(own[8] * inv, own[9] * inv), cvtpk(own[10] * inv, own[11] * inv), cvtpk(own[12] * inv, own[13] * inv), cvtpk(own[14] * inv, own[15] * inv)};
      if (h) { f1.z = 0; f1.w = 0; }
      const u32 so = WS_OF + ((tb * 2 + (w & 1)) * 16 + 2 * sl + (w >> 1)) * 2048 + vo;
      __builtin_amdgcn_raw_buffer_store_b128(f0, rs_ws, so, 0, SC1);
      __builtin_amdgcn_raw_buffer_store_b128(f1, rs_ws, so + 1024, 0, SC1);
    }
    {
      const int tm = w & 1, kh = w >> 1;
      VMCNT(0);
      if (lane == 0) gstore_flag(ws, WS_OFLAG + (tb * 32 + sl * 4 + w) * 128, tag);
      if (sl == 7) return;
      await(C_WO, 4);
      u32x4 wf[16][2];
      const u32 wa = 2 * kh * L_PRIV + L_WO_OFF + (4 * h + q4) * 128 + g1 * 32 + 8 * p4;
      const u32 wa0 = wa + 64 * kh, wa1 = wa + 64 * (1 - kh);
      const u32 hz = h ? 0 : 1024;
#pragma unroll
      for (int i = 0; i < 16; ++i) {
        const u32 a = (i >> 3) * L_PRIV + (28 * ((i >> 1) & 3) + 16 * (i & 1)) * 128;
        if ((i & 7) == 7) { wf[i][0] = trfrag(wa0 + a, wa0 + hz + a); wf[i][1] = trfrag(wa1 + a, wa1 + hz + a); }
        else { wf[i][0] = trfrag(wa0 + a, wa0 + a + 1024); wf[i][1] = trfrag(wa1 + a, wa1 + a + 1024); }
      }
#pragma unroll
      for (int i = 0; i < 16; ++i) PIN2(wf[i][0], wf[i][1]);
      if (w == 0) { poll_words(ws, WS_OFLAG + tb * 4096, 32, tag, lane); arrive(C_OF, lane); }
      else await<0>(C_OF, 1);
      u32x4 of[16];
      const u32 so = WS_OF + ((tb * 2 + tm) * 16 + 8 * kh) * 2048;
#pragma unroll
      for (int i = 0; i < 16; ++i) of[i] = __builtin_amdgcn_raw_buffer_load_b128(rs_ws, vo + (i & 3) * 1024, so + (i >> 2) * 4096, SC1);
      SBAR();
      f32x16 a0 = {}, a1 = {};
#pragma unroll
      for (int i = 0; i < 16; ++i) {
        a0 = MFMA(frag(of[i]), frag(wf[i][0]), a0);
        a1 = MFMA(frag(of[i]), frag(wf[i][1]), a1);
      }
      MPIN2(a0, a1);
      const f32x16 snd = a1;
      f32x16 own = a0;
      const u32 aw = L_C + w * 4096 + vo, ar = L_C + (w ^ 2) * 4096 + vo;
#pragma unroll
      for (int g = 0; g < 4; ++g) ldsw128(aw + g * 1024, f4(snd, g));
      arrive(C_P, lane);
      await<0>(C_P, 4);
#pragma unroll
      for (int g = 0; g < 4; ++g) add4(own, g, ldsr128(ar + g * 1024));
      {
        const u32 ov = ((tb * 64 + 32 * tm + 4 * h) * 896 + 448 + 64 * sl + 32 * kh + r) * 4;
#pragma unroll
        for (int i = 0; i < 16; ++i) __builtin_amdgcn_raw_buffer_store_b32(__float_as_uint(rbf(own[i])), rs_out, ov, ((i & 3) + 8 * (i >> 2)) * 3584, NT);
      }
    }
    }
  } else {
    if (sl == 7) __builtin_amdgcn_s_setprio(2);
    const int q = wv - 4;
    const u32 PA = q * L_PRIV;
    const int part = q >> 1, half = q & 1;
    if (sl == 7 && lane < 56) {
      const char* src = (const char*)((q >> 1) ? P.wv : P.wk) + (q & 1) * 14 * 896;
#pragma unroll
      for (int j = 0; j < 14; ++j) {
        int i = j + 4 * tl; i = i >= 14 ? i - 14 : i;
        glds16(src + i * 896, vo, L_C + (q * 14 + i) * 896);
      }
    }
    asm volatile("s_waitcnt vmcnt(0) lgkmcnt(0)\n\ts_barrier" ::: "memory");
    const u32 tag = ldsu(L_MISC + 4 * C_EPOCH) + 1u;
    if (sl == 7) {
      const int kind = q >> 1, ktm = q & 1;
      const u32 wa = L_C + kind * 25088 + (8 * h + q4) * 56 + g1 * 32 + 8 * p4;
      f32x16 acc;
      if (kind == 0) acc = proj_tile<true, 56, false>(32 * ktm + r, h, wa, lane);
      else acc = proj_tile<false, 56, false>(32 * ktm + r, h, wa, lane);
      u32x4 f0 = {cvtpk(acc[0], acc[1]), cvtpk(acc[2], acc[3]), cvtpk(acc[4], acc[5]), cvtpk(acc[6], acc[7])};
      u32x4 f1 = {cvtpk(acc[8], acc[9]), cvtpk(acc[10], acc[11]), cvtpk(acc[12], acc[13]), cvtpk(acc[14], acc[15])};
      if (kind == 0) {
        if (h) { f1.z = 0x3F803F80u; f1.w = 0x00003F80u; }
      } else if (r >= 28) {
        const u32 c = r == 28 ? 0x3F803F80u : 0u;
        f0 = (u32x4){c, c, c, c}; f1 = f0;
      }
      const u32 so = WS_KVF + (tb * 2 + ktm) * 4096 + kind * 2048 + vo;
      __builtin_amdgcn_raw_buffer_store_b128(f0, rs_ws, so, 0, SC1);
      __builtin_amdgcn_raw_buffer_store_b128(f1, rs_ws, so + 1024, 0, SC1);
      SBAR();
      if (kind == 0) {
        float n2 = 0.f;
#pragma unroll
        for (int i = 0; i < 16; ++i) { if (i < 12) n2 = fmaf(acc[i], acc[i], n2); else n2 = h ? n2 : fmaf(acc[i], acc[i], n2); }
        n2 += swap32(n2, h);
        n2 = wave_max_nonneg(n2);
        if (lane == 0) asm volatile("ds_max_u32 %0, %1" :: "v"(L_MISC + 4 * C_KMAX), "v"(n2) : "memory");
      }
      VMCNT(0);
      u32 old = 0;
      if (lane == 0) asm volatile("s_waitcnt lgkmcnt(0)\n\tds_add_rtn_u32 %0, %1, %2\n\ts_waitcnt lgkmcnt(0)" : "=&v"(old) : "v"(L_MISC + 4 * C_KVP), "v"(1u) : "memory");
      if (__builtin_amdgcn_readfirstlane(old) == 3) {
        const u32 km = ldsu(L_MISC + 4 * C_KMAX);
        if (lane < 8) __hip_atomic_store((GAS u64*)(ws + WS_KVFLAG + lane * 4096 + tb * 128), (u64)tag | ((u64)km << 32), __ATOMIC_RELAXED, __HIP_MEMORY_SCOPE_AGENT);
      }
      arrive(C_KVD, lane);
      await(C_KVD, 4);
    }

    u32 cva;
    {
      const int hi = lane >= 28, cs = lane - 28 * hi;
      cva = hi * 896 + (cs << 4);
    }
    auto stage_C4 = [&](int qq, int g) {
      const char* csrc = (const char*)((qq >> 1) ? P.Cim : P.Cre) + (size_t)sl * 56 * 896 + (qq & 1) * 448;
      int p0 = 4 * g + 8 * tl; p0 = p0 >= 28 ? p0 - 28 : p0;
      const u32 va = cva ^ ((p0 & 4) << 3), vb = va ^ 16;
      if (lane < 56) {
#pragma unroll
        for (int j = 0; j < 4; ++j) { glds16(csrc + (p0 + j) * 1792, j < 2 ? va : vb, qq * L_PRIV + (p0 + j) * 896); VMTHR(); }
      }
    };
    const int tm = (q ^ sl) & 1, tn = q >> 1;
    const bool valid = 32 * tn + r < 56;
    const int dcol = 56 * sl + (valid ? 32 * tn + r : 55), t0 = tb * 64 + 32 * tm + 4 * h;
    u32x4 cf[2][14];
    auto read_C = [&]() {
#pragma unroll
      for (int t = 0; t < 2; ++t) {
        int row = 32 * (t ^ part) + r; row = row > 55 ? 55 : row;
        const int x = (row >> 2) & 3;
        const u32 a0 = PA + row * 448 + ((h ^ x) << 4), a1 = PA + row * 448 + (((2 + h) ^ x) << 4);
#pragma unroll
        for (int j = 0; j < 14; ++j) cf[t][j] = ldsr128(((j & 1) ? a1 : a0) + 64 * (j >> 1));
      }
#pragma unroll
      for (int j = 0; j < 14; ++j) PIN2(cf[0][j], cf[1][j]);
      LGKM0();
    };
    if (sl == 7) {
      arrive(C_HD, lane);
      await(C_KVL, 4);
      {
        const char* cp = (const char*)(part ? P.Cim : P.Cre) + (tl * 2 + half) * 43904;
#pragma unroll 1
        for (int i = 0; i < 43; ++i) { glds16(cp + (i < 42 ? i * 1024 : 42880), vo, PA + (i & 7) * 1024); VMTHR(); }
        const char* wp = (const char*)P.wo + (tl * 4 + q) * 25088;
#pragma unroll 1
        for (int i = 0; i < 25; ++i) { glds16(wp + (i < 24 ? i * 1024 : 24064), vo, PA + (i & 7) * 1024); VMTHR(); }
        VMCNT(0);
      }
#pragma unroll 1
      for (int g = 0; g < 7; ++g) stage_C4(q, g);
      VMCNT(0);
      read_C();
    } else {
      const int tb2 = tb >> 1, cg = (tb & 1) * 7 + sl, ch = 32 * cg + r;
      const u32 vo1 = (lane ^ 1) * 16;
      float lr, li, cr, ci;
      if (q == 0) {
        asm volatile("s_mov_b32 m0, %2\n\ts_nop 0\n\tglobal_load_lds_ushort %0, %1" :: "v"(ch * 2), "s"(P.lre), "s"(L_AGG) : "memory");
        asm volatile("s_mov_b32 m0, %2\n\ts_nop 0\n\tglobal_load_lds_ushort %0, %1" :: "v"(ch * 2), "s"(P.lim), "s"(L_AGG + 256) : "memory");
        asm volatile("s_mov_b32 m0, %2\n\ts_nop 0\n\tglobal_load_lds_ushort %0, %1" :: "v"(ch * 2), "s"(P.ldt), "s"(L_AGG + 512) : "memory");
      }
      if (lane < 54) {
        const char* bsrc = (const char*)(part ? P.Bim : P.Bre) + (size_t)(cg * 32 + half * 16) * 896;
        const u32 va = (tb2 & 1) ? vo1 : vo, vb = (tb2 & 1) ? vo : vo1;
#pragma unroll
        for (int j = 0; j < 16; ++j) {
          const int i = j ^ (8 * (tb2 & 1));
          glds16(bsrc + i * 896, j < 8 ? va : vb, L_C + (q * 16 + i) * 864);
        }
      }
      const char* xsrc = (const char*)P.Ax + (size_t)(tb2 * 128 + q * 32) * 896;
      const u32 xv0 = (lane >> 3) * 896 + (((lane & 7) ^ (lane >> 4)) << 4), xv1 = xv0 ^ 64;
      const int rho = 16 * ((r >> 2) & 1) + (r & 3) + 4 * (r >> 3), sw = (rho >> 1) & 7;
      u32 xa[4], xb[4];
#pragma unroll
      for (int bb = 0; bb < 4; ++bb) { xa[bb] = q * 4096 + rho * 128 + (((2 * bb + h) ^ sw) << 4); xb[bb] = xa[bb] + 65536; }
      const int xrot = cg % 6;
      u32 ba[7];
#pragma unroll
      for (int c = 0; c < 7; ++c) {
        int kc = c + xrot; kc = c == 6 ? 6 : kc >= 6 ? kc - 6 : kc;
        ba[c] = L_C + r * 864 + ((h ^ ((r >> 3) & 1)) << 4) + 128 * kc;
        PIN(ba[c]);
      }
      PIN4(xa[0], xa[1], xa[2], xa[3]); PIN4(xb[0], xb[1], xb[2], xb[3]);
      constexpr u32 XBLK[7] = {0, 65536, 16384, 32768, 49152, 81920, 0};
      auto xdma = [&](int c) {
#pragma unroll
        for (int j = 0; j < 4; ++j) {
          int kc = c + xrot; kc = c == 6 ? 6 : kc >= 6 ? kc - 6 : kc;
          glds16(xsrc + kc * 128 + j * 7168, (j & 1) ? xv1 : xv0, XBLK[c] + q * 4096 + j * 1024);
        }
      };
      await(C_WQ2, 4);
      xdma(0); xdma(1);
      VMCNT(8);
      arrive(C_BFULL, lane);
      await(C_BFULL, 4);
      __builtin_amdgcn_s_setprio(2);
      {
        const u32 vlre = *(LAS u32*)(size_t)(L_AGG + 4 * lane), vlim = *(LAS u32*)(size_t)(L_AGG + 256 + 4 * lane), vldt = *(LAS u32*)(size_t)(L_AGG + 512 + 4 * lane);
        const float dt = rbf(expf(__uint_as_float(vldt << 16))), a = __uint_as_float(vlre << 16), bb = __uint_as_float(vlim << 16);
        const float er = expf(dt * a);
        float sn, cs;
        sincosf(dt * bb, &sn, &cs);
        lr = er * cs; PIN(lr); li = er * sn;
        const float nr = lr - 1.f, ni = li, den = 2.0f / (a * a + bb * bb);
        cr = (nr * a + ni * bb) * den; PIN(cr); ci = (ni * a - nr * bb) * den;
      }
      float l16r = lr, l16i = li;
#pragma unroll
      for (int i = 0; i < 4; ++i) cmul(l16r, l16i, l16r, l16i, l16r, l16i);
      float l32r, l32i, l128r, l128i;
      cmul(l16r, l16i, l16r, l16i, l32r, l32i);
      cmul(l32r, l32i, l32r, l32i, l128r, l128i);
      cmul(l128r, l128i, l128r, l128i, l128r, l128i);
      float lhr = 1.f, lhi = 0.f;
#pragma unroll 1
      for (int j = 8; j < tb2; ++j) cmul(lhr, lhi, l128r, l128i, lhr, lhi);
      PIN4(lr, li, cr, ci); PIN4(l16r, l16i, l32r, l32i); PIN4(l128r, l128i, lhr, lhi);
      arrive(C_CONST, lane);
      await(C_QDONE, 4);
      glds16((const char*)P.Bre + (size_t)cg * 32 * 896 + 864, r * 896 + 16 * h, 98304 + q * 2048);
      glds16((const char*)P.Bim + (size_t)cg * 32 * 896 + 864, r * 896 + 16 * h, 98304 + q * 2048 + 1024);
      xdma(2);
      const u32 bl = 98304 + q * 2048 + vo;
      f32x16 ur = {}, ui = {};
      PIN2(ur, ui);
      __builtin_amdgcn_s_setprio(0);
      {
        u32x4 xf[28], br[28], bi[28];
#pragma unroll
        for (int ks = 0; ks < 28 + 4; ++ks) {
          if (ks >= 4) {
            const int k = ks - 4;
            ur = MFMA(frag(xf[k]), frag(br[k]), ur);
            ui = MFMA(frag(xf[k]), frag(bi[k]), ui);
            SBAR();
          }
          if (ks < 28) {
            if ((ks & 3) == 0) {
              const int c = ks >> 2;
              if (c == 0) VMCNT(10); else if (c == 1) VMCNT(6); else if (c == 2 || c == 6) VMCNT(0); else VMCNT(4);
              if (c == 2) {
                asm volatile("s_nop 7\n\ts_nop 7" ::: "memory");
                __builtin_amdgcn_s_setprio(2);
                await(C_KVL, 4);
                xdma(3);
                __builtin_amdgcn_s_setprio(0);
              }
              if (c >= 2 && c + 2 <= 6) xdma(c + 2);
            }
            const int c = ks >> 2;
            xf[ks] = XBLK[c] >= 65536 ? ldsr128(xb[ks & 3] + (XBLK[c] - 65536)) : ldsr128(xa[ks & 3] + XBLK[c]);
            br[ks] = ks < 27 ? ldsr128(ba[c] + 32 * (ks & 3)) : ldsr128(bl);
            bi[ks] = ks < 27 ? ldsr128(ba[c] + 27648 + 32 * (ks & 3)) : ldsr128(bl + 1024);
            SBAR();
          }
        }
      }
      MPIN2(ur, ui);
      __builtin_amdgcn_s_setprio(2);
      float ar = 0.f, ai = 0.f;
#pragma unroll
      for (int i = 0; i < 16; ++i) {
        float tr, ti;
        cmul(cr, ci, ur[i], ui[i], tr, ti);
        ur[i] = tr; ui[i] = ti;
        cstep(lr, li, ar, ai, tr, ti);
      }
      const float or_ = swap32(ar, h), oi_ = swap32(ai, h);
      const float lor = h ? or_ : ar, loi = h ? oi_ : ai;
      float a32r = lor, a32i = loi;
      cstep(l16r, l16i, a32r, a32i, h ? ar : or_, h ? ai : oi_);
      await(C_CONST, 4);
      if (q < 3 && h == 0) *(LAS f32x2*)(size_t)(L_AGG + q * 256 + r * 8) = (f32x2){a32r, a32i};
      arrive(C_AGG, lane);
      await(C_AGG, 4);
      f32x2 wagg[3];
#pragma unroll
      for (int j = 0; j < 3; ++j) wagg[j] = *(LAS f32x2*)(size_t)(L_AGG + j * 256 + r * 8);
      const u32 aggb = WS_AGG + cg * 8192;
      if (q == 3 && tb2 < 15) {
        float tr = wagg[0].x, ti = wagg[0].y;
        cstep(l32r, l32i, tr, ti, wagg[1].x, wagg[1].y);
        cstep(l32r, l32i, tr, ti, wagg[2].x, wagg[2].y);
        cstep(l32r, l32i, tr, ti, a32r, a32i);
        __hip_atomic_store((GAS u64*)(ws + aggb + tb2 * 512 + r * 16 + h * 8), (u64)__float_as_uint(h ? ti : tr) | ((u64)tag << 32), __ATOMIC_RELAXED, __HIP_MEMORY_SCOPE_AGENT);
      }
      float hr = 0.f, hi = 0.f;
      int cnext = 2;
      if (q < 3) {
#pragma unroll 1
        for (int g = q; g < 7; g += 3) stage_C4(3, g);
        stage_C4(q, 0); stage_C4(q, 1);
        VMCNT(8);
        arrive(C_C3, lane);
#pragma unroll 1
        for (; cnext < 7 && ldsu(L_MISC + 4 * C_CARRY) == 0; ++cnext) stage_C4(q, cnext);
        await(C_CARRY, 1);
        const f32x2 cv = *(LAS f32x2*)(size_t)(L_CARRY + r * 8);
        hr = cv.x; hi = cv.y;
      } else {
        if (tb2 > 0) {
          u32x4 g[8];
          u32 gv[8];
#pragma unroll
          for (int j = 0; j < 8; ++j) gv[j] = r * 16 + ((8 * h + j < tb2) ? (8 * h + j) * 512 : 0);
#pragma unroll 1
          for (u32 spin = 0; spin < (1u << 17); ++spin) {
            asm volatile("" ::: "memory");
            u32 bad = 0;
#pragma unroll
            for (int j = 0; j < 8; ++j) g[j] = __builtin_amdgcn_raw_buffer_load_b128(rs_ws, gv[j], aggb, SC1);
#pragma unroll
            for (int j = 0; j < 8; ++j) bad |= (g[j].y ^ tag) | (g[j].w ^ tag);
            if (__all(bad == 0)) break;
            __builtin_amdgcn_s_sleep(4);
          }
          asm volatile("" ::: "memory");
          const int nb = tb2 - 8 * h;
#pragma unroll
          for (int j = 0; j < 8; ++j) {
            if (j < tb2) {
              float nr = hr, ni = hi;
              cstep(l128r, l128i, nr, ni, __uint_as_float(g[j].x), __uint_as_float(g[j].z));
              if (j < nb) { hr = nr; hi = ni; }
            }
          }
          float lo_r = swap32(hr, h), lo_i = swap32(hi, h), up_r = lo_r, up_i = lo_i;
          if (h) { up_r = hr; up_i = hi; } else { lo_r = hr; lo_i = hi; }
          cstep(lhr, lhi, lo_r, lo_i, up_r, up_i);
          hr = lo_r; hi = lo_i;
        }
        if (h == 0) *(LAS f32x2*)(size_t)(L_CARRY + r * 8) = (f32x2){hr, hi};
        arrive(C_CARRY, lane);
      }
#pragma unroll
      for (int j = 0; j < 3; ++j)
        if (j < q) cstep(l32r, l32i, hr, hi, wagg[j].x, wagg[j].y);
      if (h) cstep(l16r, l16i, hr, hi, lor, loi);
      u32 pr[8], pi[8];
#pragma unroll
      for (int i = 0; i < 16; i += 2) {
        cstep(lr, li, hr, hi, ur[i], ui[i]);
        const float r0 = hr, i0 = hi;
        cstep(lr, li, hr, hi, ur[i + 1], ui[i + 1]);
        pr[i >> 1] = cvtpk(r0, hr); pi[i >> 1] = cvtpk(-i0, -hi);
      }
      const u32 ta = L_C + q * 5120 + r * 80 + 32 * h;
      ldsw128(ta, (u32x4){pr[0], pr[1], pr[2], pr[3]}); ldsw128(ta + 16, (u32x4){pr[4], pr[5], pr[6], pr[7]});
      ldsw128(ta + 2560, (u32x4){pi[0], pi[1], pi[2], pi[3]}); ldsw128(ta + 2560 + 16, (u32x4){pi[4], pi[5], pi[6], pi[7]});
      LGKM0();
      const u32 tr = L_C + q * 5120 + (8 * h + q4) * 80 + g1 * 32 + 8 * p4;
      const int tbh = 2 * tb2 + (q >> 1);
      const u32 so = WS_HF + ((tbh * 2 + (q & 1)) * 56 + 2 * cg) * 1024 + vo;
#pragma unroll
      for (int pt = 0; pt < 2; ++pt)
#pragma unroll
        for (int s = 0; s < 2; ++s) {
          const u32 a = tr + pt * 2560 + s * 1280;
          __builtin_amdgcn_raw_buffer_store_b128(trfrag(a, a + 320), rs_ws, so + pt * 28 * 1024 + s * 1024, 0, SC1);
        }
      arrive(C_HD, lane);
      VMCNT(0);
      if (lane == 0) gstore_flag(ws, WS_HFLAG + (tbh * 32 + cg * 2 + (q & 1)) * 128, tag);
      if (q < 3) {
#pragma unroll 1
        for (; cnext < 7; ++cnext) stage_C4(q, cnext);
        VMCNT(0);
      } else {
        await(C_C3, 3);
      }
      read_C();
      const char* src = (const char*)P.wo + sl * 128 + q * 112 * 896;
      const u32 wv8 = (lane >> 3) * 896 + (lane & 7) * 16;
#pragma unroll
      for (int j = 0; j < 14; ++j) {
        int i = j + 4 * tl; i = i >= 14 ? i - 14 : i;
        glds16(src + i * 7168, wv8, PA + L_WO_OFF + 1024 * i); VMTHR();
      }
      VMCNT(0);
      arrive(C_WO, lane);
    }
    if (q == 0) { poll_words(ws, WS_HFLAG + tb * 4096, 28, tag, lane); arrive(C_HF, lane); }
    else await(C_HF, 1);
    {
      f32x16 y[4];
      const u32 so = WS_HF + ((tb * 2 + tm) * 56 + part * 28 + half * 14) * 1024, so1 = WS_HF + ((tb * 2 + (tm ^ 1)) * 56 + part * 28 + half * 14) * 1024;
      u32x4 hf[28];
#define LDH(j) __builtin_amdgcn_raw_buffer_load_b128(rs_ws, vo + (((j) % 14) & 3) * 1024, ((j) < 14 ? so : so1) + (((j) % 14) >> 2) * 4096, SC1)
#pragma unroll
      for (int j = 0; j < HWIN; ++j) hf[j] = LDH(j);
      SBAR();
      const f32x16 zero = {};
      __builtin_amdgcn_s_setprio(0);
#pragma unroll
      for (int j = 0; j < 28; ++j) {
        const int t = j / 14, k = j % 14;
        y[t] = MFMA(frag(hf[j]), frag(cf[0][k]), k == 0 ? zero : y[t]);
        y[2 + t] = MFMA(frag(hf[j]), frag(cf[1][k]), k == 0 ? zero : y[2 + t]);
        SBAR();
        if (j + HWIN < 28) { hf[j + HWIN] = LDH(j + HWIN); SBAR(); }
      }
      u16 xs[16];
      {
        const auto rs_ax = __builtin_amdgcn_make_buffer_rsrc((void*)P.Ax, 0, 2048 * 896, 0x00020000);
        const u32 xv = (t0 * 448 + dcol) * 2;
#pragma unroll
        for (int i = 0; i < 16; ++i) xs[i] = __builtin_amdgcn_raw_buffer_load_b16(rs_ax, xv + (i & 3) * 896, (i >> 2) * 7168, 0);
      }
      const u16 dsk16 = ((const GAS u16*)P.Dsk)[dcol];
      MPIN4(y[0], y[1], y[2], y[3]);
      __builtin_amdgcn_s_setprio(2);
#pragma unroll
      for (int t = 1; t < 4; ++t) {
#pragma unroll
        for (int g = 0; g < 4; ++g) ldsw128(PA + (t - 1) * 4096 + g * 1024 + vo, f4(y[t], g));
      }
      arrive(C_Y, lane);
      await(C_Y, 4);
      f32x16 own = y[0];
#pragma unroll
      for (int t = 1; t < 4; ++t) {
#pragma unroll
        for (int g = 0; g < 4; ++g) add4(own, g, ldsr128((q ^ t) * L_PRIV + (t - 1) * 4096 + g * 1024 + vo));
      }
      if (valid) {
        const u32 ov = (t0 * 896 + dcol) * 4;
#pragma unroll
        for (int i = 0; i < 16; ++i) {
          float sk = bf2f(xs[i]) * bf2f(dsk16);
          PIN(sk);
          __builtin_amdgcn_raw_buffer_store_b32(__float_as_uint(own[i] + rbf(sk)), rs_out, ov, ((i & 3) + 8 * (i >> 2)) * 3584, NT);
        }
      }
    }
    if (tb == 31 && sl == 0 && q == 0) {
      await<0>(C_KV, 1);
      if (lane == 0) gstore_flag(ws, WS_EPOCH, tag);
    }
  }
}

extern "C" void kernel_launch(void* const* d_in, const int* in_sizes, int n_in, void* d_out, int out_size, void* d_ws, size_t ws_size,
                              hipStream_t stream) {
  Params P;
  P.Ax = (const u16*)d_in[0]; P.lre = (const u16*)d_in[1]; P.lim = (const u16*)d_in[2]; P.Bre = (const u16*)d_in[3];
  P.Bim = (const u16*)d_in[4]; P.Cre = (const u16*)d_in[5]; P.Cim = (const u16*)d_in[6]; P.ldt = (const u16*)d_in[7];
  P.Dsk = (const u16*)d_in[8]; P.Bx = (const u16*)d_in[9]; P.wq = (const u16*)d_in[10]; P.wk = (const u16*)d_in[11];
  P.wv = (const u16*)d_in[12]; P.wo = (const u16*)d_in[13];
  P.out = (float*)d_out; P.ws = (char*)d_ws;
  (void)hipFuncSetAttribute((const void*)fused_kernel, hipFuncAttributeMaxDynamicSharedMemorySize, LDS_BYTES);
  fused_kernel<<<256, 512, LDS_BYTES, stream>>>(P);
}
```

```cpp
#define CPF 1
#include <hip/hip_runtime.h>

typedef unsigned short u16;
typedef unsigned int u32;
typedef unsigned long long u64;
using bf16x8 = __attribute__((ext_vector_type(8))) short;
using f32x16 = __attribute__((ext_vector_type(16))) float;
using u32x4 = __attribute__((ext_vector_type(4))) unsigned;
using u32x2 = __attribute__((ext_vector_type(2))) unsigned;
typedef short v4i16_t __attribute__((ext_vector_type(4)));
typedef __bf16 bf16x2_t __attribute__((ext_vector_type(2)));
typedef float f32x2 __attribute__((ext_vector_type(2)));
#define LAS __attribute__((address_space(3)))
#define GAS __attribute__((address_space(1)))
#define DI __device__ __forceinline__
#define MFMA(a, b, c) __builtin_amdgcn_mfma_f32_32x32x16_bf16((a), (b), (c), 0, 0, 0)
#define SBAR() __builtin_amdgcn_sched_barrier(0)
#define PIN(x) asm volatile("" : "+v"(x))
#define PIN2(a, b) asm volatile("" : "+v"(a), "+v"(b))
#define PIN4(a, b, c, d) asm volatile("" : "+v"(a), "+v"(b), "+v"(c), "+v"(d))
#define MPIN(x) asm volatile("s_nop 7\n\ts_nop 7" : "+v"(x))
#define MPIN2(a, b) asm volatile("s_nop 7\n\ts_nop 7" : "+v"(a), "+v"(b))
#define MPIN4(a, b, c, d) asm volatile("s_nop 7\n\ts_nop 7" : "+v"(a), "+v"(b), "+v"(c), "+v"(d))
#define VMCNT(n) asm volatile("s_waitcnt vmcnt(" #n ")" ::: "memory")
#define LGKM0() asm volatile("s_waitcnt lgkmcnt(0)" ::: "memory")
#define EX(x) __builtin_amdgcn_exp2f(x)

constexpr int SC1 = 16;
constexpr int NT = 2;
constexpr int KV_DEPTH = 4;
constexpr int HWIN = 8;

constexpr u32 WS_EPOCH = 0;
constexpr u32 WS_HFLAG = 7u << 20;
constexpr u32 WS_OFLAG = WS_HFLAG + 131072;
constexpr u32 WS_KVFLAG = WS_OFLAG + 131072;
constexpr u32 WS_AGG = 32768;
constexpr u32 WS_KVF = WS_AGG + 131072;
constexpr u32 WS_OF = WS_KVF + 262144;
constexpr u32 WS_HF = WS_OF + 2097152;
constexpr u32 WS_END = WS_KVFLAG + 32768;

constexpr u32 L_XB = 0;
constexpr u32 L_WQ = 57344;
constexpr u32 L_C = 107520;
constexpr u32 L_MISC = L_C + 55296;
constexpr u32 L_AGG = L_MISC + 128;
constexpr u32 L_KN = L_MISC + 896;
constexpr u32 L_CARRY = L_C + 20480;
constexpr u32 L_PRIV = 26880;
constexpr u32 L_WO_OFF = 12288;
constexpr u32 L_QX = L_WQ;
constexpr u32 LDS_BYTES = 163840;
enum { C_WQ = 0, C_Q2, C_QDONE, C_BFULL, C_AGG, C_WO, C_O, C_MAX, C_P, C_Y, C_FB, C_KVP, C_KMAX, C_KV, C_OF, C_HF, C_WQ2, C_HD, C_KVL, C_KVD, C_C3, C_CARRY, C_CONST, C_EPOCH = 31 };

struct Params {
  const u16 *Ax, *lre, *lim, *Bre, *Bim, *Cre, *Cim, *ldt, *Dsk, *Bx, *wq, *wk, *wv, *wo;
  float* out;
  char* ws;
};

DI float bf2f(u16 v) { return __uint_as_float(((u32)v) << 16); }
DI u32 cvtpk(float a, float b) { return __builtin_bit_cast(u32, __builtin_convertvector((f32x2){a, b}, bf16x2_t)); }
DI float rbf(float a) { return __uint_as_float(cvtpk(a, 0.f) << 16); }
DI bf16x8 frag(u32x4 v) { return __builtin_bit_cast(bf16x8, v); }
DI void glds16(const void* sbase, u32 voff, u32 lds_base) {
  asm volatile("s_mov_b32 m0, %2\n\ts_nop 0\n\tglobal_load_lds_dwordx4 %0, %1" :: "v"(voff), "s"(sbase), "s"(lds_base) : "memory");
}
DI u32x4 ldsr128(u32 a) { return *(const LAS u32x4*)(size_t)a; }
DI void ldsw128(u32 a, u32x4 v) { *(LAS u32x4*)(size_t)a = v; }
DI u32 ldsu(u32 a) { return (u32)__builtin_amdgcn_readfirstlane(*(volatile LAS u32*)(size_t)a); }
DI u32x2 ldstr(u32 a) { return __builtin_bit_cast(u32x2, __builtin_amdgcn_ds_read_tr16_b64_v4i16((LAS v4i16_t*)(size_t)a)); }
DI u32x4 trfrag(u32 a0, u32 a1) { u32x2 lo = ldstr(a0), hi = ldstr(a1); return (u32x4){lo.x, lo.y, hi.x, hi.y}; }
DI u32x4 f4(const f32x16& v, int g) {
  return (u32x4){__float_as_uint(v[4 * g]), __float_as_uint(v[4 * g + 1]), __float_as_uint(v[4 * g + 2]), __float_as_uint(v[4 * g + 3])};
}
DI void add4(f32x16& v, int g, u32x4 a) {
  v[4 * g] += __uint_as_float(a.x); v[4 * g + 1] += __uint_as_float(a.y); v[4 * g + 2] += __uint_as_float(a.z); v[4 * g + 3] += __uint_as_float(a.w);
}
DI void arrive(int c, int lane) {
  asm volatile("s_waitcnt lgkmcnt(0)" ::: "memory");
  if (lane == 0) asm volatile("ds_add_u32 %0, %1" :: "v"(L_MISC + 4 * c), "v"(1u) : "memory");
}
template <int SL = 1> DI void await(int c, u32 target) {
#pragma unroll 1
  for (u32 spin = 0; spin < (1u << 22); ++spin) {
    asm volatile("" ::: "memory");
    if (ldsu(L_MISC + 4 * c) >= target) break;
    if (SL) __builtin_amdgcn_s_sleep(SL);
  }
  asm volatile("" ::: "memory");
}
DI float swap32(float v, bool hi) {
  auto r = __builtin_amdgcn_permlane32_swap(__float_as_uint(v), __float_as_uint(v), false, false);
  return __uint_as_float(hi ? r[0] : r[1]);
}
DI float wave_max_nonneg(float v) {
  u32 x = __float_as_uint(v);
  x = max(x, (u32)__builtin_amdgcn_update_dpp(0, (int)x, 0x111, 0xf, 0xf, false));
  x = max(x, (u32)__builtin_amdgcn_update_dpp(0, (int)x, 0x112, 0xf, 0xf, false));
  x = max(x, (u32)__builtin_amdgcn_update_dpp(0, (int)x, 0x114, 0xf, 0xf, false));
  x = max(x, (u32)__builtin_amdgcn_update_dpp(0, (int)x, 0x118, 0xf, 0xf, false));
  x = max(x, (u32)__builtin_amdgcn_update_dpp(0, (int)x, 0x142, 0xa, 0xf, false));
  x = max(x, (u32)__builtin_amdgcn_update_dpp(0, (int)x, 0x143, 0xc, 0xf, false));
  return __uint_as_float((u32)__builtin_amdgcn_readlane((int)x, 63));
}
DI void gstore_flag(char* ws, u32 off, u32 v) { __hip_atomic_store((GAS u32*)(ws + off), v, __ATOMIC_RELAXED, __HIP_MEMORY_SCOPE_AGENT); }
DI void poll_words(char* ws, u32 off, int n, u32 tag, int lane) {
  GAS u32* p = (GAS u32*)(ws + off) + (lane < n ? lane : 0) * 32;
  u32 a = __hip_atomic_load(p, __ATOMIC_RELAXED, __HIP_MEMORY_SCOPE_AGENT);
#pragma unroll 1
  for (u32 spin = 0; spin < (1u << 16); ++spin) {
    __builtin_amdgcn_s_sleep(2);
    u32 c = __hip_atomic_load(p, __ATOMIC_RELAXED, __HIP_MEMORY_SCOPE_AGENT);
    SBAR();
    if (__all(a == tag)) break;
    __builtin_amdgcn_s_sleep(2);
    a = __hip_atomic_load(p, __ATOMIC_RELAXED, __HIP_MEMORY_SCOPE_AGENT);
    SBAR();
    if (__all(c == tag)) break;
  }
  asm volatile("" ::: "memory");
}
DI float poll_kv(char* ws, int rep, u32 tag, int lane) {
  GAS u64* p = (GAS u64*)(ws + WS_KVFLAG + rep * 4096) + (lane & 31) * 16;
  u64 a = __hip_atomic_load(p, __ATOMIC_RELAXED, __HIP_MEMORY_SCOPE_AGENT), g = 0;
#pragma unroll 1
  for (u32 spin = 0; spin < (1u << 16); ++spin) {
    __builtin_amdgcn_s_sleep(2);
    u64 c = __hip_atomic_load(p, __ATOMIC_RELAXED, __HIP_MEMORY_SCOPE_AGENT);
    SBAR();
    g = a;
    if (__all((u32)a == tag)) break;
    __builtin_amdgcn_s_sleep(2);
    a = __hip_atomic_load(p, __ATOMIC_RELAXED, __HIP_MEMORY_SCOPE_AGENT);
    SBAR();
    g = c;
    if (__all((u32)c == tag)) break;
  }
  asm volatile("" ::: "memory");
  return __uint_as_float((u32)(g >> 32));
}
DI void cmul(float ar, float ai, float br, float bi, float& cr, float& ci) {
  float x, y;
  asm("v_mul_f32 %0, %2, %4\n\tv_mul_f32 %1, %2, %5\n\tv_fma_f32 %0, -%3, %5, %0\n\tv_fma_f32 %1, %3, %4, %1"
      : "=&v"(x), "=&v"(y) : "v"(ar), "v"(ai), "v"(br), "v"(bi));
  cr = x; ci = y;
}
DI void cstep(float ar, float ai, float& hr, float& hi, float ur, float ui) {
  float x, y;
  asm("v_fma_f32 %0, -%3, %5, %6\n\tv_fma_f32 %1, %3, %4, %7\n\tv_fma_f32 %0, %2, %4, %0\n\tv_fma_f32 %1, %2, %5, %1"
      : "=&v"(x), "=&v"(y) : "v"(ar), "v"(ai), "v"(hr), "v"(hi), "v"(ur), "v"(ui));
  hr = x; hi = y;
}
DI void set_pad(u32x4& f, float m, int h) {
  const float a = rbf(-m), b = rbf(-m - a), c = rbf(-m - a - b);
  if (h) { f.z = cvtpk(a, b); f.w = cvtpk(c, 0.f); }
}

#define VMTHR() VMCNT(4)

template <bool W_IS_A, int WROW, bool MID>
DI f32x16 proj_tile(int row, int h, u32 wa, int lane) {
  const int sw = (row >> 1) & 7;
  u32 xa[4];
#pragma unroll
  for (int bb = 0; bb < 4; ++bb) xa[bb] = L_XB + row * 128 + (((2 * bb + h) ^ sw) << 4);
  u32x4 xf[28], wf[28];
  f32x16 acc = {};
#pragma unroll
  for (int ks = 0; ks < 28 + 5; ++ks) {
    if (ks >= 5) {
      const int k = ks - 5;
      acc = W_IS_A ? MFMA(frag(wf[k]), frag(xf[k]), acc) : MFMA(frag(xf[k]), frag(wf[k]), acc);
      SBAR();
    }
    if (ks < 28) {
      if (MID && ks == 14) {
        asm volatile("s_nop 7\n\ts_nop 7\n\ts_waitcnt vmcnt(0) lgkmcnt(0)" ::: "memory");
        if (lane == 0) asm volatile("ds_add_u32 %0, %1" :: "v"(L_MISC + 4 * C_WQ2), "v"(1u) : "memory");
        await<0>(C_WQ2, 4);
      }
      xf[ks] = ldsr128(xa[ks & 3] + 8192 * (ks >> 2));
      wf[ks] = trfrag(wa + ks * 16 * WROW, wa + ks * 16 * WROW + 4 * WROW);
      SBAR();
    }
  }
  MPIN(acc);
  return acc;
}

__global__ void __launch_bounds__(512, 2) fused_kernel(Params P) {
  extern __shared__ __attribute__((aligned(16))) char lds_dyn[];
  asm volatile("" : "+s"(P.Ax), "+s"(P.lre), "+s"(P.lim), "+s"(P.Bre), "+s"(P.Bim), "+s"(P.Cre), "+s"(P.Cim), "+s"(P.ldt));
  asm volatile("" : "+s"(P.Dsk), "+s"(P.Bx), "+s"(P.wq), "+s"(P.wk), "+s"(P.wv), "+s"(P.wo), "+s"(P.out), "+s"(P.ws));
  const int tid = threadIdx.x, lane = tid & 63, wv = __builtin_amdgcn_readfirstlane(tid >> 6);
  const int r = lane & 31, h = lane >> 5;
  const int b = blockIdx.x;
  const int tb = (b & 7) * 4 + (b >> 6), sl = (b >> 3) & 7;
  char* ws = P.ws;
  const int tl = b >> 6;
  if (tid == 0) asm volatile("s_mov_b32 m0, %2\n\ts_nop 0\n\tglobal_load_lds_dword %0, %1 sc1" :: "v"(0), "s"(ws), "s"(L_MISC + 4 * C_EPOCH) : "memory");
  const u32 vo = lane * 16;
  if (CPF && sl != 7 && wv >= 5) {
    const int jj = (b >> 3) - tl;
    const char* p = wv == 7 ? (const char*)(jj < 14 ? P.wk : P.wv) + (jj % 14) * 1792 : (const char*)P.wq + jj * 14336 + (wv - 5) * 7168;
    if (lane < (wv == 7 ? 14 : 56))
      asm volatile("s_mov_b32 m0, %2\n\ts_nop 0\n\tglobal_load_lds_dword %0, %1" :: "v"(lane * 128), "s"(p), "s"(L_AGG + (wv - 5) * 256) : "memory");
  }
  {
    const int row = 8 * wv + (lane >> 3);
    const u32 xv = row * 896 + (((lane & 7) ^ ((row >> 1) & 7)) << 4);
    const char* src = (const char*)P.Bx + (size_t)tb * 64 * 896;
#pragma unroll
    for (int j = 0; j < 7; ++j) {
      int c = j + sl; c = c >= 7 ? c - 7 : c;
      glds16(src + c * 128, xv, L_XB + c * 8192 + wv * 1024);
    }
  }
  if (tid < 31) *(LAS u32*)(size_t)(L_MISC + 4 * tid) = 0;
  const auto rs_ws = __builtin_amdgcn_make_buffer_rsrc((void*)ws, 0, WS_END, 0x00020000);
  const auto rs_out = __builtin_amdgcn_make_buffer_rsrc((void*)P.out, 0, 2048 * 896 * 4, 0x00020000);
  const int g1 = (lane >> 4) & 1, q4 = (lane >> 2) & 3, p4 = lane & 3;
  const int l7 = lane / 7;
  const u32 vo7 = l7 * 896 + (lane - 7 * l7) * 16;

  if (wv < 4) {
    const int w = wv;
    const char* wqsrc = (const char*)P.wq + sl * 112 + w * 7 * 7168;
    auto stage_wq = [&](int hf) {
      if (lane < 56) {
#pragma unroll
        for (int j = 0; j < 7; ++j) {
          int i = j + 2 * tl; i = (i >= 7 ? i - 7 : i) + 28 * hf;
          glds16(wqsrc + i * 7168, vo7, L_WQ + (w * 7 + i) * 896);
        }
      }
    };
    if (sl != 7) {
      stage_wq(0); stage_wq(1);
      asm volatile("s_waitcnt vmcnt(14) lgkmcnt(0)\n\ts_barrier" ::: "memory");
    } else {
      VMCNT(0);
      stage_wq(0); stage_wq(1);
      asm volatile("s_waitcnt lgkmcnt(0)\n\ts_barrier" ::: "memory");
    }
    const u32 tag = ldsu(L_MISC + 4 * C_EPOCH) + 1u;
    VMCNT(7);
    arrive(C_WQ, lane);
    await<0>(C_WQ, 4);
    {
      f32x16 acc = proj_tile<true, 112, true>(32 * (w & 1) + r, h, L_WQ + (8 * h + q4) * 112 + (w >> 1) * 56 + g1 * 32 + 8 * p4, lane);
      const float qs = 0.27264060f;
      float n2 = 0.f;
#pragma unroll
      for (int i = 0; i < 16; ++i) { acc[i] *= qs; if (i < 12) n2 = fmaf(acc[i], acc[i], n2); else n2 = h ? n2 : fmaf(acc[i], acc[i], n2); }
      n2 += swap32(n2, h);
      u32x4 f0 = {cvtpk(acc[0], acc[1]), cvtpk(acc[2], acc[3]), cvtpk(acc[4], acc[5]), cvtpk(acc[6], acc[7])};
      u32x4 f1 = {cvtpk(acc[8], acc[9]), cvtpk(acc[10], acc[11]), cvtpk(acc[12], acc[13]), cvtpk(acc[14], acc[15])};
      if (h) { f1.z = 0; f1.w = __float_as_uint(__builtin_amdgcn_sqrtf(n2)); }
      ldsw128(L_QX + w * 2048 + vo, f0);
      ldsw128(L_QX + w * 2048 + 1024 + vo, f1);
      arrive(C_Q2, lane);
    }
    u32x4 qf[4][2];
    await<0>(C_Q2, 4);
#pragma unroll
    for (int t = 0; t < 4; ++t) { const u32 a = L_QX + ((w + t) & 3) * 2048 + vo; qf[t][0] = ldsr128(a); qf[t][1] = ldsr128(a + 1024); }
    arrive(C_QDONE, lane);
    if (w == 0) {
      const float kn = wave_max_nonneg(poll_kv(ws, b & 7, tag, lane));
      if (lane == 0) *(LAS float*)(size_t)L_KN = kn;
      arrive(C_KV, lane);
    } else {
      await<0>(C_KV, 1);
    }
    const u32 kvbase = WS_KVF + w * 65536;
    const int rot = b >> 3;
#define KVOFF(it) (kvbase + ((((it) + rot) & 15) << 12))
#define LDK(it, s) __builtin_amdgcn_raw_buffer_load_b128(rs_ws, vo + 1024 * (s), KVOFF(it), SC1)
#define LDV(it, s) __builtin_amdgcn_raw_buffer_load_b128(rs_ws, vo + 2048 + 1024 * (s), KVOFF(it), SC1)
#define NEXT(x) asm volatile("s_add_u32 %0, %0, 0x1000\n\ts_and_b32 %0, %0, 0xf000" : "+s"(x) :: "scc")
#define LDK2(s_) __builtin_amdgcn_raw_buffer_load_b128(rs_kv, vo + 1024 * (s_), koff, SC1)
#define LDV2(s_) __builtin_amdgcn_raw_buffer_load_b128(rs_kv, vo + 2048 + 1024 * (s_), voff, SC1)
    const auto rs_kv = __builtin_amdgcn_make_buffer_rsrc((void*)(ws + kvbase), 0, 65536, 0x00020000);
    f32x16 o[4];
    for (int attempt = 0; attempt < 2; ++attempt) {
      u32x4 kf[KV_DEPTH][2], vf[KV_DEPTH][2];
#pragma unroll
      for (int d = 0; d < KV_DEPTH; ++d) {
        kf[d][0] = LDK(d, 0); kf[d][1] = LDK(d, 1);
        if (d < KV_DEPTH - 1) { vf[d][0] = LDV(d, 0); vf[d][1] = LDV(d, 1); }
      }
      u32 koff = ((KV_DEPTH - 1 + rot) & 15) << 12, voff = ((KV_DEPTH - 2 + rot) & 15) << 12;
      SBAR();
      if (attempt == 0) {
        arrive(C_KVL, lane);
        const float kmax = __builtin_amdgcn_sqrtf(*(LAS float*)(size_t)L_KN);
#pragma unroll
        for (int t = 0; t < 4; ++t) set_pad(qf[t][1], __uint_as_float(qf[t][1].w) * kmax, h);
      }
#pragma unroll
      for (int t = 0; t < 4; ++t) { PIN(qf[t][0]); PIN(qf[t][1]); }
      SBAR();
      const f32x16 zero = {};
      f32x16 sc = MFMA(frag(kf[0][0]), frag(qf[0][0]), zero);
      sc = MFMA(frag(kf[0][1]), frag(qf[0][1]), sc);
      float e0 = 0.f, e1 = 0.f, e2 = 0.f, e3 = 0.f;
      u32x4 p1 = {0, 0, 0, 0};
#pragma unroll
      for (int it = 0; it < 16; ++it) {
        const int d = it % KV_DEPTH, dprev = (it + KV_DEPTH - 1) % KV_DEPTH;
#pragma unroll
        for (int qi = 0; qi < 4; ++qi) {
          const int dn = qi == 3 ? (it + 1) % KV_DEPTH : d, qn = (qi + 1) & 3;
          const int dp = qi == 0 ? dprev : d, qp = (qi + 3) & 3;
          const bool last = it == 15 && qi == 3;
          f32x16 sn; u32x4 p0;
          float c0, c1, c2, c3;
          if (!last) sn = MFMA(frag(kf[dn][0]), frag(qf[qn][0]), zero);
          SBAR();
          if (qi == 0) NEXT(voff);
          if (qi == 2) NEXT(koff);
          p1.z = cvtpk(e0, e1); p1.w = cvtpk(e2, e3); PIN(p1); SBAR();
          c0 = EX(sc[0]); c1 = EX(sc[1]); c2 = EX(sc[2]); c3 = EX(sc[3]); PIN4(c0, c1, c2, c3); SBAR();
          if (it > 0 || qi > 0) o[qp] = MFMA(frag(vf[dp][1]), frag(p1), o[qp]);
          SBAR();
          if (qi < 2 && it - 1 + KV_DEPTH < 16) { vf[dprev][qi] = LDV2(qi); SBAR(); }
          p0.x = cvtpk(c0, c1); p0.y = cvtpk(c2, c3); PIN2(p0.x, p0.y); SBAR();
          c0 = EX(sc[4]); c1 = EX(sc[5]);
          c2 = EX(sc[6]); c3 = EX(sc[7]); PIN4(c0, c1, c2, c3); SBAR();
          if (!last) sn = MFMA(frag(kf[dn][1]), frag(qf[qn][1]), sn);
          SBAR();
          if (qi == 2 && it + KV_DEPTH < 16) { kf[d][0] = LDK2(0); SBAR(); }
          p0.z = cvtpk(c0, c1); p0.w = cvtpk(c2, c3); PIN(p0); SBAR();
          c0 = EX(sc[8]); c1 = EX(sc[9]);
          c2 = EX(sc[10]); c3 = EX(sc[11]); PIN4(c0, c1, c2, c3); SBAR();
          o[qi] = MFMA(frag(vf[d][0]), frag(p0), it == 0 ? zero : o[qi]); SBAR();
          if (qi == 2 && it + KV_DEPTH < 16) { kf[d][1] = LDK2(1); SBAR(); }
          p1.x = cvtpk(c0, c1); p1.y = cvtpk(c2, c3); PIN2(p1.x, p1.y); SBAR();
          e0 = EX(sc[12]); e1 = EX(sc[13]);
          e2 = EX(sc[14]); e3 = EX(sc[15]); PIN4(e0, e1, e2, e3); SBAR();
          if (!last) sc = sn;
        }
      }
      p1.z = cvtpk(e0, e1); p1.w = cvtpk(e2, e3);
      o[3] = MFMA(frag(vf[15 % KV_DEPTH][1]), frag(p1), o[3]);
      MPIN4(o[0], o[1], o[2], o[3]);
      u32 lv;
      asm volatile("v_mbcnt_lo_u32_b32 %0, -1, 0\n\tv_mbcnt_hi_u32_b32 %0, -1, %0\n\tv_lshlrev_b32 %0, 4, %0" : "=v"(lv));
      if (attempt == 0) {
        bool bad = false;
#pragma unroll
        for (int t = 0; t < 4; ++t) bad |= !(o[t][12] >= 0x1p-80f && o[t][12] <= 0x1p100f);
        if (__any(bad && lv >= 512)) { if (lv == 0) *(LAS u32*)(size_t)(L_MISC + 4 * C_FB) = 1u; }
        await(C_HD, 4);
      }
#pragma unroll
      for (int t = 1; t < 4; ++t) {
#pragma unroll
        for (int g = 0; g < 4; ++g) ldsw128(L_C + w * 12288 + (t - 1) * 4096 + g * 1024 + lv, f4(o[t], g));
      }
      arrive(C_O, lv);
      await<0>(C_O, 4 * (attempt + 1));
      const u32 fb = attempt == 0 ? ldsu(L_MISC + 4 * C_FB) : 0u;
      if (!fb) break;
      {
        float mx[4];
#pragma unroll
        for (int t = 0; t < 4; ++t) { mx[t] = -3.0e38f; if (lv >= 512) { qf[t][1].z = 0; qf[t][1].w = 0; } }
#pragma unroll 1
        for (int it = 0; it < 16; ++it) {
          const u32x4 k0 = __builtin_amdgcn_raw_buffer_load_b128(rs_kv, lv, it << 12, SC1), k1 = __builtin_amdgcn_raw_buffer_load_b128(rs_kv, lv + 1024, it << 12, SC1);
#pragma unroll
          for (int t = 0; t < 4; ++t) {
            f32x16 s = MFMA(frag(k0), frag(qf[t][0]), zero);
            s = MFMA(frag(k1), frag(qf[t][1]), s);
#pragma unroll
            for (int i = 0; i < 16; ++i) mx[t] = fmaxf(mx[t], s[i]);
          }
        }
        const u32 ma = L_C + 49152;
#pragma unroll
        for (int t = 0; t < 4; ++t) { mx[t] = fmaxf(mx[t], swap32(mx[t], lv >= 512)); *(LAS float*)(size_t)(ma + (w * 4 + ((w + t) & 3)) * 256 + (lv >> 2)) = mx[t]; }
        arrive(C_MAX, lv);
        await(C_MAX, 4);
#pragma unroll
        for (int t = 0; t < 4; ++t) {
          float m = -3.0e38f;
#pragma unroll
          for (int ww = 0; ww < 4; ++ww) m = fmaxf(m, *(LAS float*)(size_t)(ma + (ww * 4 + ((w + t) & 3)) * 256 + (lv >> 2)));
          set_pad(qf[t][1], m, lv >= 512);
        }
      }
    }
    {
    int lane;
    asm volatile("v_mbcnt_lo_u32_b32 %0, -1, 0\n\tv_mbcnt_hi_u32_b32 %0, -1, %0" : "=v"(lane));
    const int r = lane & 31, h = lane >> 5, g1 = (lane >> 4) & 1, q4 = (lane >> 2) & 3, p4 = lane & 3;
    const u32 vo = lane * 16;
    {
      f32x16 own = o[0];
#pragma unroll
      for (int pp = 1; pp < 4; ++pp) {
        const u32 a = L_C + ((w + pp) & 3) * 12288 + (3 - pp) * 4096 + vo;
#pragma unroll
        for (int g = 0; g < 4; ++g) add4(own, g, ldsr128(a + g * 1024));
      }
      const float other = swap32(own[12], h);
      const float inv = __builtin_amdgcn_rcpf(h ? own[12] : other);
      u32x4 f0 = {cvtpk(own[0] * inv, own[1] * inv), cvtpk(own[2] * inv, own[3] * inv), cvtpk(own[4] * inv, own[5] * inv), cvtpk(own[6] * inv, own[7] * inv)};
      u32x4 f1 = {cvtpk(own[8] * inv, own[9] * inv), cvtpk(own[10] * inv, own[11] * inv), cvtpk(own[12] * inv, own[13] * inv), cvtpk(own[14] * inv, own[15] * inv)};
      if (h) { f1.z = 0; f1.w = 0; }
      const u32 so = WS_OF + ((tb * 2 + (w & 1)) * 16 + 2 * sl + (w >> 1)) * 2048 + vo;
      __builtin_amdgcn_raw_buffer_store_b128(f0, rs_ws, so, 0, SC1);
      __builtin_amdgcn_raw_buffer_store_b128(f1, rs_ws, so + 1024, 0, SC1);
    }
    {
      const int tm = w & 1, kh = w >> 1;
      VMCNT(0);
      if (lane == 0) gstore_flag(ws, WS_OFLAG + (tb * 32 + sl * 4 + w) * 128, tag);
      if (sl == 7) return;
      await(C_WO, 4);
      u32x4 wf[16][2];
      const u32 wa = 2 * kh * L_PRIV + L_WO_OFF + (4 * h + q4) * 128 + g1 * 32 + 8 * p4;
      const u32 wa0 = wa + 64 * kh, wa1 = wa + 64 * (1 - kh);
      const u32 hz = h ? 0 : 1024;
#pragma unroll
      for (int i = 0; i < 16; ++i) {
        const u32 a = (i >> 3) * L_PRIV + (28 * ((i >> 1) & 3) + 16 * (i & 1)) * 128;
        if ((i & 7) == 7) { wf[i][0] = trfrag(wa0 + a, wa0 + hz + a); wf[i][1] = trfrag(wa1 + a, wa1 + hz + a); }
        else { wf[i][0] = trfrag(wa0 + a, wa0 + a + 1024); wf[i][1] = trfrag(wa1 + a, wa1 + a + 1024); }
      }
#pragma unroll
      for (int i = 0; i < 16; ++i) PIN2(wf[i][0], wf[i][1]);
      if (w == 0) { poll_words(ws, WS_OFLAG + tb * 4096, 32, tag, lane); arrive(C_OF, lane); }
      else await<0>(C_OF, 1);
      u32x4 of[16];
      const u32 so = WS_OF + ((tb * 2 + tm) * 16 + 8 * kh) * 2048;
#pragma unroll
      for (int i = 0; i < 16; ++i) of[i] = __builtin_amdgcn_raw_buffer_load_b128(rs_ws, vo + (i & 3) * 1024, so + (i >> 2) * 4096, SC1);
      SBAR();
      f32x16 a0 = {}, a1 = {};
#pragma unroll
      for (int i = 0; i < 16; ++i) {
        a0 = MFMA(frag(of[i]), frag(wf[i][0]), a0);
        a1 = MFMA(frag(of[i]), frag(wf[i][1]), a1);
      }
      MPIN2(a0, a1);
      const f32x16 snd = a1;
      f32x16 own = a0;
      const u32 aw = L_C + w * 4096 + vo, ar = L_C + (w ^ 2) * 4096 + vo;
#pragma unroll
      for (int g = 0; g < 4; ++g) ldsw128(aw + g * 1024, f4(snd, g));
      arrive(C_P, lane);
      await<0>(C_P, 4);
#pragma unroll
      for (int g = 0; g < 4; ++g) add4(own, g, ldsr128(ar + g * 1024));
      {
        const u32 ov = ((tb * 64 + 32 * tm + 4 * h) * 896 + 448 + 64 * sl + 32 * kh + r) * 4;
#pragma unroll
        for (int i = 0; i < 16; ++i) __builtin_amdgcn_raw_buffer_store_b32(__float_as_uint(rbf(own[i])), rs_out, ov, ((i & 3) + 8 * (i >> 2)) * 3584, NT);
      }
    }
    }
  } else {
    if (sl == 7) __builtin_amdgcn_s_setprio(2);
    const int q = wv - 4;
    const u32 PA = q * L_PRIV;
    const int part = q >> 1, half = q & 1;
    if (sl == 7 && lane < 56) {
      const char* src = (const char*)((q >> 1) ? P.wv : P.wk) + (q & 1) * 14 * 896;
#pragma unroll
      for (int j = 0; j < 14; ++j) {
        int i = j + 4 * tl; i = i >= 14 ? i - 14 : i;
        glds16(src + i * 896, vo, L_C + (q * 14 + i) * 896);
      }
    }
    asm volatile("s_waitcnt vmcnt(0) lgkmcnt(0)\n\ts_barrier" ::: "memory");
    const u32 tag = ldsu(L_MISC + 4 * C_EPOCH) + 1u;
    if (sl == 7) {
      const int kind = q >> 1, ktm = q & 1;
      const u32 wa = L_C + kind * 25088 + (8 * h + q4) * 56 + g1 * 32 + 8 * p4;
      f32x16 acc;
      if (kind == 0) acc = proj_tile<true, 56, false>(32 * ktm + r, h, wa, lane);
      else acc = proj_tile<false, 56, false>(32 * ktm + r, h, wa, lane);
      u32x4 f0 = {cvtpk(acc[0], acc[1]), cvtpk(acc[2], acc[3]), cvtpk(acc[4], acc[5]), cvtpk(acc[6], acc[7])};
      u32x4 f1 = {cvtpk(acc[8], acc[9]), cvtpk(acc[10], acc[11]), cvtpk(acc[12], acc[13]), cvtpk(acc[14], acc[15])};
      if (kind == 0) {
        if (h) { f1.z = 0x3F803F80u; f1.w = 0x00003F80u; }
      } else if (r >= 28) {
        const u32 c = r == 28 ? 0x3F803F80u : 0u;
        f0 = (u32x4){c, c, c, c}; f1 = f0;
      }
      const u32 so = WS_KVF + (tb * 2 + ktm) * 4096 + kind * 2048 + vo;
      __builtin_amdgcn_raw_buffer_store_b128(f0, rs_ws, so, 0, SC1);
      __builtin_amdgcn_raw_buffer_store_b128(f1, rs_ws, so + 1024, 0, SC1);
      SBAR();
      if (kind == 0) {
        float n2 = 0.f;
#pragma unroll
        for (int i = 0; i < 16; ++i) { if (i < 12) n2 = fmaf(acc[i], acc[i], n2); else n2 = h ? n2 : fmaf(acc[i], acc[i], n2); }
        n2 += swap32(n2, h);
        n2 = wave_max_nonneg(n2);
        if (lane == 0) asm volatile("ds_max_u32 %0, %1" :: "v"(L_MISC + 4 * C_KMAX), "v"(n2) : "memory");
      }
      VMCNT(0);
      u32 old = 0;
      if (lane == 0) asm volatile("s_waitcnt lgkmcnt(0)\n\tds_add_rtn_u32 %0, %1, %2\n\ts_waitcnt lgkmcnt(0)" : "=&v"(old) : "v"(L_MISC + 4 * C_KVP), "v"(1u) : "memory");
      if (__builtin_amdgcn_readfirstlane(old) == 3) {
        const u32 km = ldsu(L_MISC + 4 * C_KMAX);
        if (lane < 8) __hip_atomic_store((GAS u64*)(ws + WS_KVFLAG + lane * 4096 + tb * 128), (u64)tag | ((u64)km << 32), __ATOMIC_RELAXED, __HIP_MEMORY_SCOPE_AGENT);
      }
      arrive(C_KVD, lane);
      await(C_KVD, 4);
    }

    u32 cva;
    {
      const int hi = lane >= 28, cs = lane - 28 * hi;
      cva = hi * 896 + (cs << 4);
    }
    auto stage_C4 = [&](int qq, int g) {
      const char* csrc = (const char*)((qq >> 1) ? P.Cim : P.Cre) + (size_t)sl * 56 * 896 + (qq & 1) * 448;
      int p0 = 4 * g + 8 * tl; p0 = p0 >= 28 ? p0 - 28 : p0;
      const u32 va = cva ^ ((p0 & 4) << 3), vb = va ^ 16;
      if (lane < 56) {
#pragma unroll
        for (int j = 0; j < 4; ++j) { glds16(csrc + (p0 + j) * 1792, j < 2 ? va : vb, qq * L_PRIV + (p0 + j) * 896); VMTHR(); }
      }
    };
    const int tm = (q ^ sl) & 1, tn = q >> 1;
    const bool valid = 32 * tn + r < 56;
    const int dcol = 56 * sl + (valid ? 32 * tn + r : 55), t0 = tb * 64 + 32 * tm + 4 * h;
    u32x4 cf[2][14];
    auto read_C = [&]() {
#pragma unroll
      for (int t = 0; t < 2; ++t) {
        int row = 32 * (t ^ part) + r; row = row > 55 ? 55 : row;
        const int x = (row >> 2) & 3;
        const u32 a0 = PA + row * 448 + ((h ^ x) << 4), a1 = PA + row * 448 + (((2 + h) ^ x) << 4);
#pragma unroll
        for (int j = 0; j < 14; ++j) cf[t][j] = ldsr128(((j & 1) ? a1 : a0) + 64 * (j >> 1));
      }
#pragma unroll
      for (int j = 0; j < 14; ++j) PIN2(cf[0][j], cf[1][j]);
      LGKM0();
    };
    if (sl == 7) {
      arrive(C_HD, lane);
      await(C_KVL, 4);
      {
        const char* cp = (const char*)(part ? P.Cim : P.Cre) + (tl * 2 + half) * 43904;
#pragma unroll 1
        for (int i = 0; i < 43; ++i) { glds16(cp + (i < 42 ? i * 1024 : 42880), vo, PA + (i & 7) * 1024); VMTHR(); }
        const char* wp = (const char*)P.wo + (tl * 4 + q) * 25088;
#pragma unroll 1
        for (int i = 0; i < 25; ++i) { glds16(wp + (i < 24 ? i * 1024 : 24064), vo, PA + (i & 7) * 1024); VMTHR(); }
        VMCNT(0);
      }
#pragma unroll 1
      for (int g = 0; g < 7; ++g) stage_C4(q, g);
      VMCNT(0);
      read_C();
    } else {
      const int tb2 = tb >> 1, cg = (tb & 1) * 7 + sl, ch = 32 * cg + r;
      const u32 vo1 = (lane ^ 1) * 16;
      float lr, li, cr, ci;
      if (q == 0) {
        asm volatile("s_mov_b32 m0, %2\n\ts_nop 0\n\tglobal_load_lds_ushort %0, %1" :: "v"(ch * 2), "s"(P.lre), "s"(L_AGG) : "memory");
        asm volatile("s_mov_b32 m0, %2\n\ts_nop 0\n\tglobal_load_lds_ushort %0, %1" :: "v"(ch * 2), "s"(P.lim), "s"(L_AGG + 256) : "memory");
        asm volatile("s_mov_b32 m0, %2\n\ts_nop 0\n\tglobal_load_lds_ushort %0, %1" :: "v"(ch * 2), "s"(P.ldt), "s"(L_AGG + 512) : "memory");
      }
      if (lane < 54) {
        const char* bsrc = (const char*)(part ? P.Bim : P.Bre) + (size_t)(cg * 32 + half * 16) * 896;
        const u32 va = (tb2 & 1) ? vo1 : vo, vb = (tb2 & 1) ? vo : vo1;
#pragma unroll
        for (int j = 0; j < 16; ++j) {
          const int i = j ^ (8 * (tb2 & 1));
          glds16(bsrc + i * 896, j < 8 ? va : vb, L_C + (q * 16 + i) * 864);
        }
      }
      const char* xsrc = (const char*)P.Ax + (size_t)(tb2 * 128 + q * 32) * 896;
      const u32 xv0 = (lane >> 3) * 896 + (((lane & 7) ^ (lane >> 4)) << 4), xv1 = xv0 ^ 64;
      const int rho = 16 * ((r >> 2) & 1) + (r & 3) + 4 * (r >> 3), sw = (rho >> 1) & 7;
      u32 xa[4], xb[4];
#pragma unroll
      for (int bb = 0; bb < 4; ++bb) { xa[bb] = q * 4096 + rho * 128 + (((2 * bb + h) ^ sw) << 4); xb[bb] = xa[bb] + 65536; }
      const int xrot = cg % 6;
      u32 ba[7];
#pragma unroll
      for (int c = 0; c < 7; ++c) {
        int kc = c + xrot; kc = c == 6 ? 6 : kc >= 6 ? kc - 6 : kc;
        ba[c] = L_C + r * 864 + ((h ^ ((r >> 3) & 1)) << 4) + 128 * kc;
        PIN(ba[c]);
      }
      PIN4(xa[0], xa[1], xa[2], xa[3]); PIN4(xb[0], xb[1], xb[2], xb[3]);
      constexpr u32 XBLK[7] = {0, 65536, 16384, 32768, 49152, 81920, 0};
      auto xdma = [&](int c) {
#pragma unroll
        for (int j = 0; j < 4; ++j) {
          int kc = c + xrot; kc = c == 6 ? 6 : kc >= 6 ? kc - 6 : kc;
          glds16(xsrc + kc * 128 + j * 7168, (j & 1) ? xv1 : xv0, XBLK[c] + q * 4096 + j * 1024);
        }
      };
      await(C_WQ2, 4);
      xdma(0); xdma(1);
      VMCNT(8);
      arrive(C_BFULL, lane);
      await(C_BFULL, 4);
      __builtin_amdgcn_s_setprio(2);
      {
        const u32 vlre = *(LAS u32*)(size_t)(L_AGG + 4 * lane), vlim = *(LAS u32*)(size_t)(L_AGG + 256 + 4 * lane), vldt = *(LAS u32*)(size_t)(L_AGG + 512 + 4 * lane);
        const float dt = rbf(expf(__uint_as_float(vldt << 16))), a = __uint_as_float(vlre << 16), bb = __uint_as_float(vlim << 16);
        const float er = expf(dt * a);
        float sn, cs;
        sincosf(dt * bb, &sn, &cs);
        lr = er * cs; PIN(lr); li = er * sn;
        const float nr = lr - 1.f, ni = li, den = 2.0f / (a * a + bb * bb);
        cr = (nr * a + ni * bb) * den; PIN(cr); ci = (ni * a - nr * bb) * den;
      }
      float l16r = lr, l16i = li;
#pragma unroll
      for (int i = 0; i < 4; ++i) cmul(l16r, l16i, l16r, l16i, l16r, l16i);
      float l32r, l32i, l128r, l128i;
      cmul(l16r, l16i, l16r, l16i, l32r, l32i);
      cmul(l32r, l32i, l32r, l32i, l128r, l128i);
      cmul(l128r, l128i, l128r, l128i, l128r, l128i);
      float lhr = 1.f, lhi = 0.f;
#pragma unroll 1
      for (int j = 8; j < tb2; ++j) cmul(lhr, lhi, l128r, l128i, lhr, lhi);
      PIN4(lr, li, cr, ci); PIN4(l16r, l16i, l32r, l32i); PIN4(l128r, l128i, lhr, lhi);
      arrive(C_CONST, lane);
      await(C_QDONE, 4);
      glds16((const char*)P.Bre + (size_t)cg * 32 * 896 + 864, r * 896 + 16 * h, 98304 + q * 2048);
      glds16((const char*)P.Bim + (size_t)cg * 32 * 896 + 864, r * 896 + 16 * h, 98304 + q * 2048 + 1024);
      xdma(2);
      const u32 bl = 98304 + q * 2048 + vo;
      f32x16 ur = {}, ui = {};
      PIN2(ur, ui);
      __builtin_amdgcn_s_setprio(0);
      {
        u32x4 xf[28], br[28], bi[28];
#pragma unroll
        for (int ks = 0; ks < 28 + 4; ++ks) {
          if (ks >= 4) {
            const int k = ks - 4;
            ur = MFMA(frag(xf[k]), frag(br[k]), ur);
            ui = MFMA(frag(xf[k]), frag(bi[k]), ui);
            SBAR();
          }
          if (ks < 28) {
            if ((ks & 3) == 0) {
              const int c = ks >> 2;
              if (c == 0) VMCNT(10); else if (c == 1) VMCNT(6); else if (c == 2 || c == 6) VMCNT(0); else VMCNT(4);
              if (c == 2) {
                asm volatile("s_nop 7\n\ts_nop 7" ::: "memory");
                __builtin_amdgcn_s_setprio(2);
                await(C_KVL, 4);
                xdma(3);
                __builtin_amdgcn_s_setprio(0);
              }
              if (c >= 2 && c + 2 <= 6) xdma(c + 2);
            }
            const int c = ks >> 2;
            xf[ks] = XBLK[c] >= 65536 ? ldsr128(xb[ks & 3] + (XBLK[c] - 65536)) : ldsr128(xa[ks & 3] + XBLK[c]);
            br[ks] = ks < 27 ? ldsr128(ba[c] + 32 * (ks & 3)) : ldsr128(bl);
            bi[ks] = ks < 27 ? ldsr128(ba[c] + 27648 + 32 * (ks & 3)) : ldsr128(bl + 1024);
            SBAR();
          }
        }
      }
      MPIN2(ur, ui);
      __builtin_amdgcn_s_setprio(2);
      float ar = 0.f, ai = 0.f;
#pragma unroll
      for (int i = 0; i < 16; ++i) {
        float tr, ti;
        cmul(cr, ci, ur[i], ui[i], tr, ti);
        ur[i] = tr; ui[i] = ti;
        cstep(lr, li, ar, ai, tr, ti);
      }
      const float or_ = swap32(ar, h), oi_ = swap32(ai, h);
      const float lor = h ? or_ : ar, loi = h ? oi_ : ai;
      float a32r = lor, a32i = loi;
      cstep(l16r, l16i, a32r, a32i, h ? ar : or_, h ? ai : oi_);
      await(C_CONST, 4);
      if (q < 3 && h == 0) *(LAS f32x2*)(size_t)(L_AGG + q * 256 + r * 8) = (f32x2){a32r, a32i};
      arrive(C_AGG, lane);
      await(C_AGG, 4);
      f32x2 wagg[3];
#pragma unroll
      for (int j = 0; j < 3; ++j) wagg[j] = *(LAS f32x2*)(size_t)(L_AGG + j * 256 + r * 8);
      const u32 aggb = WS_AGG + cg * 8192;
      if (q == 3 && tb2 < 15) {
        float tr = wagg[0].x, ti = wagg[0].y;
        cstep(l32r, l32i, tr, ti, wagg[1].x, wagg[1].y);
        cstep(l32r, l32i, tr, ti, wagg[2].x, wagg[2].y);
        cstep(l32r, l32i, tr, ti, a32r, a32i);
        __hip_atomic_store((GAS u64*)(ws + aggb + tb2 * 512 + r * 16 + h * 8), (u64)__float_as_uint(h ? ti : tr) | ((u64)tag << 32), __ATOMIC_RELAXED, __HIP_MEMORY_SCOPE_AGENT);
      }
      float hr = 0.f, hi = 0.f;
      int cnext = 2;
      if (q < 3) {
#pragma unroll 1
        for (int g = q; g < 7; g += 3) stage_C4(3, g);
        stage_C4(q, 0); stage_C4(q, 1);
        VMCNT(8);
        arrive(C_C3, lane);
#pragma unroll 1
        for (; cnext < 7 && ldsu(L_MISC + 4 * C_CARRY) == 0; ++cnext) stage_C4(q, cnext);
        await(C_CARRY, 1);
        const f32x2 cv = *(LAS f32x2*)(size_t)(L_CARRY + r * 8);
        hr = cv.x; hi = cv.y;
      } else {
        if (tb2 > 0) {
          u32x4 g[8];
          u32 gv[8];
#pragma unroll
          for (int j = 0; j < 8; ++j) gv[j] = r * 16 + ((8 * h + j < tb2) ? (8 * h + j) * 512 : 0);
#pragma unroll 1
          for (u32 spin = 0; spin < (1u << 17); ++spin) {
            asm volatile("" ::: "memory");
            u32 bad = 0;
#pragma unroll
            for (int j = 0; j < 8; ++j) g[j] = __builtin_amdgcn_raw_buffer_load_b128(rs_ws, gv[j], aggb, SC1);
#pragma unroll
            for (int j = 0; j < 8; ++j) bad |= (g[j].y ^ tag) | (g[j].w ^ tag);
            if (__all(bad == 0)) break;
            __builtin_amdgcn_s_sleep(4);
          }
          asm volatile("" ::: "memory");
          const int nb = tb2 - 8 * h;
#pragma unroll
          for (int j = 0; j < 8; ++j) {
            if (j < tb2) {
              float nr = hr, ni = hi;
              cstep(l128r, l128i, nr, ni, __uint_as_float(g[j].x), __uint_as_float(g[j].z));
              if (j < nb) { hr = nr; hi = ni; }
            }
          }
          float lo_r = swap32(hr, h), lo_i = swap32(hi, h), up_r = lo_r, up_i = lo_i;
          if (h) { up_r = hr; up_i = hi; } else { lo_r = hr; lo_i = hi; }
          cstep(lhr, lhi, lo_r, lo_i, up_r, up_i);
          hr = lo_r; hi = lo_i;
        }
        if (h == 0) *(LAS f32x2*)(size_t)(L_CARRY + r * 8) = (f32x2){hr, hi};
        arrive(C_CARRY, lane);
      }
#pragma unroll
      for (int j = 0; j < 3; ++j)
        if (j < q) cstep(l32r, l32i, hr, hi, wagg[j].x, wagg[j].y);
      if (h) cstep(l16r, l16i, hr, hi, lor, loi);
      u32 pr[8], pi[8];
#pragma unroll
      for (int i = 0; i < 16; i += 2) {
        cstep(lr, li, hr, hi, ur[i], ui[i]);
        const float r0 = hr, i0 = hi;
        cstep(lr, li, hr, hi, ur[i + 1], ui[i + 1]);
        pr[i >> 1] = cvtpk(r0, hr); pi[i >> 1] = cvtpk(-i0, -hi);
      }
      const u32 ta = L_C + q * 5120 + r * 80 + 32 * h;
      ldsw128(ta, (u32x4){pr[0], pr[1], pr[2], pr[3]}); ldsw128(ta + 16, (u32x4){pr[4], pr[5], pr[6], pr[7]});
      ldsw128(ta + 2560, (u32x4){pi[0], pi[1], pi[2], pi[3]}); ldsw128(ta + 2560 + 16, (u32x4){pi[4], pi[5], pi[6], pi[7]});
      LGKM0();
      const u32 tr = L_C + q * 5120 + (8 * h + q4) * 80 + g1 * 32 + 8 * p4;
      const int tbh = 2 * tb2 + (q >> 1);
      const u32 so = WS_HF + ((tbh * 2 + (q & 1)) * 56 + 2 * cg) * 1024 + vo;
#pragma unroll
      for (int pt = 0; pt < 2; ++pt)
#pragma unroll
        for (int s = 0; s < 2; ++s) {
          const u32 a = tr + pt * 2560 + s * 1280;
          __builtin_amdgcn_raw_buffer_store_b128(trfrag(a, a + 320), rs_ws, so + pt * 28 * 1024 + s * 1024, 0, SC1);
        }
      arrive(C_HD, lane);
      VMCNT(0);
      if (lane == 0) gstore_flag(ws, WS_HFLAG + (tbh * 32 + cg * 2 + (q & 1)) * 128, tag);
      if (q < 3) {
#pragma unroll 1
        for (; cnext < 7; ++cnext) stage_C4(q, cnext);
        VMCNT(0);
      } else {
        await(C_C3, 3);
      }
      read_C();
      const char* src = (const char*)P.wo + sl * 128 + q * 112 * 896;
      const u32 wv8 = (lane >> 3) * 896 + (lane & 7) * 16;
#pragma unroll
      for (int j = 0; j < 14; ++j) {
        int i = j + 4 * tl; i = i >= 14 ? i - 14 : i;
        glds16(src + i * 7168, wv8, PA + L_WO_OFF + 1024 * i); VMTHR();
      }
      VMCNT(0);
      arrive(C_WO, lane);
    }
    if (q == 0) { poll_words(ws, WS_HFLAG + tb * 4096, 28, tag, lane); arrive(C_HF, lane); }
    else await(C_HF, 1);
    {
      f32x16 y[4];
      const u32 so = WS_HF + ((tb * 2 + tm) * 56 + part * 28 + half * 14) * 1024, so1 = WS_HF + ((tb * 2 + (tm ^ 1)) * 56 + part * 28 + half * 14) * 1024;
      u32x4 hf[28];
#define LDH(j) __builtin_amdgcn_raw_buffer_load_b128(rs_ws, vo + (((j) % 14) & 3) * 1024, ((j) < 14 ? so : so1) + (((j) % 14) >> 2) * 4096, SC1)
#pragma unroll
      for (int j = 0; j < HWIN; ++j) hf[j] = LDH(j);
      SBAR();
      const f32x16 zero = {};
      __builtin_amdgcn_s_setprio(0);
#pragma unroll
      for (int j = 0; j < 28; ++j) {
        const int t = j / 14, k = j % 14;
        y[t] = MFMA(frag(hf[j]), frag(cf[0][k]), k == 0 ? zero : y[t]);
        y[2 + t] = MFMA(frag(hf[j]), frag(cf[1][k]), k == 0 ? zero : y[2 + t]);
        SBAR();
        if (j + HWIN < 28) { hf[j + HWIN] = LDH(j + HWIN); SBAR(); }
      }
      u16 xs[16];
      {
        const auto rs_ax = __builtin_amdgcn_make_buffer_rsrc((void*)P.Ax, 0, 2048 * 896, 0x00020000);
        const u32 xv = (t0 * 448 + dcol) * 2;
#pragma unroll
        for (int i = 0; i < 16; ++i) xs[i] = __builtin_amdgcn_raw_buffer_load_b16(rs_ax, xv + (i & 3) * 896, (i >> 2) * 7168, 0);
      }
      const u16 dsk16 = ((const GAS u16*)P.Dsk)[dcol];
      MPIN4(y[0], y[1], y[2], y[3]);
      __builtin_amdgcn_s_setprio(2);
#pragma unroll
      for (int t = 1; t < 4; ++t) {
#pragma unroll
        for (int g = 0; g < 4; ++g) ldsw128(PA + (t - 1) * 4096 + g * 1024 + vo, f4(y[t], g));
      }
      arrive(C_Y, lane);
      await(C_Y, 4);
      f32x16 own = y[0];
#pragma unroll
      for (int t = 1; t < 4; ++t) {
#pragma unroll
        for (int g = 0; g < 4; ++g) add4(own, g, ldsr128((q ^ t) * L_PRIV + (t - 1) * 4096 + g * 1024 + vo));
      }
      if (valid) {
        const u32 ov = (t0 * 896 + dcol) * 4;
#pragma unroll
        for (int i = 0; i < 16; ++i) {
          float sk = bf2f(xs[i]) * bf2f(dsk16);
          PIN(sk);
          __builtin_amdgcn_raw_buffer_store_b32(__float_as_uint(own[i] + rbf(sk)), rs_out, ov, ((i & 3) + 8 * (i >> 2)) * 3584, NT);
        }
      }
    }
    if (tb == 31 && sl == 0 && q == 0) {
      await<0>(C_KV, 1);
      if (lane == 0) gstore_flag(ws, WS_EPOCH, tag);
    }
  }
}

extern "C" void kernel_launch(void* const* d_in, const int* in_sizes, int n_in, void* d_out, int out_size, void* d_ws, size_t ws_size,
                              hipStream_t stream) {
  Params P;
  P.Ax = (const u16*)d_in[0]; P.lre = (const u16*)d_in[1]; P.lim = (const u16*)d_in[2]; P.Bre = (const u16*)d_in[3];
  P.Bim = (const u16*)d_in[4]; P.Cre = (const u16*)d_in[5]; P.Cim = (const u16*)d_in[6]; P.ldt = (const u16*)d_in[7];
  P.Dsk = (const u16*)d_in[8]; P.Bx = (const u16*)d_in[9]; P.wq = (const u16*)d_in[10]; P.wk = (const u16*)d_in[11];
  P.wv = (const u16*)d_in[12]; P.wo = (const u16*)d_in[13];
  P.out = (float*)d_out; P.ws = (char*)d_ws;
  (void)hipFuncSetAttribute((const void*)fused_kernel, hipFuncAttributeMaxDynamicSharedMemorySize, LDS_BYTES);
  fused_kernel<<<256, 512, LDS_BYTES, stream>>>(P);
}
```

```cpp
#include <hip/hip_runtime.h>

typedef unsigned short u16;
typedef unsigned int u32;
typedef unsigned long long u64;
using bf16x8 = __attribute__((ext_vector_type(8))) short;
using f32x16 = __attribute__((ext_vector_type(16))) float;
using u32x4 = __attribute__((ext_vector_type(4))) unsigned;
using u32x2 = __attribute__((ext_vector_type(2))) unsigned;
typedef short v4i16_t __attribute__((ext_vector_type(4)));
typedef __bf16 bf16x2_t __attribute__((ext_vector_type(2)));
typedef float f32x2 __attribute__((ext_vector_type(2)));
#define LAS __attribute__((address_space(3)))
#define GAS __attribute__((address_space(1)))
#define DI __device__ __forceinline__
#define MFMA(a, b, c) __builtin_amdgcn_mfma_f32_32x32x16_bf16((a), (b), (c), 0, 0, 0)
#define SBAR() __builtin_amdgcn_sched_barrier(0)
#define PIN(x) asm volatile("" : "+v"(x))
#define PIN2(a, b) asm volatile("" : "+v"(a), "+v"(b))
#define PIN4(a, b, c, d) asm volatile("" : "+v"(a), "+v"(b), "+v"(c), "+v"(d))
#define MPIN(x) asm volatile("s_nop 7\n\ts_nop 7" : "+v"(x))
#define MPIN2(a, b) asm volatile("s_nop 7\n\ts_nop 7" : "+v"(a), "+v"(b))
#define MPIN4(a, b, c, d) asm volatile("s_nop 7\n\ts_nop 7" : "+v"(a), "+v"(b), "+v"(c), "+v"(d))
#define VMCNT(n) asm volatile("s_waitcnt vmcnt(" #n ")" ::: "memory")
#define LGKM0() asm volatile("s_waitcnt lgkmcnt(0)" ::: "memory")
#define EX(x) __builtin_amdgcn_exp2f(x)

constexpr int SC1 = 16;
constexpr int NT = 2;
constexpr int KV_DEPTH = 4;
constexpr int HWIN = 8;

constexpr u32 WS_EPOCH = 0;
constexpr u32 WS_HFLAG = 7u << 20;
constexpr u32 WS_OFLAG = WS_HFLAG + 131072;
constexpr u32 WS_KVFLAG = WS_OFLAG + 131072;
constexpr u32 WS_AGG = 32768;
constexpr u32 WS_KVF = WS_AGG + 131072;
constexpr u32 WS_OF = WS_KVF + 262144;
constexpr u32 WS_HF = WS_OF + 2097152;
constexpr u32 WS_END = WS_KVFLAG + 32768;

constexpr u32 L_XB = 0;
constexpr u32 L_WQ = 57344;
constexpr u32 L_C = 107520;
constexpr u32 L_MISC = L_C + 55296;
constexpr u32 L_AGG = L_MISC + 128;
constexpr u32 L_KN = L_MISC + 896;
constexpr u32 L_CARRY = L_C + 20480;
constexpr u32 L_PRIV = 26880;
constexpr u32 L_WO_OFF = 12288;
constexpr u32 L_QX = L_WQ;
constexpr u32 LDS_BYTES = 163840;
enum { C_WQ = 0, C_Q2, C_QDONE, C_BFULL, C_AGG, C_WO, C_O, C_MAX, C_P, C_Y, C_FB, C_KVP, C_KMAX, C_KV, C_OF, C_HF, C_WQ2, C_HD, C_KVL, C_KVD, C_C3, C_CARRY, C_CONST, C_EPOCH = 31 };

struct Params {
  const u16 *Ax, *lre, *lim, *Bre, *Bim, *Cre, *Cim, *ldt, *Dsk, *Bx, *wq, *wk, *wv, *wo;
  float* out;
  char* ws;
};

DI float bf2f(u16 v) { return __uint_as_float(((u32)v) << 16); }
DI u32 cvtpk(float a, float b) { return __builtin_bit_cast(u32, __builtin_convertvector((f32x2){a, b}, bf16x2_t)); }
DI float rbf(float a) { return __uint_as_float(cvtpk(a, 0.f) << 16); }
DI bf16x8 frag(u32x4 v) { return __builtin_bit_cast(bf16x8, v); }
DI void glds16(const void* sbase, u32 voff, u32 lds_base) {
  asm volatile("s_mov_b32 m0, %2\n\ts_nop 0\n\tglobal_load_lds_dwordx4 %0, %1" :: "v"(voff), "s"(sbase), "s"(lds_base) : "memory");
}
DI u32x4 ldsr128(u32 a) { return *(const LAS u32x4*)(size_t)a; }
DI void ldsw128(u32 a, u32x4 v) { *(LAS u32x4*)(size_t)a = v; }
DI u32 ldsu(u32 a) { return (u32)__builtin_amdgcn_readfirstlane(*(volatile LAS u32*)(size_t)a); }
DI u32x2 ldstr(u32 a) { return __builtin_bit_cast(u32x2, __builtin_amdgcn_ds_read_tr16_b64_v4i16((LAS v4i16_t*)(size_t)a)); }
DI u32x4 trfrag(u32 a0, u32 a1) { u32x2 lo = ldstr(a0), hi = ldstr(a1); return (u32x4){lo.x, lo.y, hi.x, hi.y}; }
DI u32x4 f4(const f32x16& v, int g) {
  return (u32x4){__float_as_uint(v[4 * g]), __float_as_uint(v[4 * g + 1]), __float_as_uint(v[4 * g + 2]), __float_as_uint(v[4 * g + 3])};
}
DI void add4(f32x16& v, int g, u32x4 a) {
  v[4 * g] += __uint_as_float(a.x); v[4 * g + 1] += __uint_as_float(a.y); v[4 * g + 2] += __uint_as_float(a.z); v[4 * g + 3] += __uint_as_float(a.w);
}
DI void arrive(int c, int lane) {
  asm volatile("s_waitcnt lgkmcnt(0)" ::: "memory");
  if (lane == 0) asm volatile("ds_add_u32 %0, %1" :: "v"(L_MISC + 4 * c), "v"(1u) : "memory");
}
template <int SL = 1> DI void await(int c, u32 target) {
#pragma unroll 1
  for (u32 spin = 0; spin < (1u << 22); ++spin) {
    asm volatile("" ::: "memory");
    if (ldsu(L_MISC + 4 * c) >= target) break;
    if (SL) __builtin_amdgcn_s_sleep(SL);
  }
  asm volatile("" ::: "memory");
}
DI float swap32(float v, bool hi) {
  auto r = __builtin_amdgcn_permlane32_swap(__float_as_uint(v), __float_as_uint(v), false, false);
  return __uint_as_float(hi ? r[0] : r[1]);
}
DI float wave_max_nonneg(float v) {
  u32 x = __float_as_uint(v);
  x = max(x, (u32)__builtin_amdgcn_update_dpp(0, (int)x, 0x111, 0xf, 0xf, false));
  x = max(x, (u32)__builtin_amdgcn_update_dpp(0, (int)x, 0x112, 0xf, 0xf, false));
  x = max(x, (u32)__builtin_amdgcn_update_dpp(0, (int)x, 0x114, 0xf, 0xf, false));
  x = max(x, (u32)__builtin_amdgcn_update_dpp(0, (int)x, 0x118, 0xf, 0xf, false));
  x = max(x, (u32)__builtin_amdgcn_update_dpp(0, (int)x, 0x142, 0xa, 0xf, false));
  x = max(x, (u32)__builtin_amdgcn_update_dpp(0, (int)x, 0x143, 0xc, 0xf, false));
  return __uint_as_float((u32)__builtin_amdgcn_readlane((int)x, 63));
}
DI void gstore_flag(char* ws, u32 off, u32 v) { __hip_atomic_store((GAS u32*)(ws + off), v, __ATOMIC_RELAXED, __HIP_MEMORY_SCOPE_AGENT); }
DI void poll_words(char* ws, u32 off, int n, u32 tag, int lane) {
  GAS u32* p = (GAS u32*)(ws + off) + (lane < n ? lane : 0) * 32;
  u32 a = __hip_atomic_load(p, __ATOMIC_RELAXED, __HIP_MEMORY_SCOPE_AGENT);
#pragma unroll 1
  for (u32 spin = 0; spin < (1u << 16); ++spin) {
    __builtin_amdgcn_s_sleep(2);
    u32 c = __hip_atomic_load(p, __ATOMIC_RELAXED, __HIP_MEMORY_SCOPE_AGENT);
    SBAR();
    if (__all(a == tag)) break;
    __builtin_amdgcn_s_sleep(2);
    a = __hip_atomic_load(p, __ATOMIC_RELAXED, __HIP_MEMORY_SCOPE_AGENT);
    SBAR();
    if (__all(c == tag)) break;
  }
  asm volatile("" ::: "memory");
}
DI float poll_kv(char* ws, int rep, u32 tag, int lane) {
  GAS u64* p = (GAS u64*)(ws + WS_KVFLAG + rep * 4096) + (lane & 31) * 16;
  u64 a = __hip_atomic_load(p, __ATOMIC_RELAXED, __HIP_MEMORY_SCOPE_AGENT), g = 0;
#pragma unroll 1
  for (u32 spin = 0; spin < (1u << 16); ++spin) {
    __builtin_amdgcn_s_sleep(2);
    u64 c = __hip_atomic_load(p, __ATOMIC_RELAXED, __HIP_MEMORY_SCOPE_AGENT);
    SBAR();
    g = a;
    if (__all((u32)a == tag)) break;
    __builtin_amdgcn_s_sleep(2);
    a = __hip_atomic_load(p, __ATOMIC_RELAXED, __HIP_MEMORY_SCOPE_AGENT);
    SBAR();
    g = c;
    if (__all((u32)c == tag)) break;
  }
  asm volatile("" ::: "memory");
  return __uint_as_float((u32)(g >> 32));
}
DI void cmul(float ar, float ai, float br, float bi, float& cr, float& ci) {
  float x, y;
  asm("v_mul_f32 %0, %2, %4\n\tv_mul_f32 %1, %2, %5\n\tv_fma_f32 %0, -%3, %5, %0\n\tv_fma_f32 %1, %3, %4, %1"
      : "=&v"(x), "=&v"(y) : "v"(ar), "v"(ai), "v"(br), "v"(bi));
  cr = x; ci = y;
}
DI void cstep(float ar, float ai, float& hr, float& hi, float ur, float ui) {
  float x, y;
  asm("v_fma_f32 %0, -%3, %5, %6\n\tv_fma_f32 %1, %3, %4, %7\n\tv_fma_f32 %0, %2, %4, %0\n\tv_fma_f32 %1, %2, %5, %1"
      : "=&v"(x), "=&v"(y) : "v"(ar), "v"(ai), "v"(hr), "v"(hi), "v"(ur), "v"(ui));
  hr = x; hi = y;
}
DI void set_pad(u32x4& f, float m, int h) {
  const float a = rbf(-m), b = rbf(-m - a), c = rbf(-m - a - b);
  if (h) { f.z = cvtpk(a, b); f.w = cvtpk(c, 0.f); }
}

#define VMTHR() VMCNT(4)

template <bool W_IS_A, int WROW, bool MID>
DI f32x16 proj_tile(int row, int h, u32 wa, int lane) {
  const int sw = (row >> 1) & 7;
  u32 xa[4];
#pragma unroll
  for (int bb = 0; bb < 4; ++bb) xa[bb] = L_XB + row * 128 + (((2 * bb + h) ^ sw) << 4);
  u32x4 xf[28], wf[28];
  f32x16 acc = {};
#pragma unroll
  for (int ks = 0; ks < 28 + 5; ++ks) {
    if (ks >= 5) {
      const int k = ks - 5;
      acc = W_IS_A ? MFMA(frag(wf[k]), frag(xf[k]), acc) : MFMA(frag(xf[k]), frag(wf[k]), acc);
      SBAR();
    }
    if (ks < 28) {
      if (MID && ks == 14) {
        asm volatile("s_nop 7\n\ts_nop 7\n\ts_waitcnt vmcnt(0) lgkmcnt(0)" ::: "memory");
        if (lane == 0) asm volatile("ds_add_u32 %0, %1" :: "v"(L_MISC + 4 * C_WQ2), "v"(1u) : "memory");
        await<0>(C_WQ2, 4);
      }
      xf[ks] = ldsr128(xa[ks & 3] + 8192 * (ks >> 2));
      wf[ks] = trfrag(wa + ks * 16 * WROW, wa + ks * 16 * WROW + 4 * WROW);
      SBAR();
    }
  }
  MPIN(acc);
  return acc;
}

__global__ void __launch_bounds__(512, 2) fused_kernel(Params P) {
  extern __shared__ __attribute__((aligned(16))) char lds_dyn[];
  asm volatile("" : "+s"(P.Ax), "+s"(P.lre), "+s"(P.lim), "+s"(P.Bre), "+s"(P.Bim), "+s"(P.Cre), "+s"(P.Cim), "+s"(P.ldt));
  asm volatile("" : "+s"(P.Dsk), "+s"(P.Bx), "+s"(P.wq), "+s"(P.wk), "+s"(P.wv), "+s"(P.wo), "+s"(P.out), "+s"(P.ws));
  const int tid = threadIdx.x, lane = tid & 63, wv = __builtin_amdgcn_readfirstlane(tid >> 6);
  const int r = lane & 31, h = lane >> 5;
  const int b = blockIdx.x;
  const int tb = (b & 7) * 4 + (b >> 6), sl = (b >> 3) & 7;
  char* ws = P.ws;
  const int tl = b >> 6;
  if (tid == 0) asm volatile("s_mov_b32 m0, %2\n\ts_nop 0\n\tglobal_load_lds_dword %0, %1 sc1" :: "v"(0), "s"(ws), "s"(L_MISC + 4 * C_EPOCH) : "memory");
  const u32 vo = lane * 16;
  {
    const int row = 8 * wv + (lane >> 3);
    const u32 xv = row * 896 + (((lane & 7) ^ ((row >> 1) & 7)) << 4);
    const char* src = (const char*)P.Bx + (size_t)tb * 64 * 896;
#pragma unroll
    for (int j = 0; j < 7; ++j) {
      int c = j + sl; c = c >= 7 ? c - 7 : c;
      glds16(src + c * 128, xv, L_XB + c * 8192 + wv * 1024);
    }
  }
  if (tid < 31) *(LAS u32*)(size_t)(L_MISC + 4 * tid) = 0;
  const auto rs_ws = __builtin_amdgcn_make_buffer_rsrc((void*)ws, 0, WS_END, 0x00020000);
  const auto rs_out = __builtin_amdgcn_make_buffer_rsrc((void*)P.out, 0, 2048 * 896 * 4, 0x00020000);
  const int g1 = (lane >> 4) & 1, q4 = (lane >> 2) & 3, p4 = lane & 3;
  const int l7 = lane / 7;
  const u32 vo7 = l7 * 896 + (lane - 7 * l7) * 16;

  if (wv < 4) {
    const int w = wv;
    const char* wqsrc = (const char*)P.wq + sl * 112 + w * 7 * 7168;
    auto stage_wq = [&](int hf) {
      if (lane < 56) {
#pragma unroll
        for (int j = 0; j < 7; ++j) {
          int i = j + 2 * tl; i = (i >= 7 ? i - 7 : i) + 28 * hf;
          glds16(wqsrc + i * 7168, vo7, L_WQ + (w * 7 + i) * 896);
        }
      }
    };
    if (sl != 7) {
      stage_wq(0); stage_wq(1);
      asm volatile("s_waitcnt vmcnt(14) lgkmcnt(0)\n\ts_barrier" ::: "memory");
    } else {
      VMCNT(0);
      stage_wq(0); stage_wq(1);
      asm volatile("s_waitcnt lgkmcnt(0)\n\ts_barrier" ::: "memory");
    }
    const u32 tag = ldsu(L_MISC + 4 * C_EPOCH) + 1u;
    VMCNT(7);
    arrive(C_WQ, lane);
    await<0>(C_WQ, 4);
    {
      f32x16 acc = proj_tile<true, 112, true>(32 * (w & 1) + r, h, L_WQ + (8 * h + q4) * 112 + (w >> 1) * 56 + g1 * 32 + 8 * p4, lane);
      const float qs = 0.27264060f;
      float n2 = 0.f;
#pragma unroll
      for (int i = 0; i < 16; ++i) { acc[i] *= qs; if (i < 12) n2 = fmaf(acc[i], acc[i], n2); else n2 = h ? n2 : fmaf(acc[i], acc[i], n2); }
      n2 += swap32(n2, h);
      u32x4 f0 = {cvtpk(acc[0], acc[1]), cvtpk(acc[2], acc[3]), cvtpk(acc[4], acc[5]), cvtpk(acc[6], acc[7])};
      u32x4 f1 = {cvtpk(acc[8], acc[9]), cvtpk(acc[10], acc[11]), cvtpk(acc[12], acc[13]), cvtpk(acc[14], acc[15])};
      if (h) { f1.z = 0; f1.w = __float_as_uint(__builtin_amdgcn_sqrtf(n2)); }
      ldsw128(L_QX + w * 2048 + vo, f0);
      ldsw128(L_QX + w * 2048 + 1024 + vo, f1);
      arrive(C_Q2, lane);
    }
    u32x4 qf[4][2];
    await<0>(C_Q2, 4);
#pragma unroll
    for (int t = 0; t < 4; ++t) { const u32 a = L_QX + ((w + t) & 3) * 2048 + vo; qf[t][0] = ldsr128(a); qf[t][1] = ldsr128(a + 1024); }
    arrive(C_QDONE, lane);
    if (w == 0) {
      const float kn = wave_max_nonneg(poll_kv(ws, b & 7, tag, lane));
      if (lane == 0) *(LAS float*)(size_t)L_KN = kn;
      arrive(C_KV, lane);
    } else {
      await<0>(C_KV, 1);
    }
    const u32 kvbase = WS_KVF + w * 65536;
    const int rot = b >> 3;
#define KVOFF(it) (kvbase + ((((it) + rot) & 15) << 12))
#define LDK(it, s) __builtin_amdgcn_raw_buffer_load_b128(rs_ws, vo + 1024 * (s), KVOFF(it), SC1)
#define LDV(it, s) __builtin_amdgcn_raw_buffer_load_b128(rs_ws, vo + 2048 + 1024 * (s), KVOFF(it), SC1)
#define NEXT(x) asm volatile("s_add_u32 %0, %0, 0x1000\n\ts_and_b32 %0, %0, 0xf000" : "+s"(x) :: "scc")
#define LDK2(s_) __builtin_amdgcn_raw_buffer_load_b128(rs_kv, vo + 1024 * (s_), koff, SC1)
#define LDV2(s_) __builtin_amdgcn_raw_buffer_load_b128(rs_kv, vo + 2048 + 1024 * (s_), voff, SC1)
    const auto rs_kv = __builtin_amdgcn_make_buffer_rsrc((void*)(ws + kvbase), 0, 65536, 0x00020000);
    f32x16 o[4];
    for (int attempt = 0; attempt < 2; ++attempt) {
      u32x4 kf[KV_DEPTH][2], vf[KV_DEPTH][2];
#pragma unroll
      for (int d = 0; d < KV_DEPTH; ++d) {
        kf[d][0] = LDK(d, 0); kf[d][1] = LDK(d, 1);
        if (d < KV_DEPTH - 1) { vf[d][0] = LDV(d, 0); vf[d][1] = LDV(d, 1); }
      }
      u32 koff = ((KV_DEPTH - 1 + rot) & 15) << 12, voff = ((KV_DEPTH - 2 + rot) & 15) << 12;
      SBAR();
      if (attempt == 0) {
        arrive(C_KVL, lane);
        const float kmax = __builtin_amdgcn_sqrtf(*(LAS float*)(size_t)L_KN);
#pragma unroll
        for (int t = 0; t < 4; ++t) set_pad(qf[t][1], __uint_as_float(qf[t][1].w) * kmax, h);
      }
#pragma unroll
      for (int t = 0; t < 4; ++t) { PIN(qf[t][0]); PIN(qf[t][1]); }
      SBAR();
      const f32x16 zero = {};
      f32x16 sc = MFMA(frag(kf[0][0]), frag(qf[0][0]), zero);
      sc = MFMA(frag(kf[0][1]), frag(qf[0][1]), sc);
      float e0 = 0.f, e1 = 0.f, e2 = 0.f, e3 = 0.f;
      u32x4 p1 = {0, 0, 0, 0};
#pragma unroll
      for (int it = 0; it < 16; ++it) {
        const int d = it % KV_DEPTH, dprev = (it + KV_DEPTH - 1) % KV_DEPTH;
#pragma unroll
        for (int qi = 0; qi < 4; ++qi) {
          const int dn = qi == 3 ? (it + 1) % KV_DEPTH : d, qn = (qi + 1) & 3;
          const int dp = qi == 0 ? dprev : d, qp = (qi + 3) & 3;
          const bool last = it == 15 && qi == 3;
          f32x16 sn; u32x4 p0;
          float c0, c1, c2, c3;
          if (!last) sn = MFMA(frag(kf[dn][0]), frag(qf[qn][0]), zero);
          SBAR();
          if (qi == 0) NEXT(voff);
          if (qi == 2) NEXT(koff);
          p1.z = cvtpk(e0, e1); p1.w = cvtpk(e2, e3); PIN(p1); SBAR();
          c0 = EX(sc[0]); c1 = EX(sc[1]); c2 = EX(sc[2]); c3 = EX(sc[3]); PIN4(c0, c1, c2, c3); SBAR();
          if (it > 0 || qi > 0) o[qp] = MFMA(frag(vf[dp][1]), frag(p1), o[qp]);
          SBAR();
          if (qi < 2 && it - 1 + KV_DEPTH < 16) { vf[dprev][qi] = LDV2(qi); SBAR(); }
          p0.x = cvtpk(c0, c1); p0.y = cvtpk(c2, c3); PIN2(p0.x, p0.y); SBAR();
          c0 = EX(sc[4]); c1 = EX(sc[5]);
          c2 = EX(sc[6]); c3 = EX(sc[7]); PIN4(c0, c1, c2, c3); SBAR();
          if (!last) sn = MFMA(frag(kf[dn][1]), frag(qf[qn][1]), sn);
          SBAR();
          if (qi == 2 && it + KV_DEPTH < 16) { kf[d][0] = LDK2(0); SBAR(); }
          p0.z = cvtpk(c0, c1); p0.w = cvtpk(c2, c3); PIN(p0); SBAR();
          c0 = EX(sc[8]); c1 = EX(sc[9]);
          c2 = EX(sc[10]); c3 = EX(sc[11]); PIN4(c0, c1, c2, c3); SBAR();
          o[qi] = MFMA(frag(vf[d][0]), frag(p0), it == 0 ? zero : o[qi]); SBAR();
          if (qi == 2 && it + KV_DEPTH < 16) { kf[d][1] = LDK2(1); SBAR(); }
          p1.x = cvtpk(c0, c1); p1.y = cvtpk(c2, c3); PIN2(p1.x, p1.y); SBAR();
          e0 = EX(sc[12]); e1 = EX(sc[13]);
          e2 = EX(sc[14]); e3 = EX(sc[15]); PIN4(e0, e1, e2, e3); SBAR();
          if (!last) sc = sn;
        }
      }
      p1.z = cvtpk(e0, e1); p1.w = cvtpk(e2, e3);
      o[3] = MFMA(frag(vf[15 % KV_DEPTH][1]), frag(p1), o[3]);
      MPIN4(o[0], o[1], o[2], o[3]);
      u32 lv;
      asm volatile("v_mbcnt_lo_u32_b32 %0, -1, 0\n\tv_mbcnt_hi_u32_b32 %0, -1, %0\n\tv_lshlrev_b32 %0, 4, %0" : "=v"(lv));
      if (attempt == 0) {
        bool bad = false;
#pragma unroll
        for (int t = 0; t < 4; ++t) bad |= !(o[t][12] >= 0x1p-80f && o[t][12] <= 0x1p100f);
        if (__any(bad && lv >= 512)) { if (lv == 0) *(LAS u32*)(size_t)(L_MISC + 4 * C_FB) = 1u; }
        await(C_HD, 4);
      }
#pragma unroll
      for (int t = 1; t < 4; ++t) {
#pragma unroll
        for (int g = 0; g < 4; ++g) ldsw128(L_C + w * 12288 + (t - 1) * 4096 + g * 1024 + lv, f4(o[t], g));
      }
      arrive(C_O, lv);
      await<0>(C_O, 4 * (attempt + 1));
      const u32 fb = attempt == 0 ? ldsu(L_MISC + 4 * C_FB) : 0u;
      if (!fb) break;
      {
        float mx[4];
#pragma unroll
        for (int t = 0; t < 4; ++t) { mx[t] = -3.0e38f; if (lv >= 512) { qf[t][1].z = 0; qf[t][1].w = 0; } }
#pragma unroll 1
        for (int it = 0; it < 16; ++it) {
          const u32x4 k0 = __builtin_amdgcn_raw_buffer_load_b128(rs_kv, lv, it << 12, SC1), k1 = __builtin_amdgcn_raw_buffer_load_b128(rs_kv, lv + 1024, it << 12, SC1);
#pragma unroll
          for (int t = 0; t < 4; ++t) {
            f32x16 s = MFMA(frag(k0), frag(qf[t][0]), zero);
            s = MFMA(frag(k1), frag(qf[t][1]), s);
#pragma unroll
            for (int i = 0; i < 16; ++i) mx[t] = fmaxf(mx[t], s[i]);
          }
        }
        const u32 ma = L_C + 49152;
#pragma unroll
        for (int t = 0; t < 4; ++t) { mx[t] = fmaxf(mx[t], swap32(mx[t], lv >= 512)); *(LAS float*)(size_t)(ma + (w * 4 + ((w + t) & 3)) * 256 + (lv >> 2)) = mx[t]; }
        arrive(C_MAX, lv);
        await(C_MAX, 4);
#pragma unroll
        for (int t = 0; t < 4; ++t) {
          float m = -3.0e38f;
#pragma unroll
          for (int ww = 0; ww < 4; ++ww) m = fmaxf(m, *(LAS float*)(size_t)(ma + (ww * 4 + ((w + t) & 3)) * 256 + (lv >> 2)));
          set_pad(qf[t][1], m, lv >= 512);
        }
      }
    }
    {
    int lane;
    asm volatile("v_mbcnt_lo_u32_b32 %0, -1, 0\n\tv_mbcnt_hi_u32_b32 %0, -1, %0" : "=v"(lane));
    const int r = lane & 31, h = lane >> 5, g1 = (lane >> 4) & 1, q4 = (lane >> 2) & 3, p4 = lane & 3;
    const u32 vo = lane * 16;
    {
      f32x16 own = o[0];
#pragma unroll
      for (int pp = 1; pp < 4; ++pp) {
        const u32 a = L_C + ((w + pp) & 3) * 12288 + (3 - pp) * 4096 + vo;
#pragma unroll
        for (int g = 0; g < 4; ++g) add4(own, g, ldsr128(a + g * 1024));
      }
      const float other = swap32(own[12], h);
      const float inv = __builtin_amdgcn_rcpf(h ? own[12] : other);
      u32x4 f0 = {cvtpk(own[0] * inv, own[1] * inv), cvtpk(own[2] * inv, own[3] * inv), cvtpk(own[4] * inv, own[5] * inv), cvtpk(own[6] * inv, own[7] * inv)};
      u32x4 f1 = {cvtpk(own[8] * inv, own[9] * inv), cvtpk(own[10] * inv, own[11] * inv), cvtpk(own[12] * inv, own[13] * inv), cvtpk(own[14] * inv, own[15] * inv)};
      if (h) { f1.z = 0; f1.w = 0; }
      const u32 so = WS_OF + ((tb * 2 + (w & 1)) * 16 + 2 * sl + (w >> 1)) * 2048 + vo;
      __builtin_amdgcn_raw_buffer_store_b128(f0, rs_ws, so, 0, SC1);
      __builtin_amdgcn_raw_buffer_store_b128(f1, rs_ws, so + 1024, 0, SC1);
    }
    {
      const int tm = w & 1, kh = w >> 1;
      VMCNT(0);
      if (lane == 0) gstore_flag(ws, WS_OFLAG + (tb * 32 + sl * 4 + w) * 128, tag);
      if (sl == 7) return;
      await(C_WO, 4);
      u32x4 wf[16][2];
      const u32 wa = 2 * kh * L_PRIV + L_WO_OFF + (4 * h + q4) * 128 + g1 * 32 + 8 * p4;
      const u32 wa0 = wa + 64 * kh, wa1 = wa + 64 * (1 - kh);
      const u32 hz = h ? 0 : 1024;
#pragma unroll
      for (int i = 0; i < 16; ++i) {
        const u32 a = (i >> 3) * L_PRIV + (28 * ((i >> 1) & 3) + 16 * (i & 1)) * 128;
        if ((i & 7) == 7) { wf[i][0] = trfrag(wa0 + a, wa0 + hz + a); wf[i][1] = trfrag(wa1 + a, wa1 + hz + a); }
        else { wf[i][0] = trfrag(wa0 + a, wa0 + a + 1024); wf[i][1] = trfrag(wa1 + a, wa1 + a + 1024); }
      }
#pragma unroll
      for (int i = 0; i < 16; ++i) PIN2(wf[i][0], wf[i][1]);
      if (w == 0) { poll_words(ws, WS_OFLAG + tb * 4096, 32, tag, lane); arrive(C_OF, lane); }
      else await<0>(C_OF, 1);
      u32x4 of[16];
      const u32 so = WS_OF + ((tb * 2 + tm) * 16 + 8 * kh) * 2048;
#pragma unroll
      for (int i = 0; i < 16; ++i) of[i] = __builtin_amdgcn_raw_buffer_load_b128(rs_ws, vo + (i & 3) * 1024, so + (i >> 2) * 4096, SC1);
      SBAR();
      f32x16 a0 = {}, a1 = {};
#pragma unroll
      for (int i = 0; i < 16; ++i) {
        a0 = MFMA(frag(of[i]), frag(wf[i][0]), a0);
        a1 = MFMA(frag(of[i]), frag(wf[i][1]), a1);
      }
      MPIN2(a0, a1);
      const f32x16 snd = a1;
      f32x16 own = a0;
      const u32 aw = L_C + w * 4096 + vo, ar = L_C + (w ^ 2) * 4096 + vo;
#pragma unroll
      for (int g = 0; g < 4; ++g) ldsw128(aw + g * 1024, f4(snd, g));
      arrive(C_P, lane);
      await<0>(C_P, 4);
#pragma unroll
      for (int g = 0; g < 4; ++g) add4(own, g, ldsr128(ar + g * 1024));
      {
        const u32 ov = ((tb * 64 + 32 * tm + 4 * h) * 896 + 448 + 64 * sl + 32 * kh + r) * 4;
#pragma unroll
        for (int i = 0; i < 16; ++i) __builtin_amdgcn_raw_buffer_store_b32(__float_as_uint(rbf(own[i])), rs_out, ov, ((i & 3) + 8 * (i >> 2)) * 3584, NT);
      }
    }
    }
  } else {
    if (sl == 7) __builtin_amdgcn_s_setprio(2);
    const int q = wv - 4;
    const u32 PA = q * L_PRIV;
    const int part = q >> 1, half = q & 1;
    if (sl == 7 && lane < 56) {
      const char* src = (const char*)((q >> 1) ? P.wv : P.wk) + (q & 1) * 14 * 896;
#pragma unroll
      for (int j = 0; j < 14; ++j) {
        int i = j + 4 * tl; i = i >= 14 ? i - 14 : i;
        glds16(src + i * 896, vo, L_C + (q * 14 + i) * 896);
      }
    }
    asm volatile("s_waitcnt vmcnt(0) lgkmcnt(0)\n\ts_barrier" ::: "memory");
    const u32 tag = ldsu(L_MISC + 4 * C_EPOCH) + 1u;
    if (sl == 7) {
      const int kind = q >> 1, ktm = q & 1;
      const u32 wa = L_C + kind * 25088 + (8 * h + q4) * 56 + g1 * 32 + 8 * p4;
      f32x16 acc;
      if (kind == 0) acc = proj_tile<true, 56, false>(32 * ktm + r, h, wa, lane);
      else acc = proj_tile<false, 56, false>(32 * ktm + r, h, wa, lane);
      u32x4 f0 = {cvtpk(acc[0], acc[1]), cvtpk(acc[2], acc[3]), cvtpk(acc[4], acc[5]), cvtpk(acc[6], acc[7])};
      u32x4 f1 = {cvtpk(acc[8], acc[9]), cvtpk(acc[10], acc[11]), cvtpk(acc[12], acc[13]), cvtpk(acc[14], acc[15])};
      if (kind == 0) {
        if (h) { f1.z = 0x3F803F80u; f1.w = 0x00003F80u; }
      } else if (r >= 28) {
        const u32 c = r == 28 ? 0x3F803F80u : 0u;
        f0 = (u32x4){c, c, c, c}; f1 = f0;
      }
      const u32 so = WS_KVF + (tb * 2 + ktm) * 4096 + kind * 2048 + vo;
      __builtin_amdgcn_raw_buffer_store_b128(f0, rs_ws, so, 0, SC1);
      __builtin_amdgcn_raw_buffer_store_b128(f1, rs_ws, so + 1024, 0, SC1);
      SBAR();
      if (kind == 0) {
        float n2 = 0.f;
#pragma unroll
        for (int i = 0; i < 16; ++i) { if (i < 12) n2 = fmaf(acc[i], acc[i], n2); else n2 = h ? n2 : fmaf(acc[i], acc[i], n2); }
        n2 += swap32(n2, h);
        n2 = wave_max_nonneg(n2);
        if (lane == 0) asm volatile("ds_max_u32 %0, %1" :: "v"(L_MISC + 4 * C_KMAX), "v"(n2) : "memory");
      }
      VMCNT(0);
      u32 old = 0;
      if (lane == 0) asm volatile("s_waitcnt lgkmcnt(0)\n\tds_add_rtn_u32 %0, %1, %2\n\ts_waitcnt lgkmcnt(0)" : "=&v"(old) : "v"(L_MISC + 4 * C_KVP), "v"(1u) : "memory");
      if (__builtin_amdgcn_readfirstlane(old) == 3) {
        const u32 km = ldsu(L_MISC + 4 * C_KMAX);
        if (lane < 8) __hip_atomic_store((GAS u64*)(ws + WS_KVFLAG + lane * 4096 + tb * 128), (u64)tag | ((u64)km << 32), __ATOMIC_RELAXED, __HIP_MEMORY_SCOPE_AGENT);
      }
      arrive(C_KVD, lane);
      await(C_KVD, 4);
    }

    u32 cva;
    {
      const int hi = lane >= 28, cs = lane - 28 * hi;
      cva = hi * 896 + (cs << 4);
    }
    auto stage_C4 = [&](int qq, int g) {
      const char* csrc = (const char*)((qq >> 1) ? P.Cim : P.Cre) + (size_t)sl * 56 * 896 + (qq & 1) * 448;
      int p0 = 4 * g + 8 * tl; p0 = p0 >= 28 ? p0 - 28 : p0;
      const u32 va = cva ^ ((p0 & 4) << 3), vb = va ^ 16;
      if (lane < 56) {
#pragma unroll
        for (int j = 0; j < 4; ++j) { glds16(csrc + (p0 + j) * 1792, j < 2 ? va : vb, qq * L_PRIV + (p0 + j) * 896); VMTHR(); }
      }
    };
    const int tm = (q ^ sl) & 1, tn = q >> 1;
    const bool valid = 32 * tn + r < 56;
    const int dcol = 56 * sl + (valid ? 32 * tn + r : 55), t0 = tb * 64 + 32 * tm + 4 * h;
    u32x4 cf[2][14];
    auto read_C = [&]() {
#pragma unroll
      for (int t = 0; t < 2; ++t) {
        int row = 32 * (t ^ part) + r; row = row > 55 ? 55 : row;
        const int x = (row >> 2) & 3;
        const u32 a0 = PA + row * 448 + ((h ^ x) << 4), a1 = PA + row * 448 + (((2 + h) ^ x) << 4);
#pragma unroll
        for (int j = 0; j < 14; ++j) cf[t][j] = ldsr128(((j & 1) ? a1 : a0) + 64 * (j >> 1));
      }
#pragma unroll
      for (int j = 0; j < 14; ++j) PIN2(cf[0][j], cf[1][j]);
      LGKM0();
    };
    if (sl == 7) {
      arrive(C_HD, lane);
      await(C_KVL, 4);
      {
        const char* cp = (const char*)(part ? P.Cim : P.Cre) + (tl * 2 + half) * 43904;
#pragma unroll 1
        for (int i = 0; i < 43; ++i) { glds16(cp + (i < 42 ? i * 1024 : 42880), vo, PA + (i & 7) * 1024); VMTHR(); }
        const char* wp = (const char*)P.wo + (tl * 4 + q) * 25088;
#pragma unroll 1
        for (int i = 0; i < 25; ++i) { glds16(wp + (i < 24 ? i * 1024 : 24064), vo, PA + (i & 7) * 1024); VMTHR(); }
        VMCNT(0);
      }
#pragma unroll 1
      for (int g = 0; g < 7; ++g) stage_C4(q, g);
      VMCNT(0);
      read_C();
    } else {
      const int tb2 = tb >> 1, cg = (tb & 1) * 7 + sl, ch = 32 * cg + r;
      const u32 vo1 = (lane ^ 1) * 16;
      float lr, li, cr, ci;
      if (q == 0) {
        asm volatile("s_mov_b32 m0, %2\n\ts_nop 0\n\tglobal_load_lds_ushort %0, %1" :: "v"(ch * 2), "s"(P.lre), "s"(L_AGG) : "memory");
        asm volatile("s_mov_b32 m0, %2\n\ts_nop 0\n\tglobal_load_lds_ushort %0, %1" :: "v"(ch * 2), "s"(P.lim), "s"(L_AGG + 256) : "memory");
        asm volatile("s_mov_b32 m0, %2\n\ts_nop 0\n\tglobal_load_lds_ushort %0, %1" :: "v"(ch * 2), "s"(P.ldt), "s"(L_AGG + 512) : "memory");
      }
      if (lane < 54) {
        const char* bsrc = (const char*)(part ? P.Bim : P.Bre) + (size_t)(cg * 32 + half * 16) * 896;
        const u32 va = (tb2 & 1) ? vo1 : vo, vb = (tb2 & 1) ? vo : vo1;
#pragma unroll
        for (int j = 0; j < 16; ++j) {
          const int i = j ^ (8 * (tb2 & 1));
          glds16(bsrc + i * 896, j < 8 ? va : vb, L_C + (q * 16 + i) * 864);
        }
      }
      const char* xsrc = (const char*)P.Ax + (size_t)(tb2 * 128 + q * 32) * 896;
      const u32 xv0 = (lane >> 3) * 896 + (((lane & 7) ^ (lane >> 4)) << 4), xv1 = xv0 ^ 64;
      const int rho = 16 * ((r >> 2) & 1) + (r & 3) + 4 * (r >> 3), sw = (rho >> 1) & 7;
      u32 xa[4], xb[4];
#pragma unroll
      for (int bb = 0; bb < 4; ++bb) { xa[bb] = q * 4096 + rho * 128 + (((2 * bb + h) ^ sw) << 4); xb[bb] = xa[bb] + 65536; }
      const int xrot = cg % 6;
      u32 ba[7];
#pragma unroll
      for (int c = 0; c < 7; ++c) {
        int kc = c + xrot; kc = c == 6 ? 6 : kc >= 6 ? kc - 6 : kc;
        ba[c] = L_C + r * 864 + ((h ^ ((r >> 3) & 1)) << 4) + 128 * kc;
        PIN(ba[c]);
      }
      PIN4(xa[0], xa[1], xa[2], xa[3]); PIN4(xb[0], xb[1], xb[2], xb[3]);
      constexpr u32 XBLK[7] = {0, 65536, 16384, 32768, 49152, 81920, 0};
      auto xdma = [&](int c) {
#pragma unroll
        for (int j = 0; j < 4; ++j) {
          int kc = c + xrot; kc = c == 6 ? 6 : kc >= 6 ? kc - 6 : kc;
          glds16(xsrc + kc * 128 + j * 7168, (j & 1) ? xv1 : xv0, XBLK[c] + q * 4096 + j * 1024);
        }
      };
      await(C_WQ2, 4);
      xdma(0); xdma(1);
      VMCNT(8);
      arrive(C_BFULL, lane);
      await(C_BFULL, 4);
      __builtin_amdgcn_s_setprio(2);
      {
        const u32 vlre = *(LAS u32*)(size_t)(L_AGG + 4 * lane), vlim = *(LAS u32*)(size_t)(L_AGG + 256 + 4 * lane), vldt = *(LAS u32*)(size_t)(L_AGG + 512 + 4 * lane);
        const float dt = rbf(expf(__uint_as_float(vldt << 16))), a = __uint_as_float(vlre << 16), bb = __uint_as_float(vlim << 16);
        const float er = expf(dt * a);
        float sn, cs;
        sincosf(dt * bb, &sn, &cs);
        lr = er * cs; PIN(lr); li = er * sn;
        const float nr = lr - 1.f, ni = li, den = 2.0f / (a * a + bb * bb);
        cr = (nr * a + ni * bb) * den; PIN(cr); ci = (ni * a - nr * bb) * den;
      }
      float l16r = lr, l16i = li;
#pragma unroll
      for (int i = 0; i < 4; ++i) cmul(l16r, l16i, l16r, l16i, l16r, l16i);
      float l32r, l32i, l128r, l128i;
      cmul(l16r, l16i, l16r, l16i, l32r, l32i);
      cmul(l32r, l32i, l32r, l32i, l128r, l128i);
      cmul(l128r, l128i, l128r, l128i, l128r, l128i);
      float lhr = 1.f, lhi = 0.f;
#pragma unroll 1
      for (int j = 8; j < tb2; ++j) cmul(lhr, lhi, l128r, l128i, lhr, lhi);
      PIN4(lr, li, cr, ci); PIN4(l16r, l16i, l32r, l32i); PIN4(l128r, l128i, lhr, lhi);
      arrive(C_CONST, lane);
      await(C_QDONE, 4);
      glds16((const char*)P.Bre + (size_t)cg * 32 * 896 + 864, r * 896 + 16 * h, 98304 + q * 2048);
      glds16((const char*)P.Bim + (size_t)cg * 32 * 896 + 864, r * 896 + 16 * h, 98304 + q * 2048 + 1024);
      xdma(2);
      const u32 bl = 98304 + q * 2048 + vo;
      f32x16 ur = {}, ui = {};
      PIN2(ur, ui);
      __builtin_amdgcn_s_setprio(0);
      {
        u32x4 xf[28], br[28], bi[28];
#pragma unroll
        for (int ks = 0; ks < 28 + 4; ++ks) {
          if (ks >= 4) {
            const int k = ks - 4;
            ur = MFMA(frag(xf[k]), frag(br[k]), ur);
            ui = MFMA(frag(xf[k]), frag(bi[k]), ui);
            SBAR();
          }
          if (ks < 28) {
            if ((ks & 3) == 0) {
              const int c = ks >> 2;
              if (c == 0) VMCNT(10); else if (c == 1) VMCNT(6); else if (c == 2 || c == 6) VMCNT(0); else VMCNT(4);
              if (c == 2) {
                asm volatile("s_nop 7\n\ts_nop 7" ::: "memory");
                __builtin_amdgcn_s_setprio(2);
                await(C_KVL, 4);
                xdma(3);
                __builtin_amdgcn_s_setprio(0);
              }
              if (c >= 2 && c + 2 <= 6) xdma(c + 2);
            }
            const int c = ks >> 2;
            xf[ks] = XBLK[c] >= 65536 ? ldsr128(xb[ks & 3] + (XBLK[c] - 65536)) : ldsr128(xa[ks & 3] + XBLK[c]);
            br[ks] = ks < 27 ? ldsr128(ba[c] + 32 * (ks & 3)) : ldsr128(bl);
            bi[ks] = ks < 27 ? ldsr128(ba[c] + 27648 + 32 * (ks & 3)) : ldsr128(bl + 1024);
            SBAR();
          }
        }
      }
      MPIN2(ur, ui);
      __builtin_amdgcn_s_setprio(2);
      float ar = 0.f, ai = 0.f;
#pragma unroll
      for (int i = 0; i < 16; ++i) {
        float tr, ti;
        cmul(cr, ci, ur[i], ui[i], tr, ti);
        ur[i] = tr; ui[i] = ti;
        cstep(lr, li, ar, ai, tr, ti);
      }
      const float or_ = swap32(ar, h), oi_ = swap32(ai, h);
      const float lor = h ? or_ : ar, loi = h ? oi_ : ai;
      float a32r = lor, a32i = loi;
      cstep(l16r, l16i, a32r, a32i, h ? ar : or_, h ? ai : oi_);
      await(C_CONST, 4);
      if (q < 3 && h == 0) *(LAS f32x2*)(size_t)(L_AGG + q * 256 + r * 8) = (f32x2){a32r, a32i};
      arrive(C_AGG, lane);
      await(C_AGG, 4);
      f32x2 wagg[3];
#pragma unroll
      for (int j = 0; j < 3; ++j) wagg[j] = *(LAS f32x2*)(size_t)(L_AGG + j * 256 + r * 8);
      const u32 aggb = WS_AGG + cg * 8192;
      if (q == 3 && tb2 < 15) {
        float tr = wagg[0].x, ti = wagg[0].y;
        cstep(l32r, l32i, tr, ti, wagg[1].x, wagg[1].y);
        cstep(l32r, l32i, tr, ti, wagg[2].x, wagg[2].y);
        cstep(l32r, l32i, tr, ti, a32r, a32i);
        __hip_atomic_store((GAS u64*)(ws + aggb + tb2 * 512 + r * 16 + h * 8), (u64)__float_as_uint(h ? ti : tr) | ((u64)tag << 32), __ATOMIC_RELAXED, __HIP_MEMORY_SCOPE_AGENT);
      }
      float hr = 0.f, hi = 0.f;
      int cnext = 2;
      if (q < 3) {
#pragma unroll 1
        for (int g = q; g < 7; g += 3) stage_C4(3, g);
        stage_C4(q, 0); stage_C4(q, 1);
        VMCNT(8);
        arrive(C_C3, lane);
#pragma unroll 1
        for (; cnext < 7 && ldsu(L_MISC + 4 * C_CARRY) == 0; ++cnext) stage_C4(q, cnext);
        await(C_CARRY, 1);
        const f32x2 cv = *(LAS f32x2*)(size_t)(L_CARRY + r * 8);
        hr = cv.x; hi = cv.y;
      } else {
        if (tb2 > 0) {
          u32x4 g[8];
          u32 gv[8];
#pragma unroll
          for (int j = 0; j < 8; ++j) gv[j] = r * 16 + ((8 * h + j < tb2) ? (8 * h + j) * 512 : 0);
#pragma unroll 1
          for (u32 spin = 0; spin < (1u << 17); ++spin) {
            asm volatile("" ::: "memory");
            u32 bad = 0;
#pragma unroll
            for (int j = 0; j < 8; ++j) g[j] = __builtin_amdgcn_raw_buffer_load_b128(rs_ws, gv[j], aggb, SC1);
#pragma unroll
            for (int j = 0; j < 8; ++j) bad |= (g[j].y ^ tag) | (g[j].w ^ tag);
            if (__all(bad == 0)) break;
            __builtin_amdgcn_s_sleep(4);
          }
          asm volatile("" ::: "memory");
          const int nb = tb2 - 8 * h;
#pragma unroll
          for (int j = 0; j < 8; ++j) {
            if (j < tb2) {
              float nr = hr, ni = hi;
              cstep(l128r, l128i, nr, ni, __uint_as_float(g[j].x), __uint_as_float(g[j].z));
              if (j < nb) { hr = nr; hi = ni; }
            }
          }
          float lo_r = swap32(hr, h), lo_i = swap32(hi, h), up_r = lo_r, up_i = lo_i;
          if (h) { up_r = hr; up_i = hi; } else { lo_r = hr; lo_i = hi; }
          cstep(lhr, lhi, lo_r, lo_i, up_r, up_i);
          hr = lo_r; hi = lo_i;
        }
        if (h == 0) *(LAS f32x2*)(size_t)(L_CARRY + r * 8) = (f32x2){hr, hi};
        arrive(C_CARRY, lane);
      }
#pragma unroll
      for (int j = 0; j < 3; ++j)
        if (j < q) cstep(l32r, l32i, hr, hi, wagg[j].x, wagg[j].y);
      if (h) cstep(l16r, l16i, hr, hi, lor, loi);
      u32 pr[8], pi[8];
#pragma unroll
      for (int i = 0; i < 16; i += 2) {
        cstep(lr, li, hr, hi, ur[i], ui[i]);
        const float r0 = hr, i0 = hi;
        cstep(lr, li, hr, hi, ur[i + 1], ui[i + 1]);
        pr[i >> 1] = cvtpk(r0, hr); pi[i >> 1] = cvtpk(-i0, -hi);
      }
      const u32 ta = L_C + q * 5120 + r * 80 + 32 * h;
      ldsw128(ta, (u32x4){pr[0], pr[1], pr[2], pr[3]}); ldsw128(ta + 16, (u32x4){pr[4], pr[5], pr[6], pr[7]});
      ldsw128(ta + 2560, (u32x4){pi[0], pi[1], pi[2], pi[3]}); ldsw128(ta + 2560 + 16, (u32x4){pi[4], pi[5], pi[6], pi[7]});
      LGKM0();
      const u32 tr = L_C + q * 5120 + (8 * h + q4) * 80 + g1 * 32 + 8 * p4;
      const int tbh = 2 * tb2 + (q >> 1);
      const u32 so = WS_HF + ((tbh * 2 + (q & 1)) * 56 + 2 * cg) * 1024 + vo;
#pragma unroll
      for (int pt = 0; pt < 2; ++pt)
#pragma unroll
        for (int s = 0; s < 2; ++s) {
          const u32 a = tr + pt * 2560 + s * 1280;
          __builtin_amdgcn_raw_buffer_store_b128(trfrag(a, a + 320), rs_ws, so + pt * 28 * 1024 + s * 1024, 0, SC1);
        }
      arrive(C_HD, lane);
      VMCNT(0);
      if (lane == 0) gstore_flag(ws, WS_HFLAG + (tbh * 32 + cg * 2 + (q & 1)) * 128, tag);
      if (q < 3) {
#pragma unroll 1
        for (; cnext < 7; ++cnext) stage_C4(q, cnext);
        VMCNT(0);
      } else {
        await(C_C3, 3);
      }
      read_C();
      const char* src = (const char*)P.wo + sl * 128 + q * 112 * 896;
      const u32 wv8 = (lane >> 3) * 896 + (lane & 7) * 16;
#pragma unroll
      for (int j = 0; j < 14; ++j) {
        int i = j + 4 * tl; i = i >= 14 ? i - 14 : i;
        glds16(src + i * 7168, wv8, PA + L_WO_OFF + 1024 * i); VMTHR();
      }
      VMCNT(0);
      arrive(C_WO, lane);
    }
    if (q == 0) { poll_words(ws, WS_HFLAG + tb * 4096, 28, tag, lane); arrive(C_HF, lane); }
    else await(C_HF, 1);
    {
      f32x16 y[4];
      const u32 so = WS_HF + ((tb * 2 + tm) * 56 + part * 28 + half * 14) * 1024, so1 = WS_HF + ((tb * 2 + (tm ^ 1)) * 56 + part * 28 + half * 14) * 1024;
      u32x4 hf[28];
#define LDH(j) __builtin_amdgcn_raw_buffer_load_b128(rs_ws, vo + (((j) % 14) & 3) * 1024, ((j) < 14 ? so : so1) + (((j) % 14) >> 2) * 4096, SC1)
#pragma unroll
      for (int j = 0; j < HWIN; ++j) hf[j] = LDH(j);
      SBAR();
      const f32x16 zero = {};
      __builtin_amdgcn_s_setprio(0);
#pragma unroll
      for (int j = 0; j < 28; ++j) {
        const int t = j / 14, k = j % 14;
        y[t] = MFMA(frag(hf[j]), frag(cf[0][k]), k == 0 ? zero : y[t]);
        y[2 + t] = MFMA(frag(hf[j]), frag(cf[1][k]), k == 0 ? zero : y[2 + t]);
        SBAR();
        if (j + HWIN < 28) { hf[j + HWIN] = LDH(j + HWIN); SBAR(); }
      }
      u16 xs[16];
      {
        const auto rs_ax = __builtin_amdgcn_make_buffer_rsrc((void*)P.Ax, 0, 2048 * 896, 0x00020000);
        const u32 xv = (t0 * 448 + dcol) * 2;
#pragma unroll
        for (int i = 0; i < 16; ++i) xs[i] = __builtin_amdgcn_raw_buffer_load_b16(rs_ax, xv + (i & 3) * 896, (i >> 2) * 7168, 0);
      }
      const u16 dsk16 = ((const GAS u16*)P.Dsk)[dcol];
      MPIN4(y[0], y[1], y[2], y[3]);
      __builtin_amdgcn_s_setprio(2);
#pragma unroll
      for (int t = 1; t < 4; ++t) {
#pragma unroll
        for (int g = 0; g < 4; ++g) ldsw128(PA + (t - 1) * 4096 + g * 1024 + vo, f4(y[t], g));
      }
      arrive(C_Y, lane);
      await(C_Y, 4);
      f32x16 own = y[0];
#pragma unroll
      for (int t = 1; t < 4; ++t) {
#pragma unroll
        for (int g = 0; g < 4; ++g) add4(own, g, ldsr128((q ^ t) * L_PRIV + (t - 1) * 4096 + g * 1024 + vo));
      }
      if (valid) {
        const u32 ov = (t0 * 896 + dcol) * 4;
#pragma unroll
        for (int i = 0; i < 16; ++i) {
          float sk = bf2f(xs[i]) * bf2f(dsk16);
          PIN(sk);
          __builtin_amdgcn_raw_buffer_store_b32(__float_as_uint(own[i] + rbf(sk)), rs_out, ov, ((i & 3) + 8 * (i >> 2)) * 3584, NT);
        }
      }
    }
    if (tb == 31 && sl == 0 && q == 0) {
      await<0>(C_KV, 1);
      if (lane == 0) gstore_flag(ws, WS_EPOCH, tag);
    }
  }
}

extern "C" void kernel_launch(void* const* d_in, const int* in_sizes, int n_in, void* d_out, int out_size, void* d_ws, size_t ws_size,
                              hipStream_t stream) {
  Params P;
  P.Ax = (const u16*)d_in[0]; P.lre = (const u16*)d_in[1]; P.lim = (const u16*)d_in[2]; P.Bre = (const u16*)d_in[3];
  P.Bim = (const u16*)d_in[4]; P.Cre = (const u16*)d_in[5]; P.Cim = (const u16*)d_in[6]; P.ldt = (const u16*)d_in[7];
  P.Dsk = (const u16*)d_in[8]; P.Bx = (const u16*)d_in[9]; P.wq = (const u16*)d_in[10]; P.wk = (const u16*)d_in[11];
  P.wv = (const u16*)d_in[12]; P.wo = (const u16*)d_in[13];
  P.out = (float*)d_out; P.ws = (char*)d_ws;
  (void)hipFuncSetAttribute((const void*)fused_kernel, hipFuncAttributeMaxDynamicSharedMemorySize, LDS_BYTES);
  fused_kernel<<<256, 512, LDS_BYTES, stream>>>(P);
}
```
